```python
import torch

def _softmax(x, axis=-1):
    return torch.softmax(x, dim=axis)
def _gelu(x):
    return torch.nn.functional.gelu(x, approximate="tanh")
def _silu(x):
    return torch.nn.functional.silu(x)
def _sigmoid(x):
    return torch.sigmoid(x)
def _layernorm(x, axis=-1, eps=1e-5):
    m = x.mean(dim=axis, keepdim=True)
    v = x.var(dim=axis, keepdim=True, unbiased=False)
    return (x - m) / torch.sqrt(v + eps)
def _scatter_add(tgt, idx, src, axis=0):
    return tgt.clone().index_add_(axis, idx, src)
def _segment_sum(src, seg, n):
    out = torch.zeros((n,) + src.shape[1:], dtype=src.dtype)
    return out.index_add_(0, seg, src)
def _pad(x, pw):
    flat = tuple(v for pair in reversed(pw) for v in pair)
    return torch.nn.functional.pad(x, flat)

kernel_name = 'recurrent_scan__mha_block_cached__geglu_ffn__decode'

def setup_inputs():

    _w = (lambda a: (lambda t: t.to(torch.bfloat16) if t.is_floating_point() else t)(torch.as_tensor(a)))
    def _A_setup():
        B, Sq, D = 2, 1, 512
        H, Sk, Dh = 32, 8192, 128
        return {
            "x": _w((lambda *s: torch.randn(*s, dtype=torch.bfloat16) * 0.05)(B, Sq, D)),
            "k_cache": _w((lambda *s: torch.randn(*s, dtype=torch.bfloat16) * 1.6)(B, H, Sk, Dh)),
            "v_cache": _w((lambda *s: torch.randn(*s, dtype=torch.bfloat16) * 0.2)(B, H, Sk, Dh)),
            "wq": _w((lambda *s: torch.randn(*s, dtype=torch.bfloat16) * 1.6)(D, H * Dh) * (D ** -0.5)),
            "wo": _w((lambda *s: torch.randn(*s, dtype=torch.bfloat16) * 0.2)(H * Dh, D) * ((H * Dh) ** -0.5)),
        }
    _A_in = _A_setup()

    def _B_setup():
        B, S, D, F = 2, 1, 512, 1376
        return {
            "x": _w((lambda *s: torch.randn(*s, dtype=torch.bfloat16) * 0.05)(B, S, D)),
            "w_gate": _w((lambda *s: torch.randn(*s, dtype=torch.bfloat16) * 0.1)(D, F)),
            "w_up": _w((lambda *s: torch.randn(*s, dtype=torch.bfloat16) * 0.1)(D, F)),
            "w_down": _w((lambda *s: torch.randn(*s, dtype=torch.bfloat16) * 0.1)(F, D)),
        }
    _B_in = _B_setup()

    return {
        "A_x": _A_in["x"],
        "A_k_cache": _A_in["k_cache"],
        "A_v_cache": _A_in["v_cache"],
        "A_wq": _A_in["wq"],
        "A_wo": _A_in["wo"],
        "B_x": _B_in["x"],
        "B_w_gate": _B_in["w_gate"],
        "B_w_up": _B_in["w_up"],
        "B_w_down": _B_in["w_down"],
    }

def reference(A_x, A_k_cache, A_v_cache, A_wq, A_wo, B_x, B_w_gate, B_w_up, B_w_down):
    def _A_ref(x, k_cache, v_cache, wq, wo):
        B, Sq, D = x.shape
        H, Dh = 32, 128
        q = (lambda x, *d: x.permute(*d))((x @ wq).reshape(B, Sq, H, Dh), 0, 2, 1, 3)
        scores = torch.einsum("bhqd,bhkd->bhqk", q, k_cache) * (Dh ** -0.5)
        p = _softmax(scores, axis=-1)
        o = torch.einsum("bhqk,bhkd->bhqd", p, v_cache)
        return (lambda x, *d: x.permute(*d))(o, 0, 2, 1, 3).reshape(B, Sq, H * Dh) @ wo

    def _B_ref(x, w_gate, w_up, w_down):
        return (_gelu(x @ w_gate) * (x @ w_up)) @ w_down

    x = A_x
    xn = _layernorm(x, axis=-1)
    s = _A_ref(xn, A_k_cache, A_v_cache, A_wq, A_wo)
    g = _silu(_B_ref(xn, B_w_gate, B_w_up, B_w_down))
    return x + s * g
```

```cpp
#include <hip/hip_runtime.h>
#include <hip/hip_bf16.h>
#include <stdint.h>

typedef unsigned v4u __attribute__((ext_vector_type(4)));
typedef float f32x4 __attribute__((ext_vector_type(4)));
typedef __bf16 bf16x2 __attribute__((ext_vector_type(2)));
#define GAS __attribute__((address_space(1)))

constexpr int D = 512, H = 32, SK = 8192, DH = 128, F = 1376;
constexpr int NSPLIT = 4, KEYS_PER_WG = SK / NSPLIT;
constexpr int NWAVE = 8, NTHR = NWAVE * 64;
constexpr int NFFN = F / 8;
#define U_ 4
#define NG_ 4
constexpr int U = U_;
constexpr int NG = NG_;
constexpr int GROUPS = KEYS_PER_WG / NWAVE / (4 * U);
static_assert(GROUPS % NG == 0, "");

constexpr size_t WS_Y = 0;
constexpr size_t WS_ML = WS_Y + 256 * 512;
constexpr size_t WS_DP = WS_ML + 512;

__device__ __forceinline__ float dot2(unsigned k, unsigned q, float acc) {
  return __builtin_amdgcn_fdot2_f32_bf16(__builtin_bit_cast(bf16x2, k), __builtin_bit_cast(bf16x2, q), acc, false);
}
template <int CTRL> __device__ __forceinline__ float dpp(float x) {
  return __builtin_bit_cast(float, __builtin_amdgcn_mov_dpp(__builtin_bit_cast(int, x), CTRL, 0xf, 0xf, true));
}
__device__ __forceinline__ float row16_sum(float x) {
  x += dpp<0x128>(x);
  x += dpp<0x124>(x);
  x += dpp<0x122>(x);
  x += dpp<0x121>(x);
  return x;
}
__device__ __forceinline__ float xrow_sum(float x) {
  auto s = __builtin_amdgcn_permlane16_swap(__float_as_uint(x), __float_as_uint(x), false, false);
  x = __uint_as_float(s[0]) + __uint_as_float(s[1]);
  auto t = __builtin_amdgcn_permlane32_swap(__float_as_uint(x), __float_as_uint(x), false, false);
  return __uint_as_float(t[0]) + __uint_as_float(t[1]);
}
__device__ __forceinline__ float xrow_max(float x) {
  auto s = __builtin_amdgcn_permlane16_swap(__float_as_uint(x), __float_as_uint(x), false, false);
  x = fmaxf(__uint_as_float(s[0]), __uint_as_float(s[1]));
  auto t = __builtin_amdgcn_permlane32_swap(__float_as_uint(x), __float_as_uint(x), false, false);
  return fmaxf(__uint_as_float(t[0]), __uint_as_float(t[1]));
}
__device__ __forceinline__ float wave_sum(float x) { return xrow_sum(row16_sum(x)); }
__device__ __forceinline__ float lo(unsigned u) { return __uint_as_float(u << 16); }
__device__ __forceinline__ float hi(unsigned u) { return __uint_as_float(u & 0xffff0000u); }
__device__ __forceinline__ unsigned pk_bf16(float a, float b) {
  bf16x2 r = {(__bf16)a, (__bf16)b};
  return __builtin_bit_cast(unsigned, r);
}
__device__ __forceinline__ v4u ldnt(const GAS v4u* p) { return __builtin_nontemporal_load(p); }
__device__ __forceinline__ float gelu_tanh(float x) {
  const float u = 0.7978845608028654f * (x + 0.044715f * x * x * x);
  const float e = __expf(2.f * u);
  const float t = 1.f - 2.f / (e + 1.f);
  return 0.5f * x * (1.f + t);
}

struct Args {
  const unsigned short* x; const unsigned short* k; const unsigned short* v; const unsigned short* wq; const unsigned short* wo;
  const unsigned short* wg; const unsigned short* wu; const unsigned short* wd; unsigned short* out; float* ws;
};

__device__ __forceinline__ void ln_stats(v4u xr, float& mean, float& rstd) {
  float f[8];
#pragma unroll
  for (int i = 0; i < 4; ++i) { f[2 * i] = lo(xr[i]); f[2 * i + 1] = hi(xr[i]); }
  float s = 0.f;
#pragma unroll
  for (int i = 0; i < 8; ++i) s += f[i];
  mean = wave_sum(s) * (1.f / D);
  float q = 0.f;
#pragma unroll
  for (int i = 0; i < 8; ++i) { const float d = f[i] - mean; q = fmaf(d, d, q); }
  const float var = wave_sum(q) * (1.f / D);
  rstd = 1.f / sqrtf(var + 1e-5f);
}

__global__ void __launch_bounds__(NTHR, 2) k_attn(Args a) {
  __shared__ __attribute__((aligned(16))) float lds[8 * 1024 + 8 * 132 + 8 * 32 + 8 * 128];
  float* ldsY = lds;
  float* ldsM = lds + 8 * 1024;
  float* ldsF = ldsM + 8 * 132;
  float* ldsQ = ldsF + 8 * 32;
  const int bid = blockIdx.x;
  const int xcd = bid & 7, j = bid >> 3;
  const int h = xcd * 4 + (j >> 3), b = (j >> 2) & 1, s = j & 3;
  const int tid = threadIdx.x, lane = tid & 63, w = __builtin_amdgcn_readfirstlane(tid >> 6);
  const int g = lane >> 4, c = lane & 15;
  const int fs = xcd * 22 + j;
  const bool has_ffn = (j < 22) && (fs < NFFN);

  const GAS v4u* xg = (const GAS v4u*)a.x;
  const v4u x0 = xg[lane], x1 = xg[64 + lane];
  const GAS v4u* xs = (const GAS v4u*)(a.x + b * D + 64 * w + 16 * g);
  const v4u xa = xs[0], xb = xs[1];
  v4u wqr[16];
  {
    const GAS v4u* p = (const GAS v4u*)(a.wq + (size_t)(64 * w + 16 * g) * (H * DH) + h * DH + 8 * c);
#pragma unroll
    for (int i = 0; i < 16; ++i) wqr[i] = p[(size_t)i * (H * DH / 8)];
  }
  const int fsc = has_ffn ? fs : 0;
  const v4u wgr = *(const GAS v4u*)(a.wg + (size_t)(64 * w + lane) * F + 8 * fsc);
  const v4u wur = *(const GAS v4u*)(a.wu + (size_t)(64 * w + lane) * F + 8 * fsc);
  const v4u wdr = *(const GAS v4u*)(a.wd + (size_t)(8 * fsc + w) * D + 8 * lane);
  const unsigned short xf0 = ((const GAS unsigned short*)a.x)[64 * w + lane];
  const unsigned short xf1 = ((const GAS unsigned short*)a.x)[D + 64 * w + lane];
  const size_t kvbase = ((size_t)(b * H + h) * SK + (size_t)s * KEYS_PER_WG) * DH;
  const GAS v4u* kp = (const GAS v4u*)(a.k + kvbase) + (size_t)w * (U * 64) + lane;
  const GAS v4u* vp = (const GAS v4u*)(a.v + kvbase) + (size_t)w * (U * 64) + lane;
  constexpr int GSTR = NWAVE * U * 64;
  v4u kr[NG][U], vr[NG][U];
#pragma unroll
  for (int n = 0; n < NG; ++n) {
#pragma unroll
    for (int u = 0; u < U; ++u) kr[n][u] = ldnt(kp + n * GSTR + u * 64);
#pragma unroll
    for (int u = 0; u < U; ++u) vr[n][u] = ldnt(vp + n * GSTR + u * 64);
  }
  __builtin_amdgcn_sched_barrier(0);

  float mean0, rstd0, mean1, rstd1;
  ln_stats(x0, mean0, rstd0);
  ln_stats(x1, mean1, rstd1);
  const float mean = b ? mean1 : mean0, rstd = b ? rstd1 : rstd0;
  {
    float qa[8];
#pragma unroll
    for (int k = 0; k < 8; ++k) qa[k] = 0.f;
#pragma unroll
    for (int i = 0; i < 16; ++i) {
      const unsigned xw = (i < 8) ? xa[i >> 1] : xb[(i - 8) >> 1];
      const float xv = (((i & 1) ? hi(xw) : lo(xw)) - mean) * rstd;
#pragma unroll
      for (int k = 0; k < 4; ++k) { qa[2 * k] = fmaf(xv, lo(wqr[i][k]), qa[2 * k]); qa[2 * k + 1] = fmaf(xv, hi(wqr[i][k]), qa[2 * k + 1]); }
    }
#pragma unroll
    for (int k = 0; k < 8; ++k) qa[k] = xrow_sum(qa[k]);
    if (g == 0) {
      *(f32x4*)(ldsQ + w * 128 + 8 * c) = f32x4{qa[0], qa[1], qa[2], qa[3]};
      *(f32x4*)(ldsQ + w * 128 + 8 * c + 4) = f32x4{qa[4], qa[5], qa[6], qa[7]};
    }
  }
  if (has_ffn) {
    const float n0 = (__uint_as_float((unsigned)xf0 << 16) - mean0) * rstd0;
    const float n1 = (__uint_as_float((unsigned)xf1 << 16) - mean1) * rstd1;
    float t[32];
#pragma unroll
    for (int k = 0; k < 4; ++k) {
      t[2 * k] = n0 * lo(wgr[k]); t[2 * k + 1] = n0 * hi(wgr[k]);
      t[8 + 2 * k] = n1 * lo(wgr[k]); t[8 + 2 * k + 1] = n1 * hi(wgr[k]);
      t[16 + 2 * k] = n0 * lo(wur[k]); t[16 + 2 * k + 1] = n0 * hi(wur[k]);
      t[24 + 2 * k] = n1 * lo(wur[k]); t[24 + 2 * k + 1] = n1 * hi(wur[k]);
    }
#pragma unroll
    for (int k = 0; k < 32; ++k) t[k] = wave_sum(t[k]);
    float tv = t[0];
#pragma unroll
    for (int k = 1; k < 32; ++k) tv = (lane == k) ? t[k] : tv;
    if (lane < 32) ldsF[w * 32 + lane] = tv;
  }
  __syncthreads();
  unsigned qp[4];
  {
    f32x4 q0 = {0, 0, 0, 0}, q1 = {0, 0, 0, 0};
#pragma unroll
    for (int ww = 0; ww < NWAVE; ++ww) { q0 += *(const f32x4*)(ldsQ + ww * 128 + 8 * c); q1 += *(const f32x4*)(ldsQ + ww * 128 + 8 * c + 4); }
    qp[0] = pk_bf16(q0[0], q0[1]); qp[1] = pk_bf16(q0[2], q0[3]); qp[2] = pk_bf16(q1[0], q1[1]); qp[3] = pk_bf16(q1[2], q1[3]);
  }
  if (has_ffn) {
    float g0 = 0.f, g1 = 0.f, u0 = 0.f, u1 = 0.f;
#pragma unroll
    for (int ww = 0; ww < NWAVE; ++ww) { g0 += ldsF[ww * 32 + w]; g1 += ldsF[ww * 32 + 8 + w]; u0 += ldsF[ww * 32 + 16 + w]; u1 += ldsF[ww * 32 + 24 + w]; }
    const float h0 = gelu_tanh(g0) * u0, h1 = gelu_tanh(g1) * u1;
    *(f32x4*)(ldsY + w * 1024 + 8 * lane) = f32x4{h0 * lo(wdr[0]), h0 * hi(wdr[0]), h0 * lo(wdr[1]), h0 * hi(wdr[1])};
    *(f32x4*)(ldsY + w * 1024 + 8 * lane + 4) = f32x4{h0 * lo(wdr[2]), h0 * hi(wdr[2]), h0 * lo(wdr[3]), h0 * hi(wdr[3])};
    *(f32x4*)(ldsY + w * 1024 + 512 + 8 * lane) = f32x4{h1 * lo(wdr[0]), h1 * hi(wdr[0]), h1 * lo(wdr[1]), h1 * hi(wdr[1])};
    *(f32x4*)(ldsY + w * 1024 + 512 + 8 * lane + 4) = f32x4{h1 * lo(wdr[2]), h1 * hi(wdr[2]), h1 * lo(wdr[3]), h1 * hi(wdr[3])};
    __syncthreads();
    float d0 = 0.f, d1 = 0.f;
#pragma unroll
    for (int ww = 0; ww < NWAVE; ++ww) { d0 += ldsY[ww * 1024 + tid]; d1 += ldsY[ww * 1024 + 512 + tid]; }
    float* dp = a.ws + WS_DP + (size_t)fs * 1024;
    dp[tid] = d0; dp[512 + tid] = d1;
  }

  constexpr float C2 = 0.08838834764831845f * 1.4426950408889634f;
  float m = -INFINITY, l = 0.f, acc[8];
#pragma unroll
  for (int k = 0; k < 8; ++k) acc[k] = 0.f;
  v4u wor[16];
  const GAS v4u* wop = (const GAS v4u*)(a.wo + (size_t)(h * DH + 16 * w) * D) + lane;
#pragma unroll
  for (int gi = 0; gi < GROUPS; ++gi) {
    const int n = gi % NG;
    float sc[U];
#pragma unroll
    for (int u = 0; u < U; ++u) {
      float d = dot2(kr[n][u][0], qp[0], 0.f);
      d = dot2(kr[n][u][1], qp[1], d);
      d = dot2(kr[n][u][2], qp[2], d);
      d = dot2(kr[n][u][3], qp[3], d);
      sc[u] = d;
    }
    __builtin_amdgcn_sched_barrier(0);
    if (gi + NG < GROUPS) {
#pragma unroll
      for (int u = 0; u < U; ++u) kr[n][u] = ldnt(kp + (gi + NG) * GSTR + u * 64);
    }
    __builtin_amdgcn_sched_barrier(0);
#pragma unroll
    for (int u = 0; u < U; ++u) sc[u] = row16_sum(sc[u]) * C2;
    float mx = m;
#pragma unroll
    for (int u = 0; u < U; ++u) mx = fmaxf(mx, sc[u]);
    const float alpha = __builtin_amdgcn_exp2f(m - mx);
    m = mx;
    float p[U], ps = 0.f;
#pragma unroll
    for (int u = 0; u < U; ++u) { p[u] = __builtin_amdgcn_exp2f(sc[u] - mx); ps += p[u]; }
    l = fmaf(l, alpha, ps);
#pragma unroll
    for (int k = 0; k < 8; ++k) acc[k] *= alpha;
#pragma unroll
    for (int u = 0; u < U; ++u) {
#pragma unroll
      for (int k = 0; k < 4; ++k) { acc[2 * k] = fmaf(p[u], lo(vr[n][u][k]), acc[2 * k]); acc[2 * k + 1] = fmaf(p[u], hi(vr[n][u][k]), acc[2 * k + 1]); }
    }
    __builtin_amdgcn_sched_barrier(0);
    if (gi + NG < GROUPS) {
#pragma unroll
      for (int u = 0; u < U; ++u) vr[n][u] = ldnt(vp + (gi + NG) * GSTR + u * 64);
    } else {
      constexpr int PER = 16 / NG;
      const int t = gi + NG - GROUPS;
#pragma unroll
      for (int r = 0; r < PER; ++r) wor[t * PER + r] = wop[(t * PER + r) * 64];
    }
    __builtin_amdgcn_sched_barrier(0);
  }

  {
    const float mw = xrow_max(m);
    const float sc = __builtin_amdgcn_exp2f(m - mw);
    l = xrow_sum(l * sc);
#pragma unroll
    for (int k = 0; k < 8; ++k) acc[k] = xrow_sum(acc[k] * sc);
    if (g == 0) {
      *(f32x4*)(ldsM + w * 132 + 4 + 8 * c) = f32x4{acc[0], acc[1], acc[2], acc[3]};
      *(f32x4*)(ldsM + w * 132 + 4 + 8 * c + 4) = f32x4{acc[4], acc[5], acc[6], acc[7]};
      if (c == 0) { ldsM[w * 132] = mw; ldsM[w * 132 + 1] = l; }
    }
  }
  __syncthreads();
  float M = -INFINITY, L = 0.f;
  {
    float mv[8], lv[8];
#pragma unroll
    for (int ww = 0; ww < NWAVE; ++ww) { mv[ww] = ldsM[ww * 132]; lv[ww] = ldsM[ww * 132 + 1]; M = fmaxf(M, mv[ww]); }
    float o = 0.f;
#pragma unroll
    for (int ww = 0; ww < NWAVE; ++ww) { const float e = __builtin_amdgcn_exp2f(mv[ww] - M); L = fmaf(e, lv[ww], L); o = fmaf(e, ldsM[ww * 132 + 4 + 16 * w + c], o); }
    float y[8];
#pragma unroll
    for (int k = 0; k < 8; ++k) y[k] = 0.f;
#pragma unroll
    for (int r = 0; r < 16; ++r) {
      const float orr = __builtin_bit_cast(float, __builtin_amdgcn_readlane(__builtin_bit_cast(int, o), r));
#pragma unroll
      for (int k = 0; k < 4; ++k) { y[2 * k] = fmaf(orr, lo(wor[r][k]), y[2 * k]); y[2 * k + 1] = fmaf(orr, hi(wor[r][k]), y[2 * k + 1]); }
    }
    *(f32x4*)(ldsY + w * 512 + 8 * lane) = f32x4{y[0], y[1], y[2], y[3]};
    *(f32x4*)(ldsY + w * 512 + 8 * lane + 4) = f32x4{y[4], y[5], y[6], y[7]};
  }
  __syncthreads();
  {
    float y = 0.f;
#pragma unroll
    for (int ww = 0; ww < NWAVE; ++ww) y += ldsY[ww * 512 + tid];
    const int pid = (b * H + h) * NSPLIT + s;
    a.ws[WS_Y + (size_t)pid * 512 + tid] = y;
    if (tid == 0) { a.ws[WS_ML + 2 * pid] = M; a.ws[WS_ML + 2 * pid + 1] = L; }
  }
}

__global__ void __launch_bounds__(256) k_fin(Args a) {
  __shared__ __attribute__((aligned(16))) float red[2][64][16];
  const int b = blockIdx.x >> 5, col0 = (blockIdx.x & 31) * 16;
  const int tid = threadIdx.x, p = tid >> 2, cq = tid & 3;
  const float* ws = a.ws;
  f32x4 y[2], ml[2][2], dd[3];
#pragma unroll
  for (int i = 0; i < 2; ++i) {
    const int pid = b * 128 + p + 64 * i;
    y[i] = *(const f32x4*)(ws + WS_Y + (size_t)pid * 512 + col0 + 4 * cq);
    ml[i][0] = *(const f32x4*)(ws + WS_ML + 2 * (pid & ~3));
    ml[i][1] = *(const f32x4*)(ws + WS_ML + 2 * (pid & ~3) + 4);
  }
#pragma unroll
  for (int i = 0; i < 3; ++i) {
    const int fs = p + 64 * i;
    dd[i] = (fs < NFFN) ? *(const f32x4*)(ws + WS_DP + (size_t)fs * 1024 + b * 512 + col0 + 4 * cq) : f32x4{0, 0, 0, 0};
  }
  f32x4 sa = {0, 0, 0, 0};
#pragma unroll
  for (int i = 0; i < 2; ++i) {
    const float m0 = ml[i][0][0], l0 = ml[i][0][1], m1 = ml[i][0][2], l1 = ml[i][0][3];
    const float m2 = ml[i][1][0], l2 = ml[i][1][1], m3 = ml[i][1][2], l3 = ml[i][1][3];
    const float M = fmaxf(fmaxf(m0, m1), fmaxf(m2, m3));
    const float e0 = __builtin_amdgcn_exp2f(m0 - M), e1 = __builtin_amdgcn_exp2f(m1 - M), e2 = __builtin_amdgcn_exp2f(m2 - M), e3 = __builtin_amdgcn_exp2f(m3 - M);
    const float L = e0 * l0 + e1 * l1 + e2 * l2 + e3 * l3;
    const int sp = p & 3;
    const float e = sp == 0 ? e0 : sp == 1 ? e1 : sp == 2 ? e2 : e3;
    const float wt = e / L;
    sa += y[i] * wt;
  }
  const f32x4 da = dd[0] + dd[1] + dd[2];
  *(f32x4*)(&red[0][p][4 * cq]) = sa;
  *(f32x4*)(&red[1][p][4 * cq]) = da;
  __syncthreads();
  if (tid < 32) {
    const int which = tid >> 4, col = tid & 15;
    float t = 0.f;
#pragma unroll 8
    for (int i = 0; i < 64; ++i) t += red[which][i][col];
    const float other = __shfl_xor(t, 16);
    if (which == 0) {
      const float sv = t, dv = other;
      const float gte = dv / (1.f + __expf(-dv));
      const float xv = __uint_as_float((unsigned)a.x[b * D + col0 + col] << 16);
      const float o = xv + sv * gte;
      const __bf16 ob = (__bf16)o;
      a.out[b * D + col0 + col] = __builtin_bit_cast(unsigned short, ob);
    }
  }
}

extern "C" void kernel_launch(void* const* d_in, const int* in_sizes, int n_in, void* d_out, int out_size, void* d_ws, size_t ws_size,
                              hipStream_t stream) {
  Args a;
  a.x = (const unsigned short*)d_in[0];
  a.k = (const unsigned short*)d_in[1];
  a.v = (const unsigned short*)d_in[2];
  a.wq = (const unsigned short*)d_in[3];
  a.wo = (const unsigned short*)d_in[4];
  a.wg = (const unsigned short*)d_in[6];
  a.wu = (const unsigned short*)d_in[7];
  a.wd = (const unsigned short*)d_in[8];
  a.out = (unsigned short*)d_out;
  a.ws = (float*)d_ws;
  hipLaunchKernelGGL(k_attn, dim3(256), dim3(NTHR), 0, stream, a);
  hipLaunchKernelGGL(k_fin, dim3(64), dim3(256), 0, stream, a);
}
```

```cpp
#include <hip/hip_runtime.h>
#include <hip/hip_bf16.h>
#include <stdint.h>

typedef unsigned v4u __attribute__((ext_vector_type(4)));
typedef unsigned v2u __attribute__((ext_vector_type(2)));
typedef float f32x4 __attribute__((ext_vector_type(4)));
typedef float f32x2 __attribute__((ext_vector_type(2)));
typedef __bf16 bf16x2 __attribute__((ext_vector_type(2)));
#define GAS __attribute__((address_space(1)))

constexpr int D = 512, H = 32, SK = 8192, DH = 128, F = 1376;
constexpr int NSPLIT = 4, KEYS_PER_WG = SK / NSPLIT;
constexpr int NWAVE = 8, NTHR = NWAVE * 64;
constexpr int NFFN = F / 32;
#define U_ 4
#define NG_ 3
#define WQP_ 6
#define FL_ 0
#define FG_ 4
#define WO0_ 10
#define WOP_ 10
#define DPG_ 12
#define FPP_ 2
#define WPP_ 2
#define POLL_SLEEP_ 2
constexpr int U = U_;
constexpr int NG = NG_;
constexpr int GROUPS = KEYS_PER_WG / NWAVE / (4 * U);

constexpr int WS_EPOCH = 0;
constexpr int WS_ML = 4096;
constexpr int WS_Y = WS_ML + 256 * 16;
constexpr int WS_DP = WS_Y + 256 * 256 * 16;
constexpr int WS_END = WS_DP + NFFN * 512 * 16;
constexpr unsigned SPIN_CAP = 400000;

#define STAMP(i)
#define SB() __builtin_amdgcn_sched_barrier(0)

__device__ __forceinline__ float dot2(unsigned k, unsigned q, float acc) {
  return __builtin_amdgcn_fdot2_f32_bf16(__builtin_bit_cast(bf16x2, k), __builtin_bit_cast(bf16x2, q), acc, false);
}
template <int CTRL> __device__ __forceinline__ float dpp(float x) {
  return __builtin_bit_cast(float, __builtin_amdgcn_mov_dpp(__builtin_bit_cast(int, x), CTRL, 0xf, 0xf, true));
}
__device__ __forceinline__ float row16_sum(float x) {
  x += dpp<0x128>(x);
  x += dpp<0x124>(x);
  x += dpp<0x122>(x);
  x += dpp<0x121>(x);
  return x;
}
__device__ __forceinline__ float fold32(float a, float b) {
  auto t = __builtin_amdgcn_permlane32_swap(__float_as_uint(a), __float_as_uint(b), false, false);
  return __uint_as_float(t[0]) + __uint_as_float(t[1]);
}
__device__ __forceinline__ float fold16(float a, float b) {
  auto t = __builtin_amdgcn_permlane16_swap(__float_as_uint(a), __float_as_uint(b), false, false);
  return __uint_as_float(t[0]) + __uint_as_float(t[1]);
}
__device__ __forceinline__ float xrow_sum(float x) { x = fold16(x, x); return fold32(x, x); }
__device__ __forceinline__ float xrow_max(float x) {
  auto s = __builtin_amdgcn_permlane16_swap(__float_as_uint(x), __float_as_uint(x), false, false);
  x = fmaxf(__uint_as_float(s[0]), __uint_as_float(s[1]));
  auto t = __builtin_amdgcn_permlane32_swap(__float_as_uint(x), __float_as_uint(x), false, false);
  return fmaxf(__uint_as_float(t[0]), __uint_as_float(t[1]));
}
__device__ __forceinline__ f32x2 fold8(const float (&v)[8]) {
  const float u0 = fold32(v[0], v[4]), u1 = fold32(v[1], v[5]), u2 = fold32(v[2], v[6]), u3 = fold32(v[3], v[7]);
  return f32x2{fold16(u0, u2), fold16(u1, u3)};
}
__device__ __forceinline__ float wave_sum(float x) { return xrow_sum(row16_sum(x)); }
__device__ __forceinline__ float lo(unsigned u) { return __uint_as_float(u << 16); }
__device__ __forceinline__ float hi(unsigned u) { return __uint_as_float(u & 0xffff0000u); }
__device__ __forceinline__ unsigned pk_bf16(float a, float b) {
  bf16x2 r = {(__bf16)a, (__bf16)b};
  return __builtin_bit_cast(unsigned, r);
}
__device__ __forceinline__ v4u ldnt(const GAS v4u* p) { return __builtin_nontemporal_load(p); }
__device__ __forceinline__ float gelu_tanh(float x) {
  const float u = 0.7978845608028654f * (x + 0.044715f * x * x * x);
  const float e = __expf(2.f * u);
  const float t = 1.f - 2.f / (e + 1.f);
  return 0.5f * x * (1.f + t);
}
__device__ __forceinline__ void pace_wait(unsigned due) {
  asm volatile("1:\n\ts_memrealtime s[90:91]\n\ts_waitcnt lgkmcnt(0)\n\ts_sub_u32 s90, s90, %0\n\ts_cmp_ge_i32 s90, 0\n\ts_cbranch_scc1 2f\n\ts_sleep 2\n\ts_branch 1b\n2:"
               :: "s"(due) : "s90", "s91", "scc");
}
#define TP_ 122
#define T0_ 0
#define PACE(pt) pace_wait(tbase + (unsigned)((pt) * TP_))
constexpr int AUX_SC1 = 16;
__device__ __forceinline__ void st_unit(__amdgpu_buffer_rsrc_t rs, int off, float v0, float v1, unsigned ta, unsigned tb) {
  __builtin_amdgcn_raw_buffer_store_b128(v4u{__float_as_uint(v0), ta, __float_as_uint(v1), tb}, rs, off, 0, AUX_SC1);
}
__device__ __forceinline__ v4u ld_unit(__amdgpu_buffer_rsrc_t rs, int off) { return __builtin_amdgcn_raw_buffer_load_b128(rs, off, 0, AUX_SC1); }

struct Args {
  const unsigned short* x; const unsigned short* k; const unsigned short* v; const unsigned short* wq; const unsigned short* wo;
  const unsigned short* wg; const unsigned short* wu; const unsigned short* wd; unsigned short* out; unsigned char* ws;
};

__device__ __forceinline__ void ln_stats(v4u xr, float& mean, float& rstd) {
  float f[8];
#pragma unroll
  for (int i = 0; i < 4; ++i) { f[2 * i] = lo(xr[i]); f[2 * i + 1] = hi(xr[i]); }
  float s = 0.f;
#pragma unroll
  for (int i = 0; i < 8; ++i) s += f[i];
  mean = wave_sum(s) * (1.f / D);
  float q = 0.f;
#pragma unroll
  for (int i = 0; i < 8; ++i) { const float d = f[i] - mean; q = fmaf(d, d, q); }
  const float var = wave_sum(q) * (1.f / D);
  rstd = 1.f / sqrtf(var + 1e-5f);
}

__global__ void __launch_bounds__(NTHR, 2) k_all(Args a) {
  __shared__ __attribute__((aligned(16))) float lds[8 * 1024 + 8 * 132 + 8 * 128 + 8 * 128];
  float* ldsY = lds;
  float* ldsM = lds + 8 * 1024;
  float* ldsF = ldsM + 8 * 132;
  float* ldsQ = ldsF + 8 * 128;
  const unsigned tstart = (unsigned)__builtin_amdgcn_s_memrealtime();
  SB();
  STAMP(0);
  const int bid = blockIdx.x;
  const int xcd = bid & 7, j = bid >> 3;
  const int h = xcd * 4 + (j >> 3), b = (j >> 2) & 1, s = j & 3;
  const int tid = threadIdx.x, lane = tid & 63, w = __builtin_amdgcn_readfirstlane(tid >> 6);
  const int g = lane >> 4, c = lane & 15;
  const unsigned tbase = tstart + T0_ + (unsigned)(w * TP_ / 8);
  const int fs = xcd * 6 + j;
  const bool has_ffn = (j < 6) && (fs < NFFN);
  const bool is_fin = j >= 24;
  const int cid = xcd * 8 + (j - 24);
  const __amdgpu_buffer_rsrc_t rs = __builtin_amdgcn_make_buffer_rsrc((void*)a.ws, 0, WS_END, 0x00020000);

  const GAS v4u* xg = (const GAS v4u*)a.x;
  const v4u x0 = xg[lane], x1 = xg[64 + lane];
  const GAS v4u* xs = (const GAS v4u*)(a.x + b * D + 64 * w + 16 * g);
  const v4u xa = xs[0], xb = xs[1];
  const v2u xf0 = *(const GAS v2u*)(a.x + 64 * w + 4 * (lane >> 2));
  const v2u xf1 = *(const GAS v2u*)(a.x + D + 64 * w + 4 * (lane >> 2));
  const unsigned short xo = ((const GAS unsigned short*)a.x)[((cid >> 5) & 1) * D + (cid & 31) * 16 + c];
  const unsigned long long ep = __hip_atomic_load((const GAS unsigned long long*)(a.ws + WS_EPOCH), __ATOMIC_RELAXED, __HIP_MEMORY_SCOPE_AGENT);
  const size_t kvbase = ((size_t)(b * H + h) * SK + (size_t)s * KEYS_PER_WG) * DH;
  const GAS v4u* kp = (const GAS v4u*)(a.k + kvbase) + (size_t)w * (U * 64) + lane;
  const GAS v4u* vp = (const GAS v4u*)(a.v + kvbase) + (size_t)w * (U * 64) + lane;
  constexpr int GSTR = NWAVE * U * 64;
  constexpr int NR = NG * U * 2;
  v4u kr[NG][U], vr[NG][U];
  v4u wqr[16];
  const GAS v4u* wqp = (const GAS v4u*)(a.wq + (size_t)(64 * w + 16 * g) * (H * DH) + h * DH + 8 * c);
#define RING_LOAD(i_) do { const int n_ = (i_) / (2 * U), r_ = (i_) % (2 * U); \
    if (r_ < U) kr[n_][r_] = ldnt(kp + n_ * GSTR + r_ * 64); else vr[n_][r_ - U] = ldnt(vp + n_ * GSTR + (r_ - U) * 64); } while (0)
#define RING4(pt) do { PACE(pt); SB(); RING_LOAD(4 * (pt)); RING_LOAD(4 * (pt) + 1); RING_LOAD(4 * (pt) + 2); RING_LOAD(4 * (pt) + 3); SB(); } while (0)
#define WQ_LOADS(pt) do { _Pragma("unroll") for (int i_ = (pt) * WQP_; i_ < ((pt) + 1) * WQP_; ++i_) if (i_ < 16) wqr[i_] = ldnt(wqp + (size_t)i_ * (H * DH / 8)); SB(); } while (0)
#define WQ_FMA(pt) do { _Pragma("unroll") for (int i = (pt) * WQP_; i < ((pt) + 1) * WQP_; ++i) if (i < 16) { \
      const unsigned xw = (i < 8) ? xa[i >> 1] : xb[(i - 8) >> 1]; \
      const float xv = (((i & 1) ? hi(xw) : lo(xw)) - mean) * rstd; \
      _Pragma("unroll") for (int k = 0; k < 4; ++k) { qa[2 * k] = fmaf(xv, lo(wqr[i][k]), qa[2 * k]); qa[2 * k + 1] = fmaf(xv, hi(wqr[i][k]), qa[2 * k + 1]); } } \
      asm volatile("" : "+v"(qa[0]), "+v"(qa[1]), "+v"(qa[2]), "+v"(qa[3]), "+v"(qa[4]), "+v"(qa[5]), "+v"(qa[6]), "+v"(qa[7])); SB(); } while (0)
  SB();
  RING4(0); WQ_LOADS(0);
  STAMP(1);
  float mean0, rstd0, mean1, rstd1;
  ln_stats(x0, mean0, rstd0);
  ln_stats(x1, mean1, rstd1);
  const float mean = b ? mean1 : mean0, rstd = b ? rstd1 : rstd0;
  const unsigned tagA = __builtin_amdgcn_readfirstlane((unsigned)ep) + 1u, tagB = __builtin_amdgcn_readfirstlane((unsigned)(ep >> 32)) + 0x9E3779B9u;
  float qa[8];
#pragma unroll
  for (int k = 0; k < 8; ++k) qa[k] = 0.f;
  SB();
  RING4(1); WQ_LOADS(1); WQ_FMA(0);
  RING4(2); WQ_LOADS(2); WQ_FMA(1);
  RING4(3); WQ_LOADS(3); WQ_FMA(2); WQ_FMA(3);
  *(f32x2*)(ldsQ + w * 128 + 8 * c + 2 * g) = fold8(qa);
  if (NG > 2) RING4(4);
  STAMP(2);
  __syncthreads();
  unsigned qp[4];
  {
    f32x4 q0 = {0, 0, 0, 0}, q1 = {0, 0, 0, 0};
#pragma unroll
    for (int ww = 0; ww < NWAVE; ++ww) { q0 += *(const f32x4*)(ldsQ + ww * 128 + 8 * c); q1 += *(const f32x4*)(ldsQ + ww * 128 + 8 * c + 4); }
    qp[0] = pk_bf16(q0[0], q0[1]); qp[1] = pk_bf16(q0[2], q0[3]); qp[2] = pk_bf16(q1[0], q1[1]); qp[3] = pk_bf16(q1[2], q1[3]);
  }
  if (NG > 2) RING4(5);
  STAMP(3);

  constexpr float C2 = 0.08838834764831845f * 1.4426950408889634f;
  float m = -INFINITY, l = 0.f, acc[8];
#pragma unroll
  for (int k = 0; k < 8; ++k) acc[k] = 0.f;
  v4u wor[16];
  v4u ffw[12];
  const int fgo = has_ffn ? (64 * w + 4 * (lane >> 2)) * F + 32 * fs + 8 * (lane & 3) : 0, fgs = has_ffn ? F : 0;
  const int fdo = has_ffn ? (32 * fs + 4 * w) * D + 8 * lane : 0, fds = has_ffn ? D : 0;
#define FFN_LOAD(i_) do { if ((i_) < 4) ffw[i_] = ldnt((const GAS v4u*)(a.wg + fgo + (i_) * fgs)); else if ((i_) < 8) ffw[i_] = ldnt((const GAS v4u*)(a.wu + fgo + ((i_) - 4) * fgs)); \
    else ffw[i_] = ldnt((const GAS v4u*)(a.wd + fdo + ((i_) - 8) * fds)); } while (0)
#define EXTRA(gi_, half_) do { const int pt_ = 2 * (gi_) + (half_); \
      _Pragma("unroll") for (int r_ = 0; r_ < FPP_; ++r_) { const int fi_ = (pt_ - FL_) * FPP_ + r_; if (fi_ >= 0 && fi_ < 12) FFN_LOAD(fi_); } \
      _Pragma("unroll") for (int r_ = 0; r_ < WPP_; ++r_) { const int wi_ = (pt_ - WO0_) * WPP_ + r_; if (wi_ >= 0 && wi_ < 16) wor[wi_] = ldnt(wop + wi_ * 64); } } while (0)
  const int bc = (cid >> 5) & 1, cu0 = (cid & 31) * 8;
  const int gp = tid >> 2, qd = tid & 3;
  const int dpoff = WS_DP + ((gp < NFFN ? gp : 0) * 512 + bc * 256 + cu0 + 2 * qd) * 16;
  v4u e0, e1;
  const GAS v4u* wop = (const GAS v4u*)(a.wo + (size_t)(h * DH + 16 * w) * D) + lane;
#pragma unroll
  for (int gi = 0; gi < GROUPS; ++gi) {
    const int n = gi % NG;
    STAMP(8 + gi);
    float sc[U];
#pragma unroll
    for (int u = 0; u < U; ++u) {
      float d = dot2(kr[n][u][0], qp[0], 0.f);
      d = dot2(kr[n][u][1], qp[1], d);
      d = dot2(kr[n][u][2], qp[2], d);
      d = dot2(kr[n][u][3], qp[3], d);
      sc[u] = d;
    }
    asm volatile("" : "+v"(sc[0]), "+v"(sc[1]), "+v"(sc[2]), "+v"(sc[3]));
    SB();
    if (gi + NG < GROUPS) {
      PACE(2 * (gi + NG)); SB();
#pragma unroll
      for (int u = 0; u < U; ++u) kr[n][u] = ldnt(kp + (gi + NG) * GSTR + u * 64);
    }
    EXTRA(gi, 0);
    SB();
#pragma unroll
    for (int u = 0; u < U; ++u) sc[u] = row16_sum(sc[u]) * C2;
    float mx = m;
#pragma unroll
    for (int u = 0; u < U; ++u) mx = fmaxf(mx, sc[u]);
    const float alpha = __builtin_amdgcn_exp2f(m - mx);
    m = mx;
    float p[U], ps = 0.f;
#pragma unroll
    for (int u = 0; u < U; ++u) { p[u] = __builtin_amdgcn_exp2f(sc[u] - mx); ps += p[u]; }
    l = fmaf(l, alpha, ps);
#pragma unroll
    for (int k = 0; k < 8; ++k) acc[k] *= alpha;
#pragma unroll
    for (int u = 0; u < U; ++u) {
#pragma unroll
      for (int k = 0; k < 4; ++k) { acc[2 * k] = fmaf(p[u], lo(vr[n][u][k]), acc[2 * k]); acc[2 * k + 1] = fmaf(p[u], hi(vr[n][u][k]), acc[2 * k + 1]); }
    }
    asm volatile("" : "+v"(acc[0]), "+v"(acc[1]), "+v"(acc[2]), "+v"(acc[3]), "+v"(acc[4]), "+v"(acc[5]), "+v"(acc[6]), "+v"(acc[7]), "+v"(l), "+v"(m));
    SB();
    if (gi + NG < GROUPS) {
      PACE(2 * (gi + NG) + 1); SB();
#pragma unroll
      for (int u = 0; u < U; ++u) vr[n][u] = ldnt(vp + (gi + NG) * GSTR + u * 64);
    }
    EXTRA(gi, 1);
    if (gi == DPG_) { e0 = ld_unit(rs, is_fin ? dpoff : WS_DP); e1 = ld_unit(rs, is_fin ? dpoff + 16 : WS_DP); }
    SB();
    if (gi == WOP_) {
#pragma unroll
      for (int i = 0; i < 8; ++i) {
        const v4u r0 = wor[2 * i], r1 = wor[2 * i + 1];
        v4u e, o;
#pragma unroll
        for (int k = 0; k < 2; ++k) {
          e[2 * k] = __builtin_amdgcn_perm(r1[k], r0[k], 0x05040100u); e[2 * k + 1] = __builtin_amdgcn_perm(r1[k], r0[k], 0x07060302u);
          o[2 * k] = __builtin_amdgcn_perm(r1[2 + k], r0[2 + k], 0x05040100u); o[2 * k + 1] = __builtin_amdgcn_perm(r1[2 + k], r0[2 + k], 0x07060302u);
        }
        asm volatile("" : "+v"(e), "+v"(o));
        wor[2 * i] = e; wor[2 * i + 1] = o;
      }
      SB();
    }
    if (gi == FG_) {
      if (has_ffn) {
        float t[32];
#pragma unroll
        for (int k = 0; k < 32; ++k) t[k] = 0.f;
#pragma unroll
        for (int i = 0; i < 4; ++i) {
          const float n0 = (((i & 1) ? hi(xf0[i >> 1]) : lo(xf0[i >> 1])) - mean0) * rstd0, n1 = (((i & 1) ? hi(xf1[i >> 1]) : lo(xf1[i >> 1])) - mean1) * rstd1;
#pragma unroll
          for (int k = 0; k < 4; ++k) {
            const float gl = lo(ffw[i][k]), gh = hi(ffw[i][k]), ul = lo(ffw[4 + i][k]), uh = hi(ffw[4 + i][k]);
            t[2 * k] = fmaf(n0, gl, t[2 * k]); t[2 * k + 1] = fmaf(n0, gh, t[2 * k + 1]);
            t[8 + 2 * k] = fmaf(n1, gl, t[8 + 2 * k]); t[8 + 2 * k + 1] = fmaf(n1, gh, t[8 + 2 * k + 1]);
            t[16 + 2 * k] = fmaf(n0, ul, t[16 + 2 * k]); t[16 + 2 * k + 1] = fmaf(n0, uh, t[16 + 2 * k + 1]);
            t[24 + 2 * k] = fmaf(n1, ul, t[24 + 2 * k]); t[24 + 2 * k + 1] = fmaf(n1, uh, t[24 + 2 * k + 1]);
          }
        }
#pragma unroll
        for (int k = 0; k < 16; ++k) t[k] = fold32(t[k], t[16 + k]);
#pragma unroll
        for (int k = 0; k < 8; ++k) { float z = fold16(t[k], t[8 + k]); z += dpp<0x124>(z); z += dpp<0x128>(z); t[k] = z; }
        if (c < 4) {
          *(f32x4*)(ldsF + w * 128 + g * 32 + c * 8) = f32x4{t[0], t[1], t[2], t[3]};
          *(f32x4*)(ldsF + w * 128 + g * 32 + c * 8 + 4) = f32x4{t[4], t[5], t[6], t[7]};
        }
        __syncthreads();
        float gs = 0.f, us = 0.f;
        const int hx = ((lane >> 2) & 1) * 32 + 4 * w + (lane & 3);
#pragma unroll
        for (int ww = 0; ww < NWAVE; ++ww) { gs += ldsF[ww * 128 + hx]; us += ldsF[ww * 128 + 64 + hx]; }
        const float hv = gelu_tanh(gs) * us;
        float dd[16];
#pragma unroll
        for (int k = 0; k < 16; ++k) dd[k] = 0.f;
#pragma unroll
        for (int r = 0; r < 4; ++r) {
          const float h0 = __builtin_bit_cast(float, __builtin_amdgcn_readlane(__builtin_bit_cast(int, hv), r));
          const float h1 = __builtin_bit_cast(float, __builtin_amdgcn_readlane(__builtin_bit_cast(int, hv), 4 + r));
#pragma unroll
          for (int k = 0; k < 4; ++k) {
            const float wl = lo(ffw[8 + r][k]), wh = hi(ffw[8 + r][k]);
            dd[2 * k] = fmaf(h0, wl, dd[2 * k]); dd[2 * k + 1] = fmaf(h0, wh, dd[2 * k + 1]);
            dd[8 + 2 * k] = fmaf(h1, wl, dd[8 + 2 * k]); dd[8 + 2 * k + 1] = fmaf(h1, wh, dd[8 + 2 * k + 1]);
          }
        }
        *(f32x4*)(ldsY + w * 1024 + 8 * lane) = f32x4{dd[0], dd[1], dd[2], dd[3]};
        *(f32x4*)(ldsY + w * 1024 + 8 * lane + 4) = f32x4{dd[4], dd[5], dd[6], dd[7]};
        *(f32x4*)(ldsY + w * 1024 + 512 + 8 * lane) = f32x4{dd[8], dd[9], dd[10], dd[11]};
        *(f32x4*)(ldsY + w * 1024 + 512 + 8 * lane + 4) = f32x4{dd[12], dd[13], dd[14], dd[15]};
        __syncthreads();
        f32x2 dv = {0.f, 0.f};
#pragma unroll
        for (int ww = 0; ww < NWAVE; ++ww) dv += *(const f32x2*)(ldsY + ww * 1024 + 2 * tid);
        st_unit(rs, WS_DP + (fs * 512 + tid) * 16, dv[0], dv[1], tagA, tagB);
      }
      SB();
    }
  }
  STAMP(4);

  {
    const float mw = xrow_max(m);
    const float sc = __builtin_amdgcn_exp2f(m - mw);
    l = xrow_sum(l * sc);
#pragma unroll
    for (int k = 0; k < 8; ++k) acc[k] *= sc;
    *(f32x2*)(ldsM + w * 132 + 4 + 8 * c + 2 * g) = fold8(acc);
    if (lane == 0) *(f32x2*)(ldsM + w * 132) = f32x2{mw, l};
  }
  __syncthreads();
  STAMP(5);
  const int pid = (b * H + h) * NSPLIT + s;
  {
    float M = -INFINITY, L = 0.f;
    f32x2 ml[8]; float av[8];
#pragma unroll
    for (int ww = 0; ww < NWAVE; ++ww) { ml[ww] = *(const f32x2*)(ldsM + ww * 132); av[ww] = ldsM[ww * 132 + 4 + 16 * w + c]; }
#pragma unroll
    for (int ww = 0; ww < NWAVE; ++ww) M = fmaxf(M, ml[ww][0]);
    float o = 0.f;
#pragma unroll
    for (int ww = 0; ww < NWAVE; ++ww) { const float e = __builtin_amdgcn_exp2f(ml[ww][0] - M); L = fmaf(e, ml[ww][1], L); o = fmaf(e, av[ww], o); }
    if (tid == 0) st_unit(rs, WS_ML + pid * 16, M, L, tagA, tagB);
    const unsigned op = pk_bf16(o, dpp<0xB1>(o));
    float y[8];
#pragma unroll
    for (int k = 0; k < 8; ++k) y[k] = 0.f;
#pragma unroll
    for (int i = 0; i < 8; ++i) {
      const unsigned oi = __builtin_amdgcn_readlane(op, 2 * i);
#pragma unroll
      for (int k = 0; k < 4; ++k) { y[k] = dot2(wor[2 * i][k], oi, y[k]); y[4 + k] = dot2(wor[2 * i + 1][k], oi, y[4 + k]); }
    }
    *(f32x4*)(ldsY + w * 512 + 8 * lane) = f32x4{y[0], y[1], y[2], y[3]};
    *(f32x4*)(ldsY + w * 512 + 8 * lane + 4) = f32x4{y[4], y[5], y[6], y[7]};
  }
  __syncthreads();
  if (tid < 256) {
    f32x2 y = {0.f, 0.f};
#pragma unroll
    for (int ww = 0; ww < NWAVE; ++ww) y += *(const f32x2*)(ldsY + ww * 512 + 2 * tid);
    st_unit(rs, WS_Y + (pid * 256 + tid) * 16, y[0], y[1], tagA, tagB);
  }
  STAMP(6);

  if (is_fin) {
    const int col0 = 2 * cu0;
    const int yoff = WS_Y + ((bc * 128 + gp) * 256 + cu0 + 2 * qd) * 16;
    const int mloff = WS_ML + (bc * 128 + (gp & ~3) + qd) * 16;
    const int otoff = WS_ML + ((1 - bc) * 128 + gp) * 16;
    v4u y0, y1, ml;
    unsigned spin = 0;
#pragma unroll 1
    for (; spin < SPIN_CAP; ++spin) {
      asm volatile("" ::: "memory");
      ml = ld_unit(rs, mloff);
      const v4u ot = ld_unit(rs, cid == 0 ? otoff : mloff);
      if (__all((((ml[1] ^ tagA) | (ml[3] ^ tagB)) | ((ot[1] ^ tagA) | (ot[3] ^ tagB))) == 0u)) break;
      __builtin_amdgcn_s_sleep(POLL_SLEEP_);
    }
#pragma unroll 1
    for (; spin < SPIN_CAP; ++spin) {
      asm volatile("" ::: "memory");
      y0 = ld_unit(rs, yoff); y1 = ld_unit(rs, yoff + 16);
      if (__all((((y0[1] ^ tagA) | (y1[1] ^ tagA)) | ((y0[3] ^ tagB) | (y1[3] ^ tagB))) == 0u)) break;
    }
#pragma unroll 1
    for (; spin < SPIN_CAP; ++spin) {
      if (__all((((e0[1] ^ tagA) | (e1[1] ^ tagA)) | ((e0[3] ^ tagB) | (e1[3] ^ tagB))) == 0u)) break;
      __builtin_amdgcn_s_sleep(POLL_SLEEP_);
      asm volatile("" ::: "memory");
      e0 = ld_unit(rs, dpoff); e1 = ld_unit(rs, dpoff + 16);
    }
    const float mq = __uint_as_float(ml[0]), lq = __uint_as_float(ml[2]);
    float M = fmaxf(mq, dpp<0xB1>(mq)); M = fmaxf(M, dpp<0x4E>(M));
    const float e = __builtin_amdgcn_exp2f(mq - M);
    float L = e * lq; L += dpp<0xB1>(L); L += dpp<0x4E>(L);
    const float eb0 = dpp<0x00>(e), eb1 = dpp<0x55>(e), eb2 = dpp<0xAA>(e), eb3 = dpp<0xFF>(e);
    const int sp = gp & 3;
    const float wt = (sp == 0 ? eb0 : sp == 1 ? eb1 : sp == 2 ? eb2 : eb3) / L;
    const bool hd = gp < NFFN;
    float v[8];
    v[0] = wt * __uint_as_float(y0[0]); v[1] = wt * __uint_as_float(y0[2]); v[2] = wt * __uint_as_float(y1[0]); v[3] = wt * __uint_as_float(y1[2]);
    v[4] = hd ? __uint_as_float(e0[0]) : 0.f; v[5] = hd ? __uint_as_float(e0[2]) : 0.f; v[6] = hd ? __uint_as_float(e1[0]) : 0.f; v[7] = hd ? __uint_as_float(e1[2]) : 0.f;
    f32x2 r = fold8(v);
    r[0] += dpp<0x124>(r[0]); r[1] += dpp<0x124>(r[1]);
    r[0] += dpp<0x128>(r[0]); r[1] += dpp<0x128>(r[1]);
    if (c < 4) *(f32x2*)(ldsF + w * 32 + c * 8 + 2 * g) = r;
    __syncthreads();
    if (tid < 16) {
      float sv = 0.f, dv = 0.f;
#pragma unroll
      for (int ww = 0; ww < NWAVE; ++ww) { sv += ldsF[ww * 32 + (tid >> 2) * 8 + (tid & 3)]; dv += ldsF[ww * 32 + (tid >> 2) * 8 + 4 + (tid & 3)]; }
      const float gte = dv / (1.f + __expf(-dv));
      const float o = __uint_as_float((unsigned)xo << 16) + sv * gte;
      const __bf16 ob = (__bf16)o;
      ((GAS unsigned short*)a.out)[bc * D + col0 + tid] = __builtin_bit_cast(unsigned short, ob);
      if (cid == 0 && tid == 0)
        __hip_atomic_store((GAS unsigned long long*)(a.ws + WS_EPOCH), ((unsigned long long)tagB << 32) | tagA, __ATOMIC_RELAXED, __HIP_MEMORY_SCOPE_AGENT);
    }
  }
}

extern "C" void kernel_launch(void* const* d_in, const int* in_sizes, int n_in, void* d_out, int out_size, void* d_ws, size_t ws_size,
                              hipStream_t stream) {
  Args a;
  a.x = (const unsigned short*)d_in[0];
  a.k = (const unsigned short*)d_in[1];
  a.v = (const unsigned short*)d_in[2];
  a.wq = (const unsigned short*)d_in[3];
  a.wo = (const unsigned short*)d_in[4];
  a.wg = (const unsigned short*)d_in[6];
  a.wu = (const unsigned short*)d_in[7];
  a.wd = (const unsigned short*)d_in[8];
  a.out = (unsigned short*)d_out;
  a.ws = (unsigned char*)d_ws;
  hipLaunchKernelGGL(k_all, dim3(256), dim3(NTHR), 0, stream, a);
}
```

```cpp
#include <hip/hip_runtime.h>
#include <hip/hip_bf16.h>
#include <stdint.h>

typedef unsigned v4u __attribute__((ext_vector_type(4)));
typedef unsigned v2u __attribute__((ext_vector_type(2)));
typedef float f32x4 __attribute__((ext_vector_type(4)));
typedef float f32x2 __attribute__((ext_vector_type(2)));
typedef __bf16 bf16x2 __attribute__((ext_vector_type(2)));
#define GAS __attribute__((address_space(1)))

constexpr int D = 512, H = 32, SK = 8192, DH = 128, F = 1376;
constexpr int NSPLIT = 4, KEYS_PER_WG = SK / NSPLIT;
constexpr int NWAVE = 8, NTHR = NWAVE * 64;
constexpr int NFFN = F / 32;
#define U_ 4
#define NG_ 3
#define WQP_ 6
#define FL_ 0
#define FG_ 4
#define WO0_ 10
#define WOP_ 10
#define DPG_ 12
#define FPP_ 2
#define WPP_ 2
#define POLL_SLEEP_ 2
constexpr int U = U_;
constexpr int NG = NG_;
constexpr int GROUPS = KEYS_PER_WG / NWAVE / (4 * U);

constexpr int WS_EPOCH = 0;
constexpr int WS_ML = 4096;
constexpr int WS_Y = WS_ML + 256 * 16;
constexpr int WS_DP = WS_Y + 256 * 256 * 16;
constexpr int WS_END = WS_DP + NFFN * 512 * 16;
constexpr unsigned SPIN_CAP = 400000;

#define STAMP(i)
#define SB() __builtin_amdgcn_sched_barrier(0)

__device__ __forceinline__ float dot2(unsigned k, unsigned q, float acc) {
  return __builtin_amdgcn_fdot2_f32_bf16(__builtin_bit_cast(bf16x2, k), __builtin_bit_cast(bf16x2, q), acc, false);
}
template <int CTRL> __device__ __forceinline__ float dpp(float x) {
  return __builtin_bit_cast(float, __builtin_amdgcn_mov_dpp(__builtin_bit_cast(int, x), CTRL, 0xf, 0xf, true));
}
__device__ __forceinline__ float row16_sum(float x) {
  x += dpp<0x128>(x);
  x += dpp<0x124>(x);
  x += dpp<0x122>(x);
  x += dpp<0x121>(x);
  return x;
}
__device__ __forceinline__ float fold32(float a, float b) {
  auto t = __builtin_amdgcn_permlane32_swap(__float_as_uint(a), __float_as_uint(b), false, false);
  return __uint_as_float(t[0]) + __uint_as_float(t[1]);
}
__device__ __forceinline__ float fold16(float a, float b) {
  auto t = __builtin_amdgcn_permlane16_swap(__float_as_uint(a), __float_as_uint(b), false, false);
  return __uint_as_float(t[0]) + __uint_as_float(t[1]);
}
__device__ __forceinline__ float xrow_sum(float x) { x = fold16(x, x); return fold32(x, x); }
__device__ __forceinline__ float xrow_max(float x) {
  auto s = __builtin_amdgcn_permlane16_swap(__float_as_uint(x), __float_as_uint(x), false, false);
  x = fmaxf(__uint_as_float(s[0]), __uint_as_float(s[1]));
  auto t = __builtin_amdgcn_permlane32_swap(__float_as_uint(x), __float_as_uint(x), false, false);
  return fmaxf(__uint_as_float(t[0]), __uint_as_float(t[1]));
}
__device__ __forceinline__ f32x2 fold8(const float (&v)[8]) {
  const float u0 = fold32(v[0], v[4]), u1 = fold32(v[1], v[5]), u2 = fold32(v[2], v[6]), u3 = fold32(v[3], v[7]);
  return f32x2{fold16(u0, u2), fold16(u1, u3)};
}
__device__ __forceinline__ float wave_sum(float x) { return xrow_sum(row16_sum(x)); }
__device__ __forceinline__ float lo(unsigned u) { return __uint_as_float(u << 16); }
__device__ __forceinline__ float hi(unsigned u) { return __uint_as_float(u & 0xffff0000u); }
__device__ __forceinline__ unsigned pk_bf16(float a, float b) {
  bf16x2 r = {(__bf16)a, (__bf16)b};
  return __builtin_bit_cast(unsigned, r);
}
#define KNT_ 1
#define VNT_ 0
#define WNT_ 0
template <int NT> __device__ __forceinline__ v4u ldp(const GAS v4u* p) { if (NT) return __builtin_nontemporal_load(p); else return *p; }
__device__ __forceinline__ float gelu_tanh(float x) {
  const float u = 0.7978845608028654f * (x + 0.044715f * x * x * x);
  const float e = __expf(2.f * u);
  const float t = 1.f - 2.f / (e + 1.f);
  return 0.5f * x * (1.f + t);
}
__device__ __forceinline__ void pace_wait(unsigned due) {
  asm volatile("1:\n\ts_memrealtime s[90:91]\n\ts_waitcnt lgkmcnt(0)\n\ts_sub_u32 s90, s90, %0\n\ts_cmp_ge_i32 s90, 0\n\ts_cbranch_scc1 2f\n\ts_sleep 2\n\ts_branch 1b\n2:"
               :: "s"(due) : "s90", "s91", "scc");
}
#define TP_ 126
#define T0_ 0
#define PACE(pt) pace_wait(tbase + (unsigned)((pt) * TP_))
constexpr int AUX_SC1 = 16;
__device__ __forceinline__ void st_unit(__amdgpu_buffer_rsrc_t rs, int off, float v0, float v1, unsigned ta, unsigned tb) {
  __builtin_amdgcn_raw_buffer_store_b128(v4u{__float_as_uint(v0), ta, __float_as_uint(v1), tb}, rs, off, 0, AUX_SC1);
}
__device__ __forceinline__ v4u ld_unit(__amdgpu_buffer_rsrc_t rs, int off) { return __builtin_amdgcn_raw_buffer_load_b128(rs, off, 0, AUX_SC1); }

struct Args {
  const unsigned short* x; const unsigned short* k; const unsigned short* v; const unsigned short* wq; const unsigned short* wo;
  const unsigned short* wg; const unsigned short* wu; const unsigned short* wd; unsigned short* out; unsigned char* ws;
};

__device__ __forceinline__ void ln_stats(v4u xr, float& mean, float& rstd) {
  float f[8];
#pragma unroll
  for (int i = 0; i < 4; ++i) { f[2 * i] = lo(xr[i]); f[2 * i + 1] = hi(xr[i]); }
  float s = 0.f;
#pragma unroll
  for (int i = 0; i < 8; ++i) s += f[i];
  mean = wave_sum(s) * (1.f / D);
  float q = 0.f;
#pragma unroll
  for (int i = 0; i < 8; ++i) { const float d = f[i] - mean; q = fmaf(d, d, q); }
  const float var = wave_sum(q) * (1.f / D);
  rstd = 1.f / sqrtf(var + 1e-5f);
}

__global__ void __launch_bounds__(NTHR, 2) k_all(Args a) {
  __shared__ __attribute__((aligned(16))) float lds[8 * 1024 + 8 * 132 + 8 * 128 + 8 * 128];
  float* ldsY = lds;
  float* ldsM = lds + 8 * 1024;
  float* ldsF = ldsM + 8 * 132;
  float* ldsQ = ldsF + 8 * 128;
  const unsigned tstart = (unsigned)__builtin_amdgcn_s_memrealtime();
  SB();
  STAMP(0);
  const int bid = blockIdx.x;
  const int xcd = bid & 7, j = bid >> 3;
  const int h = xcd * 4 + (j >> 3), b = (j >> 2) & 1, s = j & 3;
  const int tid = threadIdx.x, lane = tid & 63, w = __builtin_amdgcn_readfirstlane(tid >> 6);
  const int g = lane >> 4, c = lane & 15;
  const unsigned tbase = tstart + T0_ + (unsigned)(w * TP_ / 8);
  const int fs = xcd * 6 + j;
  const bool has_ffn = (j < 6) && (fs < NFFN);
  const bool is_fin = j >= 24;
  const int cid = xcd * 8 + (j - 24);
  const __amdgpu_buffer_rsrc_t rs = __builtin_amdgcn_make_buffer_rsrc((void*)a.ws, 0, WS_END, 0x00020000);

  const GAS v4u* xg = (const GAS v4u*)a.x;
  const v4u x0 = xg[lane], x1 = xg[64 + lane];
  const GAS v4u* xs = (const GAS v4u*)(a.x + b * D + 64 * w + 16 * g);
  const v4u xa = xs[0], xb = xs[1];
  const v2u xf0 = *(const GAS v2u*)(a.x + 64 * w + 4 * (lane >> 2));
  const v2u xf1 = *(const GAS v2u*)(a.x + D + 64 * w + 4 * (lane >> 2));
  const unsigned short xo = ((const GAS unsigned short*)a.x)[((cid >> 5) & 1) * D + (cid & 31) * 16 + c];
  const unsigned long long ep = __hip_atomic_load((const GAS unsigned long long*)(a.ws + WS_EPOCH), __ATOMIC_RELAXED, __HIP_MEMORY_SCOPE_AGENT);
  const size_t kvbase = ((size_t)(b * H + h) * SK + (size_t)s * KEYS_PER_WG) * DH;
  const GAS v4u* kp = (const GAS v4u*)(a.k + kvbase) + (size_t)w * (U * 64) + lane;
  const GAS v4u* vp = (const GAS v4u*)(a.v + kvbase) + (size_t)w * (U * 64) + lane;
  constexpr int GSTR = NWAVE * U * 64;
  constexpr int NR = NG * U * 2;
  v4u kr[NG][U], vr[NG][U];
  v4u wqr[16];
  const GAS v4u* wqp = (const GAS v4u*)(a.wq + (size_t)(64 * w + 16 * g) * (H * DH) + h * DH + 8 * c);
#define RING_LOAD(i_) do { const int n_ = (i_) / (2 * U), r_ = (i_) % (2 * U); \
    if (r_ < U) kr[n_][r_] = ldp<KNT_>(kp + n_ * GSTR + r_ * 64); else vr[n_][r_ - U] = ldp<VNT_>(vp + n_ * GSTR + (r_ - U) * 64); } while (0)
#define RING4(pt) do { PACE(pt); SB(); RING_LOAD(4 * (pt)); RING_LOAD(4 * (pt) + 1); RING_LOAD(4 * (pt) + 2); RING_LOAD(4 * (pt) + 3); SB(); } while (0)
#define WQ_LOADS(pt) do { _Pragma("unroll") for (int i_ = (pt) * WQP_; i_ < ((pt) + 1) * WQP_; ++i_) if (i_ < 16) wqr[i_] = ldp<WNT_>(wqp + (size_t)i_ * (H * DH / 8)); SB(); } while (0)
#define WQ_FMA(pt) do { _Pragma("unroll") for (int i = (pt) * WQP_; i < ((pt) + 1) * WQP_; ++i) if (i < 16) { \
      const unsigned xw = (i < 8) ? xa[i >> 1] : xb[(i - 8) >> 1]; \
      const float xv = (((i & 1) ? hi(xw) : lo(xw)) - mean) * rstd; \
      _Pragma("unroll") for (int k = 0; k < 4; ++k) { qa[2 * k] = fmaf(xv, lo(wqr[i][k]), qa[2 * k]); qa[2 * k + 1] = fmaf(xv, hi(wqr[i][k]), qa[2 * k + 1]); } } \
      asm volatile("" : "+v"(qa[0]), "+v"(qa[1]), "+v"(qa[2]), "+v"(qa[3]), "+v"(qa[4]), "+v"(qa[5]), "+v"(qa[6]), "+v"(qa[7])); SB(); } while (0)
  SB();
  RING4(0); WQ_LOADS(0);
  STAMP(1);
  float mean0, rstd0, mean1, rstd1;
  ln_stats(x0, mean0, rstd0);
  ln_stats(x1, mean1, rstd1);
  const float mean = b ? mean1 : mean0, rstd = b ? rstd1 : rstd0;
  const unsigned tagA = __builtin_amdgcn_readfirstlane((unsigned)ep) + 1u, tagB = __builtin_amdgcn_readfirstlane((unsigned)(ep >> 32)) + 0x9E3779B9u;
  float qa[8];
#pragma unroll
  for (int k = 0; k < 8; ++k) qa[k] = 0.f;
  SB();
  RING4(1); WQ_LOADS(1); WQ_FMA(0);
  RING4(2); WQ_LOADS(2); WQ_FMA(1);
  RING4(3); WQ_LOADS(3); WQ_FMA(2); WQ_FMA(3);
  *(f32x2*)(ldsQ + w * 128 + 8 * c + 2 * g) = fold8(qa);
  if (NG > 2) RING4(4);
  STAMP(2);
  __syncthreads();
  unsigned qp[4];
  {
    f32x4 q0 = {0, 0, 0, 0}, q1 = {0, 0, 0, 0};
#pragma unroll
    for (int ww = 0; ww < NWAVE; ++ww) { q0 += *(const f32x4*)(ldsQ + ww * 128 + 8 * c); q1 += *(const f32x4*)(ldsQ + ww * 128 + 8 * c + 4); }
    qp[0] = pk_bf16(q0[0], q0[1]); qp[1] = pk_bf16(q0[2], q0[3]); qp[2] = pk_bf16(q1[0], q1[1]); qp[3] = pk_bf16(q1[2], q1[3]);
  }
  if (NG > 2) RING4(5);
  STAMP(3);

  constexpr float C2 = 0.08838834764831845f * 1.4426950408889634f;
  float m = -INFINITY, l = 0.f, acc[8];
#pragma unroll
  for (int k = 0; k < 8; ++k) acc[k] = 0.f;
  v4u wor[16];
  v4u ffw[12];
  const int fgo = has_ffn ? (64 * w + 4 * (lane >> 2)) * F + 32 * fs + 8 * (lane & 3) : 0, fgs = has_ffn ? F : 0;
  const int fdo = has_ffn ? (32 * fs + 4 * w) * D + 8 * lane : 0, fds = has_ffn ? D : 0;
#define FFN_LOAD(i_) do { if ((i_) < 4) ffw[i_] = ldp<WNT_>((const GAS v4u*)(a.wg + fgo + (i_) * fgs)); else if ((i_) < 8) ffw[i_] = ldp<WNT_>((const GAS v4u*)(a.wu + fgo + ((i_) - 4) * fgs)); \
    else ffw[i_] = ldp<WNT_>((const GAS v4u*)(a.wd + fdo + ((i_) - 8) * fds)); } while (0)
#define EXTRA(gi_, half_) do { const int pt_ = 2 * (gi_) + (half_); \
      _Pragma("unroll") for (int r_ = 0; r_ < FPP_; ++r_) { const int fi_ = (pt_ - FL_) * FPP_ + r_; if (fi_ >= 0 && fi_ < 12) FFN_LOAD(fi_); } \
      _Pragma("unroll") for (int r_ = 0; r_ < WPP_; ++r_) { const int wi_ = (pt_ - WO0_) * WPP_ + r_; if (wi_ >= 0 && wi_ < 16) wor[wi_] = ldp<WNT_>(wop + wi_ * 64); } } while (0)
  const int bc = (cid >> 5) & 1, cu0 = (cid & 31) * 8;
  const int gp = tid >> 2, qd = tid & 3;
  const int dpoff = WS_DP + ((gp < NFFN ? gp : 0) * 512 + bc * 256 + cu0 + 2 * qd) * 16;
  v4u e0, e1;
  const GAS v4u* wop = (const GAS v4u*)(a.wo + (size_t)(h * DH + 16 * w) * D) + lane;
#pragma unroll
  for (int gi = 0; gi < GROUPS; ++gi) {
    const int n = gi % NG;
    STAMP(8 + gi);
    float sc[U];
#pragma unroll
    for (int u = 0; u < U; ++u) {
      float d = dot2(kr[n][u][0], qp[0], 0.f);
      d = dot2(kr[n][u][1], qp[1], d);
      d = dot2(kr[n][u][2], qp[2], d);
      d = dot2(kr[n][u][3], qp[3], d);
      sc[u] = d;
    }
    asm volatile("" : "+v"(sc[0]), "+v"(sc[1]), "+v"(sc[2]), "+v"(sc[3]));
    SB();
    if (gi + NG < GROUPS) {
      PACE(2 * (gi + NG)); SB();
#pragma unroll
      for (int u = 0; u < U; ++u) kr[n][u] = ldp<KNT_>(kp + (gi + NG) * GSTR + u * 64);
    }
    EXTRA(gi, 0);
    SB();
#pragma unroll
    for (int u = 0; u < U; ++u) sc[u] = row16_sum(sc[u]) * C2;
    float mx = m;
#pragma unroll
    for (int u = 0; u < U; ++u) mx = fmaxf(mx, sc[u]);
    const float alpha = __builtin_amdgcn_exp2f(m - mx);
    m = mx;
    float p[U], ps = 0.f;
#pragma unroll
    for (int u = 0; u < U; ++u) { p[u] = __builtin_amdgcn_exp2f(sc[u] - mx); ps += p[u]; }
    l = fmaf(l, alpha, ps);
#pragma unroll
    for (int k = 0; k < 8; ++k) acc[k] *= alpha;
#pragma unroll
    for (int u = 0; u < U; ++u) {
#pragma unroll
      for (int k = 0; k < 4; ++k) { acc[2 * k] = fmaf(p[u], lo(vr[n][u][k]), acc[2 * k]); acc[2 * k + 1] = fmaf(p[u], hi(vr[n][u][k]), acc[2 * k + 1]); }
    }
    asm volatile("" : "+v"(acc[0]), "+v"(acc[1]), "+v"(acc[2]), "+v"(acc[3]), "+v"(acc[4]), "+v"(acc[5]), "+v"(acc[6]), "+v"(acc[7]), "+v"(l), "+v"(m));
    SB();
    if (gi + NG < GROUPS) {
      PACE(2 * (gi + NG) + 1); SB();
#pragma unroll
      for (int u = 0; u < U; ++u) vr[n][u] = ldp<VNT_>(vp + (gi + NG) * GSTR + u * 64);
    }
    EXTRA(gi, 1);
    if (gi == DPG_) { e0 = ld_unit(rs, is_fin ? dpoff : WS_DP); e1 = ld_unit(rs, is_fin ? dpoff + 16 : WS_DP); }
    SB();
    if (gi == WOP_) {
#pragma unroll
      for (int i = 0; i < 8; ++i) {
        const v4u r0 = wor[2 * i], r1 = wor[2 * i + 1];
        v4u e, o;
#pragma unroll
        for (int k = 0; k < 2; ++k) {
          e[2 * k] = __builtin_amdgcn_perm(r1[k], r0[k], 0x05040100u); e[2 * k + 1] = __builtin_amdgcn_perm(r1[k], r0[k], 0x07060302u);
          o[2 * k] = __builtin_amdgcn_perm(r1[2 + k], r0[2 + k], 0x05040100u); o[2 * k + 1] = __builtin_amdgcn_perm(r1[2 + k], r0[2 + k], 0x07060302u);
        }
        asm volatile("" : "+v"(e), "+v"(o));
        wor[2 * i] = e; wor[2 * i + 1] = o;
      }
      SB();
    }
    if (gi == FG_) {
      if (has_ffn) {
        float t[32];
#pragma unroll
        for (int k = 0; k < 32; ++k) t[k] = 0.f;
#pragma unroll
        for (int i = 0; i < 4; ++i) {
          const float n0 = (((i & 1) ? hi(xf0[i >> 1]) : lo(xf0[i >> 1])) - mean0) * rstd0, n1 = (((i & 1) ? hi(xf1[i >> 1]) : lo(xf1[i >> 1])) - mean1) * rstd1;
#pragma unroll
          for (int k = 0; k < 4; ++k) {
            const float gl = lo(ffw[i][k]), gh = hi(ffw[i][k]), ul = lo(ffw[4 + i][k]), uh = hi(ffw[4 + i][k]);
            t[2 * k] = fmaf(n0, gl, t[2 * k]); t[2 * k + 1] = fmaf(n0, gh, t[2 * k + 1]);
            t[8 + 2 * k] = fmaf(n1, gl, t[8 + 2 * k]); t[8 + 2 * k + 1] = fmaf(n1, gh, t[8 + 2 * k + 1]);
            t[16 + 2 * k] = fmaf(n0, ul, t[16 + 2 * k]); t[16 + 2 * k + 1] = fmaf(n0, uh, t[16 + 2 * k + 1]);
            t[24 + 2 * k] = fmaf(n1, ul, t[24 + 2 * k]); t[24 + 2 * k + 1] = fmaf(n1, uh, t[24 + 2 * k + 1]);
          }
        }
#pragma unroll
        for (int k = 0; k < 16; ++k) t[k] = fold32(t[k], t[16 + k]);
#pragma unroll
        for (int k = 0; k < 8; ++k) { float z = fold16(t[k], t[8 + k]); z += dpp<0x124>(z); z += dpp<0x128>(z); t[k] = z; }
        if (c < 4) {
          *(f32x4*)(ldsF + w * 128 + g * 32 + c * 8) = f32x4{t[0], t[1], t[2], t[3]};
          *(f32x4*)(ldsF + w * 128 + g * 32 + c * 8 + 4) = f32x4{t[4], t[5], t[6], t[7]};
        }
        __syncthreads();
        float gs = 0.f, us = 0.f;
        const int hx = ((lane >> 2) & 1) * 32 + 4 * w + (lane & 3);
#pragma unroll
        for (int ww = 0; ww < NWAVE; ++ww) { gs += ldsF[ww * 128 + hx]; us += ldsF[ww * 128 + 64 + hx]; }
        const float hv = gelu_tanh(gs) * us;
        float dd[16];
#pragma unroll
        for (int k = 0; k < 16; ++k) dd[k] = 0.f;
#pragma unroll
        for (int r = 0; r < 4; ++r) {
          const float h0 = __builtin_bit_cast(float, __builtin_amdgcn_readlane(__builtin_bit_cast(int, hv), r));
          const float h1 = __builtin_bit_cast(float, __builtin_amdgcn_readlane(__builtin_bit_cast(int, hv), 4 + r));
#pragma unroll
          for (int k = 0; k < 4; ++k) {
            const float wl = lo(ffw[8 + r][k]), wh = hi(ffw[8 + r][k]);
            dd[2 * k] = fmaf(h0, wl, dd[2 * k]); dd[2 * k + 1] = fmaf(h0, wh, dd[2 * k + 1]);
            dd[8 + 2 * k] = fmaf(h1, wl, dd[8 + 2 * k]); dd[8 + 2 * k + 1] = fmaf(h1, wh, dd[8 + 2 * k + 1]);
          }
        }
        *(f32x4*)(ldsY + w * 1024 + 8 * lane) = f32x4{dd[0], dd[1], dd[2], dd[3]};
        *(f32x4*)(ldsY + w * 1024 + 8 * lane + 4) = f32x4{dd[4], dd[5], dd[6], dd[7]};
        *(f32x4*)(ldsY + w * 1024 + 512 + 8 * lane) = f32x4{dd[8], dd[9], dd[10], dd[11]};
        *(f32x4*)(ldsY + w * 1024 + 512 + 8 * lane + 4) = f32x4{dd[12], dd[13], dd[14], dd[15]};
        __syncthreads();
        f32x2 dv = {0.f, 0.f};
#pragma unroll
        for (int ww = 0; ww < NWAVE; ++ww) dv += *(const f32x2*)(ldsY + ww * 1024 + 2 * tid);
        st_unit(rs, WS_DP + (fs * 512 + tid) * 16, dv[0], dv[1], tagA, tagB);
      }
      SB();
    }
  }
  STAMP(4);

  {
    const float mw = xrow_max(m);
    const float sc = __builtin_amdgcn_exp2f(m - mw);
    l = xrow_sum(l * sc);
#pragma unroll
    for (int k = 0; k < 8; ++k) acc[k] *= sc;
    *(f32x2*)(ldsM + w * 132 + 4 + 8 * c + 2 * g) = fold8(acc);
    if (lane == 0) *(f32x2*)(ldsM + w * 132) = f32x2{mw, l};
  }
  __syncthreads();
  STAMP(5);
  const int pid = (b * H + h) * NSPLIT + s;
  {
    float M = -INFINITY, L = 0.f;
    f32x2 ml[8]; float av[8];
#pragma unroll
    for (int ww = 0; ww < NWAVE; ++ww) { ml[ww] = *(const f32x2*)(ldsM + ww * 132); av[ww] = ldsM[ww * 132 + 4 + 16 * w + c]; }
#pragma unroll
    for (int ww = 0; ww < NWAVE; ++ww) M = fmaxf(M, ml[ww][0]);
    float o = 0.f;
#pragma unroll
    for (int ww = 0; ww < NWAVE; ++ww) { const float e = __builtin_amdgcn_exp2f(ml[ww][0] - M); L = fmaf(e, ml[ww][1], L); o = fmaf(e, av[ww], o); }
    if (tid == 0) st_unit(rs, WS_ML + pid * 16, M, L, tagA, tagB);
    const unsigned op = pk_bf16(o, dpp<0xB1>(o));
    float y[8];
#pragma unroll
    for (int k = 0; k < 8; ++k) y[k] = 0.f;
#pragma unroll
    for (int i = 0; i < 8; ++i) {
      const unsigned oi = __builtin_amdgcn_readlane(op, 2 * i);
#pragma unroll
      for (int k = 0; k < 4; ++k) { y[k] = dot2(wor[2 * i][k], oi, y[k]); y[4 + k] = dot2(wor[2 * i + 1][k], oi, y[4 + k]); }
    }
    *(f32x4*)(ldsY + w * 512 + 8 * lane) = f32x4{y[0], y[1], y[2], y[3]};
    *(f32x4*)(ldsY + w * 512 + 8 * lane + 4) = f32x4{y[4], y[5], y[6], y[7]};
  }
  __syncthreads();
  if (tid < 256) {
    f32x2 y = {0.f, 0.f};
#pragma unroll
    for (int ww = 0; ww < NWAVE; ++ww) y += *(const f32x2*)(ldsY + ww * 512 + 2 * tid);
    st_unit(rs, WS_Y + (pid * 256 + tid) * 16, y[0], y[1], tagA, tagB);
  }
  STAMP(6);

  if (is_fin) {
    const int col0 = 2 * cu0;
    const int yoff = WS_Y + ((bc * 128 + gp) * 256 + cu0 + 2 * qd) * 16;
    const int mloff = WS_ML + (bc * 128 + (gp & ~3) + qd) * 16;
    const int otoff = WS_ML + ((1 - bc) * 128 + gp) * 16;
    v4u y0, y1, ml;
    unsigned spin = 0;
#pragma unroll 1
    for (; spin < SPIN_CAP; ++spin) {
      asm volatile("" ::: "memory");
      ml = ld_unit(rs, mloff);
      const v4u ot = ld_unit(rs, cid == 0 ? otoff : mloff);
      if (__all((((ml[1] ^ tagA) | (ml[3] ^ tagB)) | ((ot[1] ^ tagA) | (ot[3] ^ tagB))) == 0u)) break;
      __builtin_amdgcn_s_sleep(POLL_SLEEP_);
    }
#pragma unroll 1
    for (; spin < SPIN_CAP; ++spin) {
      asm volatile("" ::: "memory");
      y0 = ld_unit(rs, yoff); y1 = ld_unit(rs, yoff + 16);
      if (__all((((y0[1] ^ tagA) | (y1[1] ^ tagA)) | ((y0[3] ^ tagB) | (y1[3] ^ tagB))) == 0u)) break;
    }
#pragma unroll 1
    for (; spin < SPIN_CAP; ++spin) {
      if (__all((((e0[1] ^ tagA) | (e1[1] ^ tagA)) | ((e0[3] ^ tagB) | (e1[3] ^ tagB))) == 0u)) break;
      __builtin_amdgcn_s_sleep(POLL_SLEEP_);
      asm volatile("" ::: "memory");
      e0 = ld_unit(rs, dpoff); e1 = ld_unit(rs, dpoff + 16);
    }
    const float mq = __uint_as_float(ml[0]), lq = __uint_as_float(ml[2]);
    float M = fmaxf(mq, dpp<0xB1>(mq)); M = fmaxf(M, dpp<0x4E>(M));
    const float e = __builtin_amdgcn_exp2f(mq - M);
    float L = e * lq; L += dpp<0xB1>(L); L += dpp<0x4E>(L);
    const float eb0 = dpp<0x00>(e), eb1 = dpp<0x55>(e), eb2 = dpp<0xAA>(e), eb3 = dpp<0xFF>(e);
    const int sp = gp & 3;
    const float wt = (sp == 0 ? eb0 : sp == 1 ? eb1 : sp == 2 ? eb2 : eb3) / L;
    const bool hd = gp < NFFN;
    float v[8];
    v[0] = wt * __uint_as_float(y0[0]); v[1] = wt * __uint_as_float(y0[2]); v[2] = wt * __uint_as_float(y1[0]); v[3] = wt * __uint_as_float(y1[2]);
    v[4] = hd ? __uint_as_float(e0[0]) : 0.f; v[5] = hd ? __uint_as_float(e0[2]) : 0.f; v[6] = hd ? __uint_as_float(e1[0]) : 0.f; v[7] = hd ? __uint_as_float(e1[2]) : 0.f;
    f32x2 r = fold8(v);
    r[0] += dpp<0x124>(r[0]); r[1] += dpp<0x124>(r[1]);
    r[0] += dpp<0x128>(r[0]); r[1] += dpp<0x128>(r[1]);
    if (c < 4) *(f32x2*)(ldsF + w * 32 + c * 8 + 2 * g) = r;
    __syncthreads();
    if (tid < 16) {
      float sv = 0.f, dv = 0.f;
#pragma unroll
      for (int ww = 0; ww < NWAVE; ++ww) { sv += ldsF[ww * 32 + (tid >> 2) * 8 + (tid & 3)]; dv += ldsF[ww * 32 + (tid >> 2) * 8 + 4 + (tid & 3)]; }
      const float gte = dv / (1.f + __expf(-dv));
      const float o = __uint_as_float((unsigned)xo << 16) + sv * gte;
      const __bf16 ob = (__bf16)o;
      ((GAS unsigned short*)a.out)[bc * D + col0 + tid] = __builtin_bit_cast(unsigned short, ob);
      if (cid == 0 && tid == 0)
        __hip_atomic_store((GAS unsigned long long*)(a.ws + WS_EPOCH), ((unsigned long long)tagB << 32) | tagA, __ATOMIC_RELAXED, __HIP_MEMORY_SCOPE_AGENT);
    }
  }
}

extern "C" void kernel_launch(void* const* d_in, const int* in_sizes, int n_in, void* d_out, int out_size, void* d_ws, size_t ws_size,
                              hipStream_t stream) {
  Args a;
  a.x = (const unsigned short*)d_in[0];
  a.k = (const unsigned short*)d_in[1];
  a.v = (const unsigned short*)d_in[2];
  a.wq = (const unsigned short*)d_in[3];
  a.wo = (const unsigned short*)d_in[4];
  a.wg = (const unsigned short*)d_in[6];
  a.wu = (const unsigned short*)d_in[7];
  a.wd = (const unsigned short*)d_in[8];
  a.out = (unsigned short*)d_out;
  a.ws = (unsigned char*)d_ws;
  hipLaunchKernelGGL(k_all, dim3(256), dim3(NTHR), 0, stream, a);
}
```

```cpp
#include <hip/hip_runtime.h>
#include <hip/hip_bf16.h>
#include <stdint.h>

typedef unsigned v4u __attribute__((ext_vector_type(4)));
typedef unsigned v2u __attribute__((ext_vector_type(2)));
typedef float f32x4 __attribute__((ext_vector_type(4)));
typedef float f32x2 __attribute__((ext_vector_type(2)));
typedef __bf16 bf16x2 __attribute__((ext_vector_type(2)));
#define GAS __attribute__((address_space(1)))

constexpr int D = 512, H = 32, SK = 8192, DH = 128, F = 1376;
constexpr int NSPLIT = 4, KEYS_PER_WG = SK / NSPLIT;
constexpr int NWAVE = 8, NTHR = NWAVE * 64;
constexpr int NFFN = F / 32;
#define U_ 4
#define NG_ 3
#define S2L_ 0
#define FG_ 2
#define WO0_ 2
#define WPP_ 1
#define WOP_ 10
#define DPG_ 11
#define POLL_SLEEP_ 1
constexpr int U = U_;
constexpr int NG = NG_;
constexpr int GROUPS = KEYS_PER_WG / NWAVE / (4 * U);

constexpr int WS_EPOCH = 0;
constexpr int WS_ML = 4096;
constexpr int WS_Y = WS_ML + 256 * 16;
constexpr int WS_DP = WS_Y + 256 * 256 * 16;
constexpr int WS_S1 = WS_DP + NFFN * 512 * 16;
constexpr int WS_END = WS_S1 + 32 * 8 * 4 * 64 * 16;
constexpr unsigned SPIN_CAP = 400000;

#define STAMP(i)
#define SB() __builtin_amdgcn_sched_barrier(0)

__device__ __forceinline__ float dot2(unsigned k, unsigned q, float acc) {
  return __builtin_amdgcn_fdot2_f32_bf16(__builtin_bit_cast(bf16x2, k), __builtin_bit_cast(bf16x2, q), acc, false);
}
template <int CTRL> __device__ __forceinline__ float dpp(float x) {
  return __builtin_bit_cast(float, __builtin_amdgcn_mov_dpp(__builtin_bit_cast(int, x), CTRL, 0xf, 0xf, true));
}
__device__ __forceinline__ float row16_sum(float x) {
  x += dpp<0x128>(x);
  x += dpp<0x124>(x);
  x += dpp<0x122>(x);
  x += dpp<0x121>(x);
  return x;
}
__device__ __forceinline__ float fold32(float a, float b) {
  auto t = __builtin_amdgcn_permlane32_swap(__float_as_uint(a), __float_as_uint(b), false, false);
  return __uint_as_float(t[0]) + __uint_as_float(t[1]);
}
__device__ __forceinline__ float fold16(float a, float b) {
  auto t = __builtin_amdgcn_permlane16_swap(__float_as_uint(a), __float_as_uint(b), false, false);
  return __uint_as_float(t[0]) + __uint_as_float(t[1]);
}
__device__ __forceinline__ float xrow_sum(float x) { x = fold16(x, x); return fold32(x, x); }
__device__ __forceinline__ float xrow_max(float x) {
  auto s = __builtin_amdgcn_permlane16_swap(__float_as_uint(x), __float_as_uint(x), false, false);
  x = fmaxf(__uint_as_float(s[0]), __uint_as_float(s[1]));
  auto t = __builtin_amdgcn_permlane32_swap(__float_as_uint(x), __float_as_uint(x), false, false);
  return fmaxf(__uint_as_float(t[0]), __uint_as_float(t[1]));
}
__device__ __forceinline__ f32x2 fold8(const float (&v)[8]) {
  const float u0 = fold32(v[0], v[4]), u1 = fold32(v[1], v[5]), u2 = fold32(v[2], v[6]), u3 = fold32(v[3], v[7]);
  return f32x2{fold16(u0, u2), fold16(u1, u3)};
}
__device__ __forceinline__ float wave_sum(float x) { return xrow_sum(row16_sum(x)); }
__device__ __forceinline__ float lo(unsigned u) { return __uint_as_float(u << 16); }
__device__ __forceinline__ float hi(unsigned u) { return __uint_as_float(u & 0xffff0000u); }
__device__ __forceinline__ unsigned pk_bf16(float a, float b) {
  bf16x2 r = {(__bf16)a, (__bf16)b};
  return __builtin_bit_cast(unsigned, r);
}
#define KNT_ 1
#define VNT_ 0
#define WNT_ 0
template <int NT> __device__ __forceinline__ v4u ldp(const GAS v4u* p) { if (NT) return __builtin_nontemporal_load(p); else return *p; }
__device__ __forceinline__ float gelu_tanh(float x) {
  const float u = 0.7978845608028654f * (x + 0.044715f * x * x * x);
  const float e = __expf(2.f * u);
  const float t = 1.f - 2.f / (e + 1.f);
  return 0.5f * x * (1.f + t);
}
__device__ __forceinline__ void pace_wait(unsigned& due16, unsigned step16, unsigned gap16) {
  asm volatile("1:\n\ts_memrealtime s[90:91]\n\ts_waitcnt lgkmcnt(0)\n\ts_lshl_b32 s90, s90, 4\n\ts_sub_u32 s91, s90, %0\n\ts_cmp_ge_i32 s91, 0\n\ts_cbranch_scc1 2f\n\ts_sleep 2\n\ts_branch 1b\n"
               "2:\n\ts_add_u32 s91, s91, %2\n\ts_max_i32 s91, s91, %1\n\ts_add_u32 %0, %0, s91"
               : "+s"(due16) : "s"(step16), "s"(gap16) : "s90", "s91", "scc");
}
#define TP_ 1880
#define TG_ (-1000000)
#define PACE(pt) pace_wait(due, TP_, TG_)
#define TPX TP_
constexpr int AUX_SC1 = 16;
__device__ __forceinline__ void st_unit(__amdgpu_buffer_rsrc_t rs, int off, float v0, float v1, unsigned ta, unsigned tb) {
  __builtin_amdgcn_raw_buffer_store_b128(v4u{__float_as_uint(v0), ta, __float_as_uint(v1), tb}, rs, off, 0, AUX_SC1);
}
__device__ __forceinline__ v4u ld_unit(__amdgpu_buffer_rsrc_t rs, int off) { return __builtin_amdgcn_raw_buffer_load_b128(rs, off, 0, AUX_SC1); }

struct Args {
  const unsigned short* x; const unsigned short* k; const unsigned short* v; const unsigned short* wq; const unsigned short* wo;
  const unsigned short* wg; const unsigned short* wu; const unsigned short* wd; unsigned short* out; unsigned char* ws;
};

__device__ __forceinline__ void ln_stats(v4u xr, float& mean, float& rstd) {
  float f[8];
#pragma unroll
  for (int i = 0; i < 4; ++i) { f[2 * i] = lo(xr[i]); f[2 * i + 1] = hi(xr[i]); }
  float s = 0.f;
#pragma unroll
  for (int i = 0; i < 8; ++i) s += f[i];
  mean = wave_sum(s) * (1.f / D);
  float q = 0.f;
#pragma unroll
  for (int i = 0; i < 8; ++i) { const float d = f[i] - mean; q = fmaf(d, d, q); }
  const float var = wave_sum(q) * (1.f / D);
  rstd = 1.f / sqrtf(var + 1e-5f);
}

__global__ void __launch_bounds__(NTHR, 2) k_all(Args a) {
  __shared__ __attribute__((aligned(16))) float lds[8 * 1024 + 8 * 132 + 8 * 128 + 8 * 128];
  float* ldsY = lds;
  float* ldsM = lds + 8 * 1024;
  float* ldsF = ldsM + 8 * 132;
  float* ldsQ = ldsF + 8 * 128;
  const unsigned tstart = (unsigned)__builtin_amdgcn_s_memrealtime();
  SB();
  STAMP(0);
  const int bid = blockIdx.x;
  const int xcd = bid & 7, j = bid >> 3;
  const int h = xcd * 4 + (j >> 3), b = (j >> 2) & 1, s = j & 3;
  const int tid = threadIdx.x, lane = tid & 63, w = __builtin_amdgcn_readfirstlane(tid >> 6);
  const int g = lane >> 4, c = lane & 15;
  unsigned due = (tstart << 4) + (unsigned)(w * TPX / 8);
  const int r8 = j & 7;
  const int fb = xcd * 3 + (j >> 3);
  const bool has_f1 = (j < 24) && (fb < 22);
  const bool is_fin = j >= 24;
  const int cid = xcd * 8 + (j - 24);
  const bool has_f2 = is_fin && cid < NFFN;
  const __amdgpu_buffer_rsrc_t rs = __builtin_amdgcn_make_buffer_rsrc((void*)a.ws, 0, WS_END, 0x00020000);

  const GAS v4u* xg = (const GAS v4u*)a.x;
  const v4u x0 = xg[lane], x1 = xg[64 + lane];
  const int row0 = 64 * r8 + 8 * w + 2 * g;
  const unsigned xs0 = *(const GAS unsigned*)(a.x + row0), xs1 = *(const GAS unsigned*)(a.x + D + row0);
  v4u sw[4];
  {
    const GAS v4u* p = (const GAS v4u*)(a.wq + (size_t)row0 * (H * DH) + h * DH + 8 * c);
    sw[0] = ldp<WNT_>(p); sw[1] = ldp<WNT_>(p + H * DH / 8);
    const int fcol = 128 * (fb < 11 ? fb : fb - 11) + 8 * c;
    const bool fv = has_f1 && fcol < F;
    const GAS v4u* pf = (const GAS v4u*)((fb < 11 ? a.wg : a.wu) + (fv ? row0 * F + fcol : 0));
    sw[2] = ldp<WNT_>(pf); sw[3] = ldp<WNT_>(pf + (fv ? F / 8 : 0));
  }
  const unsigned short xo = ((const GAS unsigned short*)a.x)[((cid >> 5) & 1) * D + (cid & 31) * 16 + c];
  const unsigned long long ep = __hip_atomic_load((const GAS unsigned long long*)(a.ws + WS_EPOCH), __ATOMIC_RELAXED, __HIP_MEMORY_SCOPE_AGENT);
  const size_t kvbase = ((size_t)(b * H + h) * SK + (size_t)s * KEYS_PER_WG) * DH;
  const GAS v4u* kp = (const GAS v4u*)(a.k + kvbase) + (size_t)w * (U * 64) + lane;
  const GAS v4u* vp = (const GAS v4u*)(a.v + kvbase) + (size_t)w * (U * 64) + lane;
  constexpr int GSTR = NWAVE * U * 64;
  constexpr int NR = NG * U * 2;
  v4u kr[NG][U], vr[NG][U];
#define RING_LOAD(i_) do { const int n_ = (i_) / (2 * U), r_ = (i_) % (2 * U); \
    if (r_ < U) kr[n_][r_] = ldp<KNT_>(kp + n_ * GSTR + r_ * 64); else vr[n_][r_ - U] = ldp<VNT_>(vp + n_ * GSTR + (r_ - U) * 64); } while (0)
#define RING4(pt) do { PACE(pt); SB(); RING_LOAD(4 * (pt)); RING_LOAD(4 * (pt) + 1); RING_LOAD(4 * (pt) + 2); RING_LOAD(4 * (pt) + 3); SB(); } while (0)
  SB();
  RING4(0);
  STAMP(1);
  float mean0, rstd0, mean1, rstd1;
  ln_stats(x0, mean0, rstd0);
  ln_stats(x1, mean1, rstd1);
  const unsigned tagA = __builtin_amdgcn_readfirstlane((unsigned)ep) + 1u, tagB = __builtin_amdgcn_readfirstlane((unsigned)(ep >> 32)) + 0x9E3779B9u;
  {
    const float n00 = (lo(xs0) - mean0) * rstd0, n01 = (hi(xs0) - mean0) * rstd0, n10 = (lo(xs1) - mean1) * rstd1, n11 = (hi(xs1) - mean1) * rstd1;
    float t[32];
#pragma unroll
    for (int mt = 0; mt < 2; ++mt)
#pragma unroll
      for (int k = 0; k < 4; ++k) {
        const float al = lo(sw[2 * mt][k]), ah = hi(sw[2 * mt][k]), bl = lo(sw[2 * mt + 1][k]), bh = hi(sw[2 * mt + 1][k]);
        t[16 * mt + 2 * k] = fmaf(n01, bl, n00 * al); t[16 * mt + 2 * k + 1] = fmaf(n01, bh, n00 * ah);
        t[16 * mt + 8 + 2 * k] = fmaf(n11, bl, n10 * al); t[16 * mt + 8 + 2 * k + 1] = fmaf(n11, bh, n10 * ah);
      }
#pragma unroll
    for (int k = 0; k < 16; ++k) t[k] = fold32(t[k], t[16 + k]);
#pragma unroll
    for (int k = 0; k < 8; ++k) t[k] = fold16(t[k], t[8 + k]);
    *(f32x4*)(ldsY + w * 512 + g * 128 + 8 * c) = f32x4{t[0], t[1], t[2], t[3]};
    *(f32x4*)(ldsY + w * 512 + g * 128 + 8 * c + 4) = f32x4{t[4], t[5], t[6], t[7]};
  }
  SB();
  RING4(1);
  __syncthreads();
  if (tid < (has_f1 ? 256 : 128)) {
    f32x2 z = {0.f, 0.f};
#pragma unroll
    for (int ww = 0; ww < NWAVE; ++ww) z += *(const f32x2*)(ldsY + ww * 512 + 2 * tid);
    st_unit(rs, WS_S1 + ((h * 8 + r8) * 256 + tid) * 16, z[0], z[1], tagA, tagB);
  }
  SB();
  RING4(2);
  RING4(3);
  {
    const int qoff = WS_S1 + ((h * 8 + w) * 256 + b * 64 + lane) * 16;
    v4u qu = ld_unit(rs, qoff);
    if (!__all(((qu[1] ^ tagA) | (qu[3] ^ tagB)) == 0u)) {
#pragma unroll 1
      for (unsigned spin = 0; spin < SPIN_CAP; ++spin) {
        __builtin_amdgcn_s_sleep(POLL_SLEEP_);
        asm volatile("" ::: "memory");
        qu = ld_unit(rs, qoff);
        if (__all(((qu[1] ^ tagA) | (qu[3] ^ tagB)) == 0u)) break;
      }
    }
    *(f32x2*)(ldsQ + w * 128 + 2 * lane) = f32x2{__uint_as_float(qu[0]), __uint_as_float(qu[2])};
  }
  SB();
  if (NG > 2) RING4(4);
  STAMP(2);
  __syncthreads();
  unsigned qp[4];
  {
    f32x4 q0 = {0, 0, 0, 0}, q1 = {0, 0, 0, 0};
#pragma unroll
    for (int ww = 0; ww < NWAVE; ++ww) { q0 += *(const f32x4*)(ldsQ + ww * 128 + 8 * c); q1 += *(const f32x4*)(ldsQ + ww * 128 + 8 * c + 4); }
    qp[0] = pk_bf16(q0[0], q0[1]); qp[1] = pk_bf16(q0[2], q0[3]); qp[2] = pk_bf16(q1[0], q1[1]); qp[3] = pk_bf16(q1[2], q1[3]);
  }
  if (NG > 2) RING4(5);
  STAMP(3);

  constexpr float C2 = 0.08838834764831845f * 1.4426950408889634f;
  float m = -INFINITY, l = 0.f, acc[8];
#pragma unroll
  for (int k = 0; k < 8; ++k) acc[k] = 0.f;
  v4u wor[16];
  v4u ffw[4], fu;
  const int fdo = has_f2 ? (32 * cid + 4 * w) * D + 8 * lane : 0, fds = has_f2 ? D : 0;
  int fuoff;
  { const int fbs = (g >> 1) * 11 + (cid >> 2), hs = (fbs / 3) * 4 + fbs % 3;
    fuoff = has_f2 ? WS_S1 + ((hs * 8 + w) * 256 + (2 + (g & 1)) * 64 + 16 * (cid & 3) + c) * 16 : WS_S1; }
#define EXTRA(gi_, half_) do { const int pt_ = 2 * (gi_) + (half_); \
      if ((gi_) == S2L_) { ffw[2 * (half_)] = ldp<WNT_>((const GAS v4u*)(a.wd + fdo + (2 * (half_)) * fds)); ffw[2 * (half_) + 1] = ldp<WNT_>((const GAS v4u*)(a.wd + fdo + (2 * (half_) + 1) * fds)); \
        if (half_) fu = ld_unit(rs, fuoff); } \
      _Pragma("unroll") for (int r_ = 0; r_ < WPP_; ++r_) { const int wi_ = (pt_ - WO0_) * WPP_ + r_; if (wi_ >= 0 && wi_ < 16) wor[wi_] = ldp<WNT_>(wop + wi_ * 64); } } while (0)
  const int bc = (cid >> 5) & 1, cu0 = (cid & 31) * 8;
  const int gp = tid >> 2, qd = tid & 3;
  const int dpoff = WS_DP + ((gp < NFFN ? gp : 0) * 512 + bc * 256 + cu0 + 2 * qd) * 16;
  v4u e0, e1;
  const GAS v4u* wop = (const GAS v4u*)(a.wo + (size_t)(h * DH + 16 * w) * D) + lane;
#pragma unroll
  for (int gi = 0; gi < GROUPS; ++gi) {
    const int n = gi % NG;
    STAMP(8 + gi);
    float sc[U];
#pragma unroll
    for (int u = 0; u < U; ++u) {
      float d = dot2(kr[n][u][0], qp[0], 0.f);
      d = dot2(kr[n][u][1], qp[1], d);
      d = dot2(kr[n][u][2], qp[2], d);
      d = dot2(kr[n][u][3], qp[3], d);
      sc[u] = d;
    }
    asm volatile("" : "+v"(sc[0]), "+v"(sc[1]), "+v"(sc[2]), "+v"(sc[3]));
    SB();
    if (gi + NG < GROUPS) {
      PACE(2 * (gi + NG)); SB();
#pragma unroll
      for (int u = 0; u < U; ++u) kr[n][u] = ldp<KNT_>(kp + (gi + NG) * GSTR + u * 64);
    }
    EXTRA(gi, 0);
    SB();
#pragma unroll
    for (int u = 0; u < U; ++u) sc[u] = row16_sum(sc[u]) * C2;
    float mx = m;
#pragma unroll
    for (int u = 0; u < U; ++u) mx = fmaxf(mx, sc[u]);
    const float alpha = __builtin_amdgcn_exp2f(m - mx);
    m = mx;
    float p[U], ps = 0.f;
#pragma unroll
    for (int u = 0; u < U; ++u) { p[u] = __builtin_amdgcn_exp2f(sc[u] - mx); ps += p[u]; }
    l = fmaf(l, alpha, ps);
#pragma unroll
    for (int k = 0; k < 8; ++k) acc[k] *= alpha;
#pragma unroll
    for (int u = 0; u < U; ++u) {
#pragma unroll
      for (int k = 0; k < 4; ++k) { acc[2 * k] = fmaf(p[u], lo(vr[n][u][k]), acc[2 * k]); acc[2 * k + 1] = fmaf(p[u], hi(vr[n][u][k]), acc[2 * k + 1]); }
    }
    asm volatile("" : "+v"(acc[0]), "+v"(acc[1]), "+v"(acc[2]), "+v"(acc[3]), "+v"(acc[4]), "+v"(acc[5]), "+v"(acc[6]), "+v"(acc[7]), "+v"(l), "+v"(m));
    SB();
    if (gi + NG < GROUPS) {
      PACE(2 * (gi + NG) + 1); SB();
#pragma unroll
      for (int u = 0; u < U; ++u) vr[n][u] = ldp<VNT_>(vp + (gi + NG) * GSTR + u * 64);
    }
    EXTRA(gi, 1);
    if (gi == DPG_) { e0 = ld_unit(rs, is_fin ? dpoff : WS_DP); e1 = ld_unit(rs, is_fin ? dpoff + 16 : WS_DP); }
    SB();
    if (gi == WOP_) {
#pragma unroll
      for (int i = 0; i < 8; ++i) {
        const v4u r0 = wor[2 * i], r1 = wor[2 * i + 1];
        v4u e, o;
#pragma unroll
        for (int k = 0; k < 2; ++k) {
          e[2 * k] = __builtin_amdgcn_perm(r1[k], r0[k], 0x05040100u); e[2 * k + 1] = __builtin_amdgcn_perm(r1[k], r0[k], 0x07060302u);
          o[2 * k] = __builtin_amdgcn_perm(r1[2 + k], r0[2 + k], 0x05040100u); o[2 * k + 1] = __builtin_amdgcn_perm(r1[2 + k], r0[2 + k], 0x07060302u);
        }
        asm volatile("" : "+v"(e), "+v"(o));
        wor[2 * i] = e; wor[2 * i + 1] = o;
      }
      SB();
    }
    if (gi == FG_) {
      if (has_f2) {
        if (!__all(((fu[1] ^ tagA) | (fu[3] ^ tagB)) == 0u)) {
#pragma unroll 1
          for (unsigned spin = 0; spin < SPIN_CAP; ++spin) {
            __builtin_amdgcn_s_sleep(POLL_SLEEP_);
            asm volatile("" ::: "memory");
            fu = ld_unit(rs, fuoff);
            if (__all(((fu[1] ^ tagA) | (fu[3] ^ tagB)) == 0u)) break;
          }
        }
        *(f32x2*)(ldsF + w * 128 + g * 32 + 2 * c) = f32x2{__uint_as_float(fu[0]), __uint_as_float(fu[2])};
        __syncthreads();
        float gs = 0.f, us = 0.f;
        const int hx = ((lane >> 2) & 1) * 32 + 4 * w + (lane & 3);
#pragma unroll
        for (int ww = 0; ww < NWAVE; ++ww) { gs += ldsF[ww * 128 + hx]; us += ldsF[ww * 128 + 64 + hx]; }
        const float hv = gelu_tanh(gs) * us;
        float dd[16];
#pragma unroll
        for (int k = 0; k < 16; ++k) dd[k] = 0.f;
#pragma unroll
        for (int r = 0; r < 4; ++r) {
          const float h0 = __builtin_bit_cast(float, __builtin_amdgcn_readlane(__builtin_bit_cast(int, hv), r));
          const float h1 = __builtin_bit_cast(float, __builtin_amdgcn_readlane(__builtin_bit_cast(int, hv), 4 + r));
#pragma unroll
          for (int k = 0; k < 4; ++k) {
            const float wl = lo(ffw[r][k]), wh = hi(ffw[r][k]);
            dd[2 * k] = fmaf(h0, wl, dd[2 * k]); dd[2 * k + 1] = fmaf(h0, wh, dd[2 * k + 1]);
            dd[8 + 2 * k] = fmaf(h1, wl, dd[8 + 2 * k]); dd[8 + 2 * k + 1] = fmaf(h1, wh, dd[8 + 2 * k + 1]);
          }
        }
        *(f32x4*)(ldsY + w * 1024 + 8 * lane) = f32x4{dd[0], dd[1], dd[2], dd[3]};
        *(f32x4*)(ldsY + w * 1024 + 8 * lane + 4) = f32x4{dd[4], dd[5], dd[6], dd[7]};
        *(f32x4*)(ldsY + w * 1024 + 512 + 8 * lane) = f32x4{dd[8], dd[9], dd[10], dd[11]};
        *(f32x4*)(ldsY + w * 1024 + 512 + 8 * lane + 4) = f32x4{dd[12], dd[13], dd[14], dd[15]};
        __syncthreads();
        f32x2 dv = {0.f, 0.f};
#pragma unroll
        for (int ww = 0; ww < NWAVE; ++ww) dv += *(const f32x2*)(ldsY + ww * 1024 + 2 * tid);
        st_unit(rs, WS_DP + (cid * 512 + tid) * 16, dv[0], dv[1], tagA, tagB);
      }
      SB();
    }
  }
  STAMP(4);

  {
    const float mw = xrow_max(m);
    const float sc = __builtin_amdgcn_exp2f(m - mw);
    l = xrow_sum(l * sc);
#pragma unroll
    for (int k = 0; k < 8; ++k) acc[k] *= sc;
    *(f32x2*)(ldsM + w * 132 + 4 + 8 * c + 2 * g) = fold8(acc);
    if (lane == 0) *(f32x2*)(ldsM + w * 132) = f32x2{mw, l};
  }
  __syncthreads();
  STAMP(5);
  const int pid = (b * H + h) * NSPLIT + s;
  {
    float M = -INFINITY, L = 0.f;
    f32x2 ml[8]; float av[8];
#pragma unroll
    for (int ww = 0; ww < NWAVE; ++ww) { ml[ww] = *(const f32x2*)(ldsM + ww * 132); av[ww] = ldsM[ww * 132 + 4 + 16 * w + c]; }
#pragma unroll
    for (int ww = 0; ww < NWAVE; ++ww) M = fmaxf(M, ml[ww][0]);
    float o = 0.f;
#pragma unroll
    for (int ww = 0; ww < NWAVE; ++ww) { const float e = __builtin_amdgcn_exp2f(ml[ww][0] - M); L = fmaf(e, ml[ww][1], L); o = fmaf(e, av[ww], o); }
    if (tid == 0) st_unit(rs, WS_ML + pid * 16, M, L, tagA, tagB);
    const unsigned op = pk_bf16(o, dpp<0xB1>(o));
    float y[8];
#pragma unroll
    for (int k = 0; k < 8; ++k) y[k] = 0.f;
#pragma unroll
    for (int i = 0; i < 8; ++i) {
      const unsigned oi = __builtin_amdgcn_readlane(op, 2 * i);
#pragma unroll
      for (int k = 0; k < 4; ++k) { y[k] = dot2(wor[2 * i][k], oi, y[k]); y[4 + k] = dot2(wor[2 * i + 1][k], oi, y[4 + k]); }
    }
    *(f32x4*)(ldsY + w * 512 + 8 * lane) = f32x4{y[0], y[1], y[2], y[3]};
    *(f32x4*)(ldsY + w * 512 + 8 * lane + 4) = f32x4{y[4], y[5], y[6], y[7]};
  }
  __syncthreads();
  if (tid < 256) {
    f32x2 y = {0.f, 0.f};
#pragma unroll
    for (int ww = 0; ww < NWAVE; ++ww) y += *(const f32x2*)(ldsY + ww * 512 + 2 * tid);
    st_unit(rs, WS_Y + (pid * 256 + tid) * 16, y[0], y[1], tagA, tagB);
  }
  STAMP(6);

  if (is_fin) {
    const int col0 = 2 * cu0;
    const int yoff = WS_Y + ((bc * 128 + gp) * 256 + cu0 + 2 * qd) * 16;
    const int mloff = WS_ML + (bc * 128 + (gp & ~3) + qd) * 16;
    const int otoff = WS_ML + ((1 - bc) * 128 + gp) * 16;
    v4u y0, y1, ml;
    unsigned spin = 0;
    bool ok = false;
#pragma unroll 1
    for (; spin < SPIN_CAP; ++spin) {
      asm volatile("" ::: "memory");
      if (!ok) {
        y0 = ld_unit(rs, yoff); y1 = ld_unit(rs, yoff + 16); ml = ld_unit(rs, mloff);
        const v4u ot = ld_unit(rs, cid == 0 ? otoff : mloff);
        ok = ((((y0[1] ^ tagA) | (y1[1] ^ tagA)) | ((ml[1] ^ tagA) | (ot[1] ^ tagA))) | (((y0[3] ^ tagB) | (y1[3] ^ tagB)) | ((ml[3] ^ tagB) | (ot[3] ^ tagB)))) == 0u;
      }
      if (__all(ok)) break;
      __builtin_amdgcn_s_sleep(POLL_SLEEP_);
    }
#pragma unroll 1
    for (; spin < SPIN_CAP; ++spin) {
      if (__all((((e0[1] ^ tagA) | (e1[1] ^ tagA)) | ((e0[3] ^ tagB) | (e1[3] ^ tagB))) == 0u)) break;
      __builtin_amdgcn_s_sleep(POLL_SLEEP_);
      asm volatile("" ::: "memory");
      e0 = ld_unit(rs, dpoff); e1 = ld_unit(rs, dpoff + 16);
    }
    const float mq = __uint_as_float(ml[0]), lq = __uint_as_float(ml[2]);
    float M = fmaxf(mq, dpp<0xB1>(mq)); M = fmaxf(M, dpp<0x4E>(M));
    const float e = __builtin_amdgcn_exp2f(mq - M);
    float L = e * lq; L += dpp<0xB1>(L); L += dpp<0x4E>(L);
    const float eb0 = dpp<0x00>(e), eb1 = dpp<0x55>(e), eb2 = dpp<0xAA>(e), eb3 = dpp<0xFF>(e);
    const int sp = gp & 3;
    const float wt = (sp == 0 ? eb0 : sp == 1 ? eb1 : sp == 2 ? eb2 : eb3) / L;
    const bool hd = gp < NFFN;
    float v[8];
    v[0] = wt * __uint_as_float(y0[0]); v[1] = wt * __uint_as_float(y0[2]); v[2] = wt * __uint_as_float(y1[0]); v[3] = wt * __uint_as_float(y1[2]);
    v[4] = hd ? __uint_as_float(e0[0]) : 0.f; v[5] = hd ? __uint_as_float(e0[2]) : 0.f; v[6] = hd ? __uint_as_float(e1[0]) : 0.f; v[7] = hd ? __uint_as_float(e1[2]) : 0.f;
    f32x2 r = fold8(v);
    r[0] += dpp<0x124>(r[0]); r[1] += dpp<0x124>(r[1]);
    r[0] += dpp<0x128>(r[0]); r[1] += dpp<0x128>(r[1]);
    if (c < 4) *(f32x2*)(ldsF + w * 32 + c * 8 + 2 * g) = r;
    __syncthreads();
    if (tid < 16) {
      float sv = 0.f, dv = 0.f;
#pragma unroll
      for (int ww = 0; ww < NWAVE; ++ww) { sv += ldsF[ww * 32 + (tid >> 2) * 8 + (tid & 3)]; dv += ldsF[ww * 32 + (tid >> 2) * 8 + 4 + (tid & 3)]; }
      const float gte = dv / (1.f + __expf(-dv));
      const float o = __uint_as_float((unsigned)xo << 16) + sv * gte;
      const __bf16 ob = (__bf16)o;
      ((GAS unsigned short*)a.out)[bc * D + col0 + tid] = __builtin_bit_cast(unsigned short, ob);
      if (cid == 0 && tid == 0)
        __hip_atomic_store((GAS unsigned long long*)(a.ws + WS_EPOCH), ((unsigned long long)tagB << 32) | tagA, __ATOMIC_RELAXED, __HIP_MEMORY_SCOPE_AGENT);
    }
  }
}

extern "C" void kernel_launch(void* const* d_in, const int* in_sizes, int n_in, void* d_out, int out_size, void* d_ws, size_t ws_size,
                              hipStream_t stream) {
  Args a;
  a.x = (const unsigned short*)d_in[0];
  a.k = (const unsigned short*)d_in[1];
  a.v = (const unsigned short*)d_in[2];
  a.wq = (const unsigned short*)d_in[3];
  a.wo = (const unsigned short*)d_in[4];
  a.wg = (const unsigned short*)d_in[6];
  a.wu = (const unsigned short*)d_in[7];
  a.wd = (const unsigned short*)d_in[8];
  a.out = (unsigned short*)d_out;
  a.ws = (unsigned char*)d_ws;
  hipLaunchKernelGGL(k_all, dim3(256), dim3(NTHR), 0, stream, a);
}
```

```cpp
#include <hip/hip_runtime.h>
#include <hip/hip_bf16.h>
#include <stdint.h>

typedef unsigned v4u __attribute__((ext_vector_type(4)));
typedef float f32x4 __attribute__((ext_vector_type(4)));
typedef float f32x2 __attribute__((ext_vector_type(2)));
typedef __bf16 bf16x2 __attribute__((ext_vector_type(2)));
#define GAS __attribute__((address_space(1)))
#define CAS __attribute__((address_space(4)))

constexpr int D = 512, H = 32, SK = 8192, DH = 128, F = 1376;
constexpr int NSPLIT = 4, KEYS_PER_WG = SK / NSPLIT;
constexpr int NWAVE = 8, NTHR = NWAVE * 64;
constexpr int NFFN = F / 32;
constexpr int U = 4;
constexpr int NG = 5;
constexpr int GROUPS = KEYS_PER_WG / NWAVE / (4 * U);
#define PFL_ 1
#define PWO_ 10
#define LATE_PRIO_ 400
#define XF1_ 54
#define XF2_ 12
#define XWO_ 8
#define KNT_ 1
#define VNT_ 1
#define WNT_ 0
#define TP_ 1800
#define T0_ 4800

constexpr int WS_EPOCH = 0;
constexpr int WS_ML = 4096;
constexpr int WS_Y = WS_ML + 256 * 16;
constexpr int WS_DP = WS_Y + 256 * 256 * 16;
constexpr int WS_S1 = WS_DP + NFFN * 512 * 16;
constexpr int WS_END = WS_S1 + 32 * 8 * 4 * 64 * 16;
constexpr int OOB = 0x7ffffff0;
constexpr unsigned SPIN_CAP = 400000;

#define SB() __builtin_amdgcn_sched_barrier(0)

__device__ __forceinline__ float dot2(unsigned k, unsigned q, float acc) {
  return __builtin_amdgcn_fdot2_f32_bf16(__builtin_bit_cast(bf16x2, k), __builtin_bit_cast(bf16x2, q), acc, false);
}
template <int CTRL> __device__ __forceinline__ float dpp(float x) {
  return __builtin_bit_cast(float, __builtin_amdgcn_mov_dpp(__builtin_bit_cast(int, x), CTRL, 0xf, 0xf, true));
}
__device__ __forceinline__ float row16_sum(float x) {
  x += dpp<0x128>(x);
  x += dpp<0x124>(x);
  x += dpp<0x122>(x);
  x += dpp<0x121>(x);
  return x;
}
__device__ __forceinline__ float fold32(float a, float b) {
  auto t = __builtin_amdgcn_permlane32_swap(__float_as_uint(a), __float_as_uint(b), false, false);
  return __uint_as_float(t[0]) + __uint_as_float(t[1]);
}
__device__ __forceinline__ float fold16(float a, float b) {
  auto t = __builtin_amdgcn_permlane16_swap(__float_as_uint(a), __float_as_uint(b), false, false);
  return __uint_as_float(t[0]) + __uint_as_float(t[1]);
}
__device__ __forceinline__ float xrow_sum(float x) { x = fold16(x, x); return fold32(x, x); }
__device__ __forceinline__ float xrow_max(float x) {
  auto s = __builtin_amdgcn_permlane16_swap(__float_as_uint(x), __float_as_uint(x), false, false);
  x = fmaxf(__uint_as_float(s[0]), __uint_as_float(s[1]));
  auto t = __builtin_amdgcn_permlane32_swap(__float_as_uint(x), __float_as_uint(x), false, false);
  return fmaxf(__uint_as_float(t[0]), __uint_as_float(t[1]));
}
__device__ __forceinline__ f32x2 fold8(const float (&v)[8]) {
  const float u0 = fold32(v[0], v[4]), u1 = fold32(v[1], v[5]), u2 = fold32(v[2], v[6]), u3 = fold32(v[3], v[7]);
  return f32x2{fold16(u0, u2), fold16(u1, u3)};
}
__device__ __forceinline__ float lo(unsigned u) { return __uint_as_float(u << 16); }
__device__ __forceinline__ float hi(unsigned u) { return __uint_as_float(u & 0xffff0000u); }
__device__ __forceinline__ unsigned pk_bf16(float a, float b) {
  bf16x2 r = {(__bf16)a, (__bf16)b};
  return __builtin_bit_cast(unsigned, r);
}
__device__ __forceinline__ unsigned sel4(v4u v, int g) { const unsigned a = (g & 1) ? v[1] : v[0], b = (g & 1) ? v[3] : v[2]; return (g & 2) ? b : a; }
template <int NT> __device__ __forceinline__ v4u ldp(const GAS v4u* p) { if (NT) return __builtin_nontemporal_load(p); else return *p; }
__device__ __forceinline__ float gelu_tanh(float x) {
  const float u = 0.7978845608028654f * (x + 0.044715f * x * x * x);
  const float e = __expf(2.f * u);
  const float t = 1.f - 2.f / (e + 1.f);
  return 0.5f * x * (1.f + t);
}
__device__ __forceinline__ void pace_wait(unsigned& due16, unsigned step16) {
  asm volatile("1:\n\ts_memrealtime s[90:91]\n\ts_waitcnt lgkmcnt(0)\n\ts_lshl_b32 s90, s90, 4\n\ts_sub_u32 s91, s90, %0\n\ts_cmp_ge_i32 s91, 0\n\ts_cbranch_scc1 2f\n\ts_sleep 2\n\ts_branch 1b\n"
               "2:\n\ts_add_u32 %0, %0, %1\n\ts_setprio 0\n\ts_cmp_lt_i32 s91, %2\n\ts_cbranch_scc1 3f\n\ts_setprio 2\n3:"
               : "+s"(due16) : "s"(step16), "i"(LATE_PRIO_) : "s90", "s91", "scc");
}
__device__ __forceinline__ unsigned now16() { return (unsigned)__builtin_amdgcn_s_memrealtime() << 4; }
#define MARK(i)
#define TPX TP_
#define T0X T0_
#define PACE(pt, x256) pace_wait(due, (unsigned)TPX * (256u + (x256)) / 256u)
constexpr int AUX_SC1 = 16;
__device__ __forceinline__ void st_unit(__amdgpu_buffer_rsrc_t rs, int off, float v0, float v1, unsigned ta, unsigned tb) {
  __builtin_amdgcn_raw_buffer_store_b128(v4u{__float_as_uint(v0), ta, __float_as_uint(v1), tb}, rs, off, 0, AUX_SC1);
}
__device__ __forceinline__ v4u ld_unit(__amdgpu_buffer_rsrc_t rs, int off) { return __builtin_amdgcn_raw_buffer_load_b128(rs, off, 0, AUX_SC1); }
__device__ __forceinline__ void await_unit(v4u& u, __amdgpu_buffer_rsrc_t rs, int off, unsigned ta, unsigned tb) {
  if (!__all(((u[1] ^ ta) | (u[3] ^ tb)) == 0u)) {
#pragma unroll 1
    for (unsigned spin = 0; spin < SPIN_CAP; ++spin) {
      __builtin_amdgcn_s_sleep(1);
      asm volatile("" ::: "memory");
      u = ld_unit(rs, off);
      if (__all(((u[1] ^ ta) | (u[3] ^ tb)) == 0u)) break;
    }
  }
}

__device__ __forceinline__ bool last_wave(unsigned* cnt, int lane) {
  asm volatile("s_waitcnt lgkmcnt(0)" ::: "memory");
  unsigned old = 0;
  if (lane == 0) old = __hip_atomic_fetch_add(cnt, 1u, __ATOMIC_RELAXED, __HIP_MEMORY_SCOPE_WORKGROUP);
  old = __builtin_amdgcn_readfirstlane(old);
  asm volatile("" ::: "memory");
  return old == NWAVE - 1;
}

__device__ __forceinline__ void wait_all(unsigned* cnt) {
  while (__builtin_amdgcn_readfirstlane(__hip_atomic_load(cnt, __ATOMIC_RELAXED, __HIP_MEMORY_SCOPE_WORKGROUP)) < NWAVE) __builtin_amdgcn_s_sleep(1);
  asm volatile("" ::: "memory");
}
template <int I> struct IC { static constexpr int value = I; };
template <int N, int I = 0, typename Fn> __device__ __forceinline__ void static_for(Fn& fn) { if constexpr (I < N) { fn(IC<I>{}); static_for<N, I + 1>(fn); } }

struct Args {
  const unsigned short* x; const unsigned short* wq;
  unsigned char* ws; const unsigned short* v; const unsigned short* wo; const unsigned short* wg;
  const unsigned short* wu; const unsigned short* wd; unsigned short* out; const unsigned short* k;
};

__global__ void __launch_bounds__(NTHR, 2) k_all(Args a) {
  __shared__ __attribute__((aligned(16))) float lds[8 * 1024 + 8 * 132 + 8 * 128 + 8 * 128 + 32 + 8];
  float* ldsY = lds;
  float* ldsM = lds + 8 * 1024;
  float* ldsF = ldsM + 8 * 132;
  float* ldsQ = ldsF + 8 * 128;
  float* ldsS = ldsQ + 8 * 128;
  unsigned* ldsC = (unsigned*)(ldsS + 32);
  const unsigned tstart = now16();
  MARK(32);
  const int bid = blockIdx.x;
  const int xcd = bid & 7, j = bid >> 3;
  const int h = xcd * 4 + (j >> 3), b = (j >> 2) & 1, s = j & 3;
  const int tid = threadIdx.x, lane = tid & 63, w = __builtin_amdgcn_readfirstlane(tid >> 6);
  const int g = lane >> 4, c = lane & 15;
  const unsigned long long tptr = ((const CAS unsigned long long*)__builtin_amdgcn_kernarg_segment_ptr())[2 + w];
  if (tid == 0) *(v4u*)ldsC = v4u{0u, 0u, 0u, 0u};
  asm volatile("s_waitcnt lgkmcnt(0)" :: "s"(tptr), "s"(a.k), "s"(a.x), "s"(a.wq), "s"(tstart) : "memory");
  const int r8 = j & 7;
  const int fb = xcd * 3 + (j >> 3);
  const bool has_f1 = (j < 24) && (fb < 22);
  const bool is_fin = j >= 24;
  const int cid = xcd * 8 + (j - 24);
  const bool has_f2 = is_fin && cid < NFFN;
  const __amdgpu_buffer_rsrc_t rs = __builtin_amdgcn_make_buffer_rsrc((void*)a.ws, 0, WS_END, 0x00020000);

  const int row0 = 64 * r8 + 8 * w + 2 * g;
  v4u sw0, sw1;
  {
    const GAS v4u* p = (const GAS v4u*)(a.wq + (size_t)row0 * (H * DH) + h * DH + 8 * c);
    sw0 = ldp<WNT_>(p); sw1 = ldp<WNT_>(p + H * DH / 8);
  }
  const unsigned xst = *(const GAS unsigned*)(a.x + (lane >> 5) * D + 64 * w + 2 * (lane & 31));
  const unsigned long long ep = __hip_atomic_load((const GAS unsigned long long*)(tptr + ((unsigned)(b * H + h) * ((0x82u >> w) & 1u) << 21) + ((unsigned)h * ((0x04u >> w) & 1u) << 17)),
                                                  __ATOMIC_RELAXED, __HIP_MEMORY_SCOPE_AGENT);
  const __amdgpu_buffer_rsrc_t rk = __builtin_amdgcn_make_buffer_rsrc((void*)(a.k + (size_t)(b * H + h) * SK * DH), 0, SK * DH * 2, 0x00020000);
  const __amdgpu_buffer_rsrc_t rv = __builtin_amdgcn_make_buffer_rsrc((void*)(a.v + (size_t)(b * H + h) * SK * DH), 0, SK * DH * 2, 0x00020000);
  const int kvo = ((w & 1) << 13) | ((w >> 1) << 15) | ((s & 1) << 12) | ((s >> 1) << 14), lo16 = lane * 16;
#define GOFF(gi_) ((((gi_) & 3) << 17) | (((gi_) & 4) ? (1 << 19) | (1 << 14) : 0) | (((gi_) & 8) ? (1 << 20) | (1 << 12) : 0))
  v4u kr[NG][U], vr[NG][U];
#define LDK(gi_, u_) kr[(gi_) % NG][u_] = __builtin_amdgcn_raw_buffer_load_b128(rk, lo16 + (u_) * 1024, kvo ^ GOFF(gi_), KNT_ ? 2 : 0)
#define LDV(gi_, u_) vr[(gi_) % NG][u_] = __builtin_amdgcn_raw_buffer_load_b128(rv, lo16 + (u_) * 1024, kvo ^ GOFF(gi_), VNT_ ? 2 : 0)
#define LOADK(gi_) do { SB(); LDK(gi_, 0); LDK(gi_, 1); LDK(gi_, 2); LDK(gi_, 3); SB(); } while (0)
#define LOADV(gi_) do { SB(); LDV(gi_, 0); LDV(gi_, 1); LDV(gi_, 2); LDV(gi_, 3); SB(); } while (0)
  v4u xq0, xq1;
  asm volatile("s_load_dwordx4 %0, %2, 0x0\n\ts_load_dwordx4 %1, %2, 0x400" : "=&s"(xq0), "=&s"(xq1) : "s"(a.x + 64 * r8 + 8 * w));
  SB();
  __builtin_amdgcn_s_barrier();
  LOADK(0);

  constexpr float C2 = 0.08838834764831845f * 1.4426950408889634f;
  float m = -INFINITY, l = 0.f, acc[8];
#pragma unroll
  for (int k = 0; k < 8; ++k) acc[k] = 0.f;
  unsigned tagA, tagB, qp[4];
  float mean0, rstd0, mean1, rstd1, n00, n01, n10, n11;
  v4u wor[16], ffw[4], fu, qu, e0, e1; unsigned short xo;
  asm volatile("s_waitcnt lgkmcnt(0)" : "+s"(xq0), "+s"(xq1));
  const unsigned xs0 = sel4(xq0, g), xs1 = sel4(xq1, g);
  const __amdgpu_buffer_rsrc_t rf = __builtin_amdgcn_make_buffer_rsrc((void*)(!has_f1 ? a.wd : fb < 11 ? a.wg : a.wu), 0, (has_f1 || has_f2) ? D * F * 2 : 0, 0x00020000);
  int fxo;
  { const int fcol = 128 * (fb < 11 ? fb : fb - 11) + 8 * c; fxo = !has_f1 ? (32 * cid + 4 * w) * D * 2 + lo16 : fcol < F ? (row0 * F + fcol) * 2 : OOB - 4 * F * 2; }
  int fuoff;
  { const int fbs = (g >> 1) * 11 + (cid >> 2), hs = (fbs / 3) * 4 + fbs % 3;
    fuoff = has_f2 ? WS_S1 + ((hs * 8 + (c >> 1)) * 256 + (2 + (g & 1)) * 64 + 16 * (cid & 3) + 2 * w + (c & 1)) * 16 : OOB; }
  const __amdgpu_buffer_rsrc_t rw = __builtin_amdgcn_make_buffer_rsrc((void*)(a.wo + (size_t)(h * DH + 16 * w) * D), 0, 16 * D * 2, 0x00020000);
  const int rot = (w + h) & 15;
  const int qoff = WS_S1 + ((h * 8 + w) * 256 + b * 64 + lane) * 16;
  const int bc = (cid >> 5) & 1, cu0 = (cid & 31) * 8;
  const int gp = tid >> 2, qd = tid & 3;
  const int dpoff = WS_DP + ((gp < NFFN ? gp : 0) * 512 + bc * 256 + cu0 + 2 * qd) * 16;

  auto stats = [&]() __attribute__((always_inline)) {
    f32x4 st[8];
#pragma unroll
    for (int i = 0; i < 8; ++i) st[i] = *(const f32x4*)(ldsS + 4 * i);
    tagA = __builtin_amdgcn_readfirstlane(ldsC[4]) + 1u; tagB = __builtin_amdgcn_readfirstlane(ldsC[5]) + 0x9E3779B9u;
    mean0 = ((st[0][0] + st[0][2]) + (st[1][0] + st[1][2]) + (st[2][0] + st[2][2]) + (st[3][0] + st[3][2])) * 0.125f;
    mean1 = ((st[4][0] + st[4][2]) + (st[5][0] + st[5][2]) + (st[6][0] + st[6][2]) + (st[7][0] + st[7][2])) * 0.125f;
    float q0 = 0.f, q1 = 0.f, d0 = 0.f, d1 = 0.f;
#pragma unroll
    for (int i = 0; i < 4; ++i) {
      q0 += st[i][1] + st[i][3]; q1 += st[4 + i][1] + st[4 + i][3];
      const float a0 = st[i][0] - mean0, b0 = st[i][2] - mean0, a1 = st[4 + i][0] - mean1, b1 = st[4 + i][2] - mean1;
      d0 += fmaf(a0, a0, b0 * b0); d1 += fmaf(a1, a1, b1 * b1);
    }
    rstd0 = 1.f / sqrtf(fmaf(64.f, d0, q0) * (1.f / D) + 1e-5f);
    rstd1 = 1.f / sqrtf(fmaf(64.f, d1, q1) * (1.f / D) + 1e-5f);
  };
  auto group = [&](auto gic) __attribute__((always_inline)) {
    constexpr int n = decltype(gic)::value % NG;
    float sc[U];
#pragma unroll
    for (int u = 0; u < U; ++u) {
      float d = dot2(kr[n][u][0], qp[0], 0.f);
      d = dot2(kr[n][u][1], qp[1], d);
      d = dot2(kr[n][u][2], qp[2], d);
      d = dot2(kr[n][u][3], qp[3], d);
      sc[u] = row16_sum(d) * C2;
    }
    float mx = m;
#pragma unroll
    for (int u = 0; u < U; ++u) mx = fmaxf(mx, sc[u]);
    const float alpha = __builtin_amdgcn_exp2f(m - mx);
    m = mx;
    float p[U], ps = 0.f;
#pragma unroll
    for (int u = 0; u < U; ++u) { p[u] = __builtin_amdgcn_exp2f(sc[u] - mx); ps += p[u]; }
    l = fmaf(l, alpha, ps);
#pragma unroll
    for (int k = 0; k < 8; ++k) acc[k] *= alpha;
    asm volatile("" : "+v"(acc[0]), "+v"(acc[1]), "+v"(acc[2]), "+v"(acc[3]), "+v"(acc[4]), "+v"(acc[5]), "+v"(acc[6]), "+v"(acc[7]), "+v"(l), "+v"(p[0]), "+v"(p[1]), "+v"(p[2]), "+v"(p[3]));
    SB();
#pragma unroll
    for (int u = 0; u < U; ++u) {
#pragma unroll
      for (int k = 0; k < 4; ++k) { acc[2 * k] = fmaf(p[u], lo(vr[n][u][k]), acc[2 * k]); acc[2 * k + 1] = fmaf(p[u], hi(vr[n][u][k]), acc[2 * k + 1]); }
    }
    asm volatile("" : "+v"(acc[0]), "+v"(acc[1]), "+v"(acc[2]), "+v"(acc[3]), "+v"(acc[4]), "+v"(acc[5]), "+v"(acc[6]), "+v"(acc[7]), "+v"(m));
    SB();
  };

  {
    float sm = row16_sum(lo(xst) + hi(xst)); sm = fold16(sm, sm) * (1.f / 64);
    const float d0 = lo(xst) - sm, d1 = hi(xst) - sm;
    float m2 = row16_sum(fmaf(d0, d0, d1 * d1)); m2 = fold16(m2, m2);
    if ((lane & 31) == 0) *(f32x2*)(ldsS + (lane >> 5) * 16 + 2 * w) = f32x2{sm, m2};
    float t[24];
#pragma unroll
    for (int k = 0; k < 4; ++k) {
      const float al = lo(sw0[k]), ah = hi(sw0[k]), bl = lo(sw1[k]), bh = hi(sw1[k]);
      t[2 * k] = fmaf(hi(xs0), bl, lo(xs0) * al); t[2 * k + 1] = fmaf(hi(xs0), bh, lo(xs0) * ah);
      t[8 + 2 * k] = fmaf(hi(xs1), bl, lo(xs1) * al); t[8 + 2 * k + 1] = fmaf(hi(xs1), bh, lo(xs1) * ah);
      t[16 + 2 * k] = al + bl; t[16 + 2 * k + 1] = ah + bh;
    }
#pragma unroll
    for (int k = 0; k < 8; ++k) t[k] = fold16(fold32(t[k], t[16 + k]), fold32(t[8 + k], t[16 + k]));
    *(f32x4*)(ldsY + w * 512 + g * 128 + 8 * c) = f32x4{t[0], t[1], t[2], t[3]};
    *(f32x4*)(ldsY + w * 512 + g * 128 + 8 * c + 4) = f32x4{t[4], t[5], t[6], t[7]};
    if (tid == 0) *(unsigned long long*)(ldsC + 4) = ep;
    asm volatile("" :: "v"(ep));
    if (last_wave(ldsC, lane)) {
      stats();
      f32x2 z0 = {0.f, 0.f}, z1 = {0.f, 0.f}, cs = {0.f, 0.f};
#pragma unroll
      for (int ww = 0; ww < NWAVE; ++ww) {
        z0 += *(const f32x2*)(ldsY + ww * 512 + 2 * lane); z1 += *(const f32x2*)(ldsY + ww * 512 + 128 + 2 * lane); cs += *(const f32x2*)(ldsY + ww * 512 + 256 + 2 * lane); }
      st_unit(rs, WS_S1 + ((h * 8 + r8) * 256 + lane) * 16, (z0[0] - mean0 * cs[0]) * rstd0, (z0[1] - mean0 * cs[1]) * rstd0, tagA, tagB);
      st_unit(rs, WS_S1 + ((h * 8 + r8) * 256 + 64 + lane) * 16, (z1[0] - mean1 * cs[0]) * rstd1, (z1[1] - mean1 * cs[1]) * rstd1, tagA, tagB);
    }
  }
  MARK(33);
  unsigned due = tstart + (unsigned)T0X + (unsigned)(w * TPX / 8);

  auto point = [&](auto pc) __attribute__((always_inline)) {
    constexpr int p = decltype(pc)::value + 1;
    constexpr int fi = p - PFL_, wi = p - PWO_;
    PACE(p, (fi == 0 || fi == 2 ? XF1_ : 0) + (fi >= 0 && fi < 4 ? XF2_ : 0) + (wi >= 0 && wi < 16 ? XWO_ : 0));
    if constexpr (p == 4) qu = ld_unit(rs, qoff);
    if constexpr (p & 1) LOADV(p / 2); else LOADK(p / 2);
    if constexpr (fi == 0 || fi == 2) ffw[fi] = __builtin_amdgcn_raw_buffer_load_b128(rf, fxo, fi * (has_f1 ? F : D * 2), WNT_ ? 2 : 0);
    if constexpr (fi == 1 || fi == 3) ffw[fi] = __builtin_amdgcn_raw_buffer_load_b128(rf, has_f1 ? OOB : fxo + fi * D * 2, 0, WNT_ ? 2 : 0);
    if constexpr (wi >= 0 && wi < 16) wor[wi] = __builtin_amdgcn_raw_buffer_load_b128(rw, lo16, (wi ^ rot) * 1024, WNT_ ? 2 : 0);
    if constexpr (p == 12) fu = ld_unit(rs, fuoff);
    if constexpr (p == 26) {
      e0 = ld_unit(rs, is_fin ? dpoff : OOB); e1 = ld_unit(rs, is_fin ? dpoff + 16 : OOB);
      xo = ((const GAS unsigned short*)a.x)[bc * D + 2 * cu0 + c];
    }
    SB();
    if constexpr (p == 2) {
      wait_all(ldsC);
      stats();
      n00 = (lo(xs0) - mean0) * rstd0; n01 = (hi(xs0) - mean0) * rstd0; n10 = (lo(xs1) - mean1) * rstd1; n11 = (hi(xs1) - mean1) * rstd1;
      asm volatile("" : "+v"(n00), "+v"(n01), "+v"(n10), "+v"(n11));
    }
    if constexpr (p == 6) {
      await_unit(qu, rs, qoff, tagA, tagB);
      *(f32x2*)(ldsQ + w * 128 + 2 * lane) = f32x2{__uint_as_float(qu[0]), __uint_as_float(qu[2])};
      last_wave(ldsC + 1, lane);
    }
    if constexpr (p == 5) {
      if (has_f1) {
        float t[8];
#pragma unroll
        for (int k = 0; k < 4; ++k) {
          const float al = lo(ffw[0][k]), ah = hi(ffw[0][k]), bl = lo(ffw[2][k]), bh = hi(ffw[2][k]);
          t[2 * k] = fold32(fmaf(n01, bl, n00 * al), fmaf(n11, bl, n10 * al)); t[2 * k + 1] = fold32(fmaf(n01, bh, n00 * ah), fmaf(n11, bh, n10 * ah));
        }
        *(f32x4*)(ldsY + 4096 + w * 256 + (g >> 1) * 128 + 8 * c + 4 * (g & 1)) = f32x4{fold16(t[0], t[4]), fold16(t[1], t[5]), fold16(t[2], t[6]), fold16(t[3], t[7])};
        last_wave(ldsC + 2, lane);
      }
    }
    if constexpr (p == 8) {
      wait_all(ldsC + 1);
      f32x4 q0 = {0, 0, 0, 0}, q1 = {0, 0, 0, 0};
#pragma unroll
      for (int ww = 0; ww < NWAVE; ++ww) { q0 += *(const f32x4*)(ldsQ + ww * 128 + 8 * c); q1 += *(const f32x4*)(ldsQ + ww * 128 + 8 * c + 4); }
      qp[0] = pk_bf16(q0[0], q0[1]); qp[1] = pk_bf16(q0[2], q0[3]); qp[2] = pk_bf16(q1[0], q1[1]); qp[3] = pk_bf16(q1[2], q1[3]);
      group(IC<0>{});
    }
    if constexpr (p == 9) { group(IC<1>{}); group(IC<2>{}); }
    if constexpr (p == 10) group(IC<3>{});
    if constexpr (p >= 11 && (p & 1)) group(IC<p / 2 - 1>{});
    if constexpr (p == 7) {
      if (has_f1 && w < 2) {
        wait_all(ldsC + 2);
        f32x2 z = {0.f, 0.f};
#pragma unroll
        for (int ww = 0; ww < NWAVE; ++ww) z += *(const f32x2*)(ldsY + 4096 + ww * 256 + 2 * tid);
        st_unit(rs, WS_S1 + ((h * 8 + r8) * 256 + 128 + tid) * 16, z[0], z[1], tagA, tagB);
      }
    }
    if constexpr (p == 14) {
      if (has_f2) {
        await_unit(fu, rs, fuoff, tagA, tagB);
        float s0 = __uint_as_float(fu[0]), s1 = __uint_as_float(fu[2]);
        s0 += dpp<0x128>(s0); s1 += dpp<0x128>(s1); s0 += dpp<0x124>(s0); s1 += dpp<0x124>(s1); s0 += dpp<0x122>(s0); s1 += dpp<0x122>(s1);
        const float sel = (c & 2) ? s1 : s0;
        const auto gu = __builtin_amdgcn_permlane32_swap(__float_as_uint(sel), __float_as_uint(sel), false, false);
        const float hv = gelu_tanh(__uint_as_float(gu[0])) * __uint_as_float(gu[1]);
        float dd[16];
#pragma unroll
        for (int k = 0; k < 16; ++k) dd[k] = 0.f;
#pragma unroll
        for (int r = 0; r < 4; ++r) {
          const float h0 = __builtin_bit_cast(float, __builtin_amdgcn_readlane(__builtin_bit_cast(int, hv), ((r & 1) << 1) | (r >> 1)));
          const float h1 = __builtin_bit_cast(float, __builtin_amdgcn_readlane(__builtin_bit_cast(int, hv), 16 + (((r & 1) << 1) | (r >> 1))));
#pragma unroll
          for (int k = 0; k < 4; ++k) {
            const float wl = lo(ffw[r][k]), wh = hi(ffw[r][k]);
            dd[2 * k] = fmaf(h0, wl, dd[2 * k]); dd[2 * k + 1] = fmaf(h0, wh, dd[2 * k + 1]);
            dd[8 + 2 * k] = fmaf(h1, wl, dd[8 + 2 * k]); dd[8 + 2 * k + 1] = fmaf(h1, wh, dd[8 + 2 * k + 1]);
          }
        }
        *(f32x4*)(ldsY + w * 1024 + 8 * lane) = f32x4{dd[0], dd[1], dd[2], dd[3]};
        *(f32x4*)(ldsY + w * 1024 + 8 * lane + 4) = f32x4{dd[4], dd[5], dd[6], dd[7]};
        *(f32x4*)(ldsY + w * 1024 + 512 + 8 * lane) = f32x4{dd[8], dd[9], dd[10], dd[11]};
        *(f32x4*)(ldsY + w * 1024 + 512 + 8 * lane + 4) = f32x4{dd[12], dd[13], dd[14], dd[15]};
        last_wave(ldsC + 3, lane);
      }
    }
    if constexpr (p == 16) {
      if (has_f2) {
        wait_all(ldsC + 3);
        f32x2 dv = {0.f, 0.f};
#pragma unroll
        for (int ww = 0; ww < NWAVE; ++ww) dv += *(const f32x2*)(ldsY + ww * 1024 + 2 * tid);
        st_unit(rs, WS_DP + (cid * 512 + tid) * 16, dv[0], dv[1], tagA, tagB);
      }
    }
    if constexpr (p == PWO_ + 18) {
#pragma unroll
      for (int i = 0; i < 8; ++i) {
        const v4u r0 = wor[2 * i], r1 = wor[2 * i + 1];
        v4u e, o;
#pragma unroll
        for (int k = 0; k < 2; ++k) {
          e[2 * k] = __builtin_amdgcn_perm(r1[k], r0[k], 0x05040100u); e[2 * k + 1] = __builtin_amdgcn_perm(r1[k], r0[k], 0x07060302u);
          o[2 * k] = __builtin_amdgcn_perm(r1[2 + k], r0[2 + k], 0x05040100u); o[2 * k + 1] = __builtin_amdgcn_perm(r1[2 + k], r0[2 + k], 0x07060302u);
        }
        asm volatile("" : "+v"(e), "+v"(o));
        wor[2 * i] = e; wor[2 * i + 1] = o;
      }
    }
    SB();
  };
  static_for<2 * GROUPS - 1>(point);
  group(IC<GROUPS - 1>{});
  MARK(34);

  {
    const float mw = xrow_max(m);
    const float sc = __builtin_amdgcn_exp2f(m - mw);
    l = xrow_sum(l * sc);
#pragma unroll
    for (int k = 0; k < 8; ++k) acc[k] *= sc;
    *(f32x2*)(ldsM + w * 132 + 4 + 8 * c + 2 * g) = fold8(acc);
    if (lane == 0) *(f32x2*)(ldsM + w * 132) = f32x2{mw, l};
  }
  __syncthreads();
  MARK(35);
  const int pid = (b * H + h) * NSPLIT + s;
  {
    float M = -INFINITY, L = 0.f;
    f32x2 ml[8]; float av[8];
#pragma unroll
    for (int ww = 0; ww < NWAVE; ++ww) { ml[ww] = *(const f32x2*)(ldsM + ww * 132); av[ww] = ldsM[ww * 132 + 4 + 16 * w + (c ^ rot)]; }
#pragma unroll
    for (int ww = 0; ww < NWAVE; ++ww) M = fmaxf(M, ml[ww][0]);
    float o = 0.f;
#pragma unroll
    for (int ww = 0; ww < NWAVE; ++ww) { const float e = __builtin_amdgcn_exp2f(ml[ww][0] - M); L = fmaf(e, ml[ww][1], L); o = fmaf(e, av[ww], o); }
    if (tid == 448) st_unit(rs, WS_ML + pid * 16, M, L, tagA, tagB);
    const unsigned op = pk_bf16(o, dpp<0xB1>(o));
    float y[8];
#pragma unroll
    for (int k = 0; k < 8; ++k) y[k] = 0.f;
#pragma unroll
    for (int i = 0; i < 8; ++i) {
      const unsigned oi = __builtin_amdgcn_readlane(op, 2 * i);
#pragma unroll
      for (int k = 0; k < 4; ++k) { y[k] = dot2(wor[2 * i][k], oi, y[k]); y[4 + k] = dot2(wor[2 * i + 1][k], oi, y[4 + k]); }
    }
    *(f32x4*)(ldsY + w * 512 + 8 * lane) = f32x4{y[0], y[1], y[2], y[3]};
    *(f32x4*)(ldsY + w * 512 + 8 * lane + 4) = f32x4{y[4], y[5], y[6], y[7]};
  }
  __syncthreads();
  if (tid < 256) {
    f32x2 y = {0.f, 0.f};
#pragma unroll
    for (int ww = 0; ww < NWAVE; ++ww) y += *(const f32x2*)(ldsY + ww * 512 + 2 * tid);
    st_unit(rs, WS_Y + (pid * 256 + tid) * 16, y[0], y[1], tagA, tagB);
  }
  MARK(36);

  if (is_fin) {
    const int yoff = WS_Y + ((bc * 128 + gp) * 256 + cu0 + 2 * qd) * 16;
    const int mloff = WS_ML + (bc * 128 + (gp & ~3) + qd) * 16;
    const int otoff = WS_ML + ((1 - bc) * 128 + gp) * 16;
    await_unit(e0, rs, dpoff, tagA, tagB);
    await_unit(e1, rs, dpoff + 16, tagA, tagB);
    v4u y0, y1, ml;
    bool ok = false;
#pragma unroll 1
    for (unsigned spin = 0; spin < SPIN_CAP; ++spin) {
      asm volatile("" ::: "memory");
      if (!ok) {
        y0 = ld_unit(rs, yoff); y1 = ld_unit(rs, yoff + 16); ml = ld_unit(rs, mloff);
        const v4u ot = ld_unit(rs, cid == 0 ? otoff : mloff);
        ok = ((((y0[1] ^ tagA) | (y1[1] ^ tagA)) | ((ml[1] ^ tagA) | (ot[1] ^ tagA))) | (((y0[3] ^ tagB) | (y1[3] ^ tagB)) | ((ml[3] ^ tagB) | (ot[3] ^ tagB)))) == 0u;
      }
      if (__all(ok)) break;
      __builtin_amdgcn_s_sleep(1);
    }
    const float mq = __uint_as_float(ml[0]), lq = __uint_as_float(ml[2]);
    float M = fmaxf(mq, dpp<0xB1>(mq)); M = fmaxf(M, dpp<0x4E>(M));
    const float e = __builtin_amdgcn_exp2f(mq - M);
    float L = e * lq; L += dpp<0xB1>(L); L += dpp<0x4E>(L);
    const float eb0 = dpp<0x00>(e), eb1 = dpp<0x55>(e), eb2 = dpp<0xAA>(e), eb3 = dpp<0xFF>(e);
    const int sp = gp & 3;
    const float wt = (sp == 0 ? eb0 : sp == 1 ? eb1 : sp == 2 ? eb2 : eb3) / L;
    const bool hd = gp < NFFN;
    float v[8];
    v[0] = wt * __uint_as_float(y0[0]); v[1] = wt * __uint_as_float(y0[2]); v[2] = wt * __uint_as_float(y1[0]); v[3] = wt * __uint_as_float(y1[2]);
    v[4] = hd ? __uint_as_float(e0[0]) : 0.f; v[5] = hd ? __uint_as_float(e0[2]) : 0.f; v[6] = hd ? __uint_as_float(e1[0]) : 0.f; v[7] = hd ? __uint_as_float(e1[2]) : 0.f;
    f32x2 r = fold8(v);
    r[0] += dpp<0x124>(r[0]); r[1] += dpp<0x124>(r[1]);
    r[0] += dpp<0x128>(r[0]); r[1] += dpp<0x128>(r[1]);
    if (c < 4) *(f32x2*)(ldsF + w * 32 + c * 8 + 2 * g) = r;
    __syncthreads();
    if (tid < 16) {
      float sv = 0.f, dv = 0.f;
#pragma unroll
      for (int ww = 0; ww < NWAVE; ++ww) { sv += ldsF[ww * 32 + (tid >> 2) * 8 + (tid & 3)]; dv += ldsF[ww * 32 + (tid >> 2) * 8 + 4 + (tid & 3)]; }
      const float gte = dv / (1.f + __expf(-dv));
      const float o = __uint_as_float((unsigned)xo << 16) + sv * gte;
      const __bf16 ob = (__bf16)o;
      ((GAS unsigned short*)a.out)[bc * D + 2 * cu0 + tid] = __builtin_bit_cast(unsigned short, ob);
      if (cid == 0 && tid == 0)
        __hip_atomic_store((GAS unsigned long long*)(a.ws + WS_EPOCH), ((unsigned long long)tagB << 32) | tagA, __ATOMIC_RELAXED, __HIP_MEMORY_SCOPE_AGENT);
    }
  }
}

extern "C" void kernel_launch(void* const* d_in, const int* in_sizes, int n_in, void* d_out, int out_size, void* d_ws, size_t ws_size,
                              hipStream_t stream) {
  Args a;
  a.x = (const unsigned short*)d_in[0];
  a.k = (const unsigned short*)d_in[1];
  a.v = (const unsigned short*)d_in[2];
  a.wq = (const unsigned short*)d_in[3];
  a.wo = (const unsigned short*)d_in[4];
  a.wg = (const unsigned short*)d_in[6];
  a.wu = (const unsigned short*)d_in[7];
  a.wd = (const unsigned short*)d_in[8];
  a.out = (unsigned short*)d_out;
  a.ws = (unsigned char*)d_ws;
  hipLaunchKernelGGL(k_all, dim3(256), dim3(NTHR), 0, stream, a);
}
```

```cpp
#include <hip/hip_runtime.h>
#include <hip/hip_bf16.h>
#include <stdint.h>

typedef unsigned v4u __attribute__((ext_vector_type(4)));
typedef float f32x4 __attribute__((ext_vector_type(4)));
typedef float f32x2 __attribute__((ext_vector_type(2)));
typedef __bf16 bf16x2 __attribute__((ext_vector_type(2)));
#define GAS __attribute__((address_space(1)))
#define CAS __attribute__((address_space(4)))

constexpr int D = 512, H = 32, SK = 8192, DH = 128, F = 1376;
constexpr int NSPLIT = 4, KEYS_PER_WG = SK / NSPLIT;
constexpr int NWAVE = 8, NTHR = NWAVE * 64;
constexpr int NFFN = F / 32;
constexpr int U = 4;
constexpr int NG = 5;
constexpr int GROUPS = KEYS_PER_WG / NWAVE / (4 * U);
#define PFL_ 1
#define PWO_ 10
#define LATE_PRIO_ 400
#define XF1_ 54
#define XF2_ 12
#define XWO_ 8
#define KNT_ 1
#define VNT_ 1
#define WNT_ 0
#define TP_ 1800
#define T0_ 4800

constexpr int WS_EPOCH = 0;
constexpr int WS_ML = 4096;
constexpr int WS_Y = WS_ML + 256 * 16;
constexpr int WS_DP = WS_Y + 256 * 256 * 16;
constexpr int WS_S1 = WS_DP + NFFN * 512 * 16;
constexpr int WS_DBG = WS_S1 + 32 * 8 * 4 * 64 * 16;
constexpr int WS_END = WS_DBG + 256 * 16;
#define DBG_ 2
constexpr int OOB = 0x7ffffff0;
constexpr unsigned SPIN_CAP = 400000;

#define SB() __builtin_amdgcn_sched_barrier(0)

__device__ __forceinline__ float dot2(unsigned k, unsigned q, float acc) {
  return __builtin_amdgcn_fdot2_f32_bf16(__builtin_bit_cast(bf16x2, k), __builtin_bit_cast(bf16x2, q), acc, false);
}
template <int CTRL> __device__ __forceinline__ float dpp(float x) {
  return __builtin_bit_cast(float, __builtin_amdgcn_mov_dpp(__builtin_bit_cast(int, x), CTRL, 0xf, 0xf, true));
}
__device__ __forceinline__ float row16_sum(float x) {
  x += dpp<0x128>(x);
  x += dpp<0x124>(x);
  x += dpp<0x122>(x);
  x += dpp<0x121>(x);
  return x;
}
__device__ __forceinline__ float fold32(float a, float b) {
  auto t = __builtin_amdgcn_permlane32_swap(__float_as_uint(a), __float_as_uint(b), false, false);
  return __uint_as_float(t[0]) + __uint_as_float(t[1]);
}
__device__ __forceinline__ float fold16(float a, float b) {
  auto t = __builtin_amdgcn_permlane16_swap(__float_as_uint(a), __float_as_uint(b), false, false);
  return __uint_as_float(t[0]) + __uint_as_float(t[1]);
}
__device__ __forceinline__ float xrow_sum(float x) { x = fold16(x, x); return fold32(x, x); }
__device__ __forceinline__ float xrow_max(float x) {
  auto s = __builtin_amdgcn_permlane16_swap(__float_as_uint(x), __float_as_uint(x), false, false);
  x = fmaxf(__uint_as_float(s[0]), __uint_as_float(s[1]));
  auto t = __builtin_amdgcn_permlane32_swap(__float_as_uint(x), __float_as_uint(x), false, false);
  return fmaxf(__uint_as_float(t[0]), __uint_as_float(t[1]));
}
__device__ __forceinline__ f32x2 fold8(const float (&v)[8]) {
  const float u0 = fold32(v[0], v[4]), u1 = fold32(v[1], v[5]), u2 = fold32(v[2], v[6]), u3 = fold32(v[3], v[7]);
  return f32x2{fold16(u0, u2), fold16(u1, u3)};
}
__device__ __forceinline__ float lo(unsigned u) { return __uint_as_float(u << 16); }
__device__ __forceinline__ float hi(unsigned u) { return __uint_as_float(u & 0xffff0000u); }
__device__ __forceinline__ unsigned pk_bf16(float a, float b) {
  bf16x2 r = {(__bf16)a, (__bf16)b};
  return __builtin_bit_cast(unsigned, r);
}
__device__ __forceinline__ unsigned sel4(v4u v, int g) { const unsigned a = (g & 1) ? v[1] : v[0], b = (g & 1) ? v[3] : v[2]; return (g & 2) ? b : a; }
template <int NT> __device__ __forceinline__ v4u ldp(const GAS v4u* p) { if (NT) return __builtin_nontemporal_load(p); else return *p; }
__device__ __forceinline__ float gelu_tanh(float x) {
  const float u = 0.7978845608028654f * (x + 0.044715f * x * x * x);
  const float e = __expf(2.f * u);
  const float t = 1.f - 2.f / (e + 1.f);
  return 0.5f * x * (1.f + t);
}
__device__ __forceinline__ void pace_wait(unsigned& due16, unsigned step16) {
  asm volatile("1:\n\ts_memrealtime s[90:91]\n\ts_waitcnt lgkmcnt(0)\n\ts_lshl_b32 s90, s90, 4\n\ts_sub_u32 s91, s90, %0\n\ts_cmp_ge_i32 s91, 0\n\ts_cbranch_scc1 2f\n\ts_sleep 2\n\ts_branch 1b\n"
               "2:\n\ts_add_u32 %0, %0, %1\n\ts_setprio 0\n\ts_cmp_lt_i32 s91, %2\n\ts_cbranch_scc1 3f\n\ts_setprio 2\n3:"
               : "+s"(due16) : "s"(step16), "i"(LATE_PRIO_) : "s90", "s91", "scc");
}
__device__ __forceinline__ unsigned now16() { return (unsigned)__builtin_amdgcn_s_memrealtime() << 4; }
#define MARK(i)
#define TPX TP_
#define T0X T0_
#define PACE(pt, x256) pace_wait(due, (unsigned)TPX * (256u + (x256)) / 256u)
constexpr int AUX_SC1 = 16;
__device__ __forceinline__ void st_unit(__amdgpu_buffer_rsrc_t rs, int off, float v0, float v1, unsigned ta, unsigned tb) {
  __builtin_amdgcn_raw_buffer_store_b128(v4u{__float_as_uint(v0), ta, __float_as_uint(v1), tb}, rs, off, 0, AUX_SC1);
}
__device__ __forceinline__ v4u ld_unit(__amdgpu_buffer_rsrc_t rs, int off) { return __builtin_amdgcn_raw_buffer_load_b128(rs, off, 0, AUX_SC1); }
__device__ __forceinline__ void await_unit(v4u& u, __amdgpu_buffer_rsrc_t rs, int off, unsigned ta, unsigned tb) {
  if (!__all(((u[1] ^ ta) | (u[3] ^ tb)) == 0u)) {
#pragma unroll 1
    for (unsigned spin = 0; spin < SPIN_CAP; ++spin) {
      __builtin_amdgcn_s_sleep(1);
      asm volatile("" ::: "memory");
      u = ld_unit(rs, off);
      if (__all(((u[1] ^ ta) | (u[3] ^ tb)) == 0u)) break;
    }
  }
}

__device__ __forceinline__ bool last_wave(unsigned* cnt, int lane) {
  asm volatile("s_waitcnt lgkmcnt(0)" ::: "memory");
  unsigned old = 0;
  if (lane == 0) old = __hip_atomic_fetch_add(cnt, 1u, __ATOMIC_RELAXED, __HIP_MEMORY_SCOPE_WORKGROUP);
  old = __builtin_amdgcn_readfirstlane(old);
  asm volatile("" ::: "memory");
  return old == NWAVE - 1;
}

__device__ __forceinline__ void wait_all(unsigned* cnt) {
  while (__builtin_amdgcn_readfirstlane(__hip_atomic_load(cnt, __ATOMIC_RELAXED, __HIP_MEMORY_SCOPE_WORKGROUP)) < NWAVE) __builtin_amdgcn_s_sleep(1);
  asm volatile("" ::: "memory");
}
template <int I> struct IC { static constexpr int value = I; };
template <int N, int I = 0, typename Fn> __device__ __forceinline__ void static_for(Fn& fn) { if constexpr (I < N) { fn(IC<I>{}); static_for<N, I + 1>(fn); } }

struct Args {
  const unsigned short* x; const unsigned short* wq;
  unsigned char* ws; const unsigned short* v; const unsigned short* wo; const unsigned short* wg;
  const unsigned short* wu; const unsigned short* wd; unsigned short* out; const unsigned short* k;
};

__global__ void __launch_bounds__(NTHR, 2) k_all(Args a) {
  __shared__ __attribute__((aligned(16))) float lds[8 * 1024 + 8 * 132 + 8 * 128 + 8 * 128 + 32 + 8];
  float* ldsY = lds;
  float* ldsM = lds + 8 * 1024;
  float* ldsF = ldsM + 8 * 132;
  float* ldsQ = ldsF + 8 * 128;
  float* ldsS = ldsQ + 8 * 128;
  unsigned* ldsC = (unsigned*)(ldsS + 32);
  const unsigned tstart = now16();
  MARK(32);
  const int bid = blockIdx.x;
  const int xcd = bid & 7, j = bid >> 3;
  const int h = xcd * 4 + (j >> 3), b = (j >> 2) & 1, s = j & 3;
  const int tid = threadIdx.x, lane = tid & 63, w = __builtin_amdgcn_readfirstlane(tid >> 6);
  const int g = lane >> 4, c = lane & 15;
  const unsigned long long tptr = ((const CAS unsigned long long*)__builtin_amdgcn_kernarg_segment_ptr())[2 + w];
  if (tid == 0) *(v4u*)ldsC = v4u{0u, 0u, 0u, 0u};
  asm volatile("s_waitcnt lgkmcnt(0)" :: "s"(tptr), "s"(a.k), "s"(a.x), "s"(a.wq), "s"(tstart) : "memory");
  const int r8 = j & 7;
  const int fb = xcd * 3 + (j >> 3);
  const bool has_f1 = (j < 24) && (fb < 22);
  const bool is_fin = j >= 24;
  const int cid = xcd * 8 + (j - 24);
  const bool has_f2 = is_fin && cid < NFFN;
  const __amdgpu_buffer_rsrc_t rs = __builtin_amdgcn_make_buffer_rsrc((void*)a.ws, 0, WS_END, 0x00020000);

  const int row0 = 64 * r8 + 8 * w + 2 * g;
  v4u sw0, sw1;
  {
    const GAS v4u* p = (const GAS v4u*)(a.wq + (size_t)row0 * (H * DH) + h * DH + 8 * c);
    sw0 = ldp<WNT_>(p); sw1 = ldp<WNT_>(p + H * DH / 8);
  }
  const unsigned xst = *(const GAS unsigned*)(a.x + (lane >> 5) * D + 64 * w + 2 * (lane & 31));
  const unsigned long long ep = __hip_atomic_load((const GAS unsigned long long*)(tptr + ((unsigned)(b * H + h) * ((0x82u >> w) & 1u) << 21) + ((unsigned)h * ((0x04u >> w) & 1u) << 17)),
                                                  __ATOMIC_RELAXED, __HIP_MEMORY_SCOPE_AGENT);
  const __amdgpu_buffer_rsrc_t rk = __builtin_amdgcn_make_buffer_rsrc((void*)(a.k + (size_t)(b * H + h) * SK * DH), 0, SK * DH * 2, 0x00020000);
  const __amdgpu_buffer_rsrc_t rv = __builtin_amdgcn_make_buffer_rsrc((void*)(a.v + (size_t)(b * H + h) * SK * DH), 0, SK * DH * 2, 0x00020000);
  const int kvo = ((w & 1) << 13) | ((w >> 1) << 15) | ((s & 1) << 12) | ((s >> 1) << 14), lo16 = lane * 16;
#define GOFF(gi_) ((((gi_) & 3) << 17) | (((gi_) & 4) ? (1 << 19) | (1 << 14) : 0) | (((gi_) & 8) ? (1 << 20) | (1 << 12) : 0))
  v4u kr[NG][U], vr[NG][U];
#define LDK(gi_, u_) kr[(gi_) % NG][u_] = __builtin_amdgcn_raw_buffer_load_b128(rk, lo16 + (u_) * 1024, kvo ^ GOFF(gi_), KNT_ ? 2 : 0)
#define LDV(gi_, u_) vr[(gi_) % NG][u_] = __builtin_amdgcn_raw_buffer_load_b128(rv, lo16 + (u_) * 1024, kvo ^ GOFF(gi_), VNT_ ? 2 : 0)
#define LOADK(gi_) do { SB(); LDK(gi_, 0); LDK(gi_, 1); LDK(gi_, 2); LDK(gi_, 3); SB(); } while (0)
#define LOADV(gi_) do { SB(); LDV(gi_, 0); LDV(gi_, 1); LDV(gi_, 2); LDV(gi_, 3); SB(); } while (0)
  v4u xq0, xq1;
  asm volatile("s_load_dwordx4 %0, %2, 0x0\n\ts_load_dwordx4 %1, %2, 0x400" : "=&s"(xq0), "=&s"(xq1) : "s"(a.x + 64 * r8 + 8 * w));
  SB();
  __builtin_amdgcn_s_barrier();
  LOADK(0);

  constexpr float C2 = 0.08838834764831845f * 1.4426950408889634f;
  float m = -INFINITY, l = 0.f, acc[8];
#pragma unroll
  for (int k = 0; k < 8; ++k) acc[k] = 0.f;
  unsigned tagA, tagB, qp[4];
  float mean0, rstd0, mean1, rstd1, n00, n01, n10, n11;
  v4u wor[16], ffw[4], fu, qu, e0, e1; unsigned short xo;
  asm volatile("s_waitcnt lgkmcnt(0)" : "+s"(xq0), "+s"(xq1));
  const unsigned xs0 = sel4(xq0, g), xs1 = sel4(xq1, g);
  const __amdgpu_buffer_rsrc_t rf = __builtin_amdgcn_make_buffer_rsrc((void*)(!has_f1 ? a.wd : fb < 11 ? a.wg : a.wu), 0, (has_f1 || has_f2) ? D * F * 2 : 0, 0x00020000);
  int fxo;
  { const int fcol = 128 * (fb < 11 ? fb : fb - 11) + 8 * c; fxo = !has_f1 ? (32 * cid + 4 * w) * D * 2 + lo16 : fcol < F ? (row0 * F + fcol) * 2 : OOB - 4 * F * 2; }
  int fuoff;
  { const int fbs = (g >> 1) * 11 + (cid >> 2), hs = (fbs / 3) * 4 + fbs % 3;
    fuoff = has_f2 ? WS_S1 + ((hs * 8 + (c >> 1)) * 256 + (2 + (g & 1)) * 64 + 16 * (cid & 3) + 2 * w + (c & 1)) * 16 : OOB; }
  const __amdgpu_buffer_rsrc_t rw = __builtin_amdgcn_make_buffer_rsrc((void*)(a.wo + (size_t)(h * DH + 16 * w) * D), 0, 16 * D * 2, 0x00020000);
  const int rot = (w + h) & 15;
  const int qoff = WS_S1 + ((h * 8 + w) * 256 + b * 64 + lane) * 16;
  const int bc = (cid >> 5) & 1, cu0 = (cid & 31) * 8;
  const int gp = tid >> 2, qd = tid & 3;
  const int dpoff = WS_DP + ((gp < NFFN ? gp : 0) * 512 + bc * 256 + cu0 + 2 * qd) * 16;

  auto stats = [&]() __attribute__((always_inline)) {
    f32x4 st[8];
#pragma unroll
    for (int i = 0; i < 8; ++i) st[i] = *(const f32x4*)(ldsS + 4 * i);
    tagA = __builtin_amdgcn_readfirstlane(ldsC[4]) + 1u; tagB = __builtin_amdgcn_readfirstlane(ldsC[5]) + 0x9E3779B9u;
    mean0 = ((st[0][0] + st[0][2]) + (st[1][0] + st[1][2]) + (st[2][0] + st[2][2]) + (st[3][0] + st[3][2])) * 0.125f;
    mean1 = ((st[4][0] + st[4][2]) + (st[5][0] + st[5][2]) + (st[6][0] + st[6][2]) + (st[7][0] + st[7][2])) * 0.125f;
    float q0 = 0.f, q1 = 0.f, d0 = 0.f, d1 = 0.f;
#pragma unroll
    for (int i = 0; i < 4; ++i) {
      q0 += st[i][1] + st[i][3]; q1 += st[4 + i][1] + st[4 + i][3];
      const float a0 = st[i][0] - mean0, b0 = st[i][2] - mean0, a1 = st[4 + i][0] - mean1, b1 = st[4 + i][2] - mean1;
      d0 += fmaf(a0, a0, b0 * b0); d1 += fmaf(a1, a1, b1 * b1);
    }
    rstd0 = 1.f / sqrtf(fmaf(64.f, d0, q0) * (1.f / D) + 1e-5f);
    rstd1 = 1.f / sqrtf(fmaf(64.f, d1, q1) * (1.f / D) + 1e-5f);
  };
  auto group = [&](auto gic) __attribute__((always_inline)) {
    constexpr int n = decltype(gic)::value % NG;
    float sc[U];
#pragma unroll
    for (int u = 0; u < U; ++u) {
      float d = dot2(kr[n][u][0], qp[0], 0.f);
      d = dot2(kr[n][u][1], qp[1], d);
      d = dot2(kr[n][u][2], qp[2], d);
      d = dot2(kr[n][u][3], qp[3], d);
      sc[u] = row16_sum(d) * C2;
    }
    float mx = m;
#pragma unroll
    for (int u = 0; u < U; ++u) mx = fmaxf(mx, sc[u]);
    const float alpha = __builtin_amdgcn_exp2f(m - mx);
    m = mx;
    float p[U], ps = 0.f;
#pragma unroll
    for (int u = 0; u < U; ++u) { p[u] = __builtin_amdgcn_exp2f(sc[u] - mx); ps += p[u]; }
    l = fmaf(l, alpha, ps);
#pragma unroll
    for (int k = 0; k < 8; ++k) acc[k] *= alpha;
    asm volatile("" : "+v"(acc[0]), "+v"(acc[1]), "+v"(acc[2]), "+v"(acc[3]), "+v"(acc[4]), "+v"(acc[5]), "+v"(acc[6]), "+v"(acc[7]), "+v"(l), "+v"(p[0]), "+v"(p[1]), "+v"(p[2]), "+v"(p[3]));
    SB();
#pragma unroll
    for (int u = 0; u < U; ++u) {
#pragma unroll
      for (int k = 0; k < 4; ++k) { acc[2 * k] = fmaf(p[u], lo(vr[n][u][k]), acc[2 * k]); acc[2 * k + 1] = fmaf(p[u], hi(vr[n][u][k]), acc[2 * k + 1]); }
    }
    asm volatile("" : "+v"(acc[0]), "+v"(acc[1]), "+v"(acc[2]), "+v"(acc[3]), "+v"(acc[4]), "+v"(acc[5]), "+v"(acc[6]), "+v"(acc[7]), "+v"(m));
    SB();
  };

  {
    float sm = row16_sum(lo(xst) + hi(xst)); sm = fold16(sm, sm) * (1.f / 64);
    const float d0 = lo(xst) - sm, d1 = hi(xst) - sm;
    float m2 = row16_sum(fmaf(d0, d0, d1 * d1)); m2 = fold16(m2, m2);
    if ((lane & 31) == 0) *(f32x2*)(ldsS + (lane >> 5) * 16 + 2 * w) = f32x2{sm, m2};
    float t[24];
#pragma unroll
    for (int k = 0; k < 4; ++k) {
      const float al = lo(sw0[k]), ah = hi(sw0[k]), bl = lo(sw1[k]), bh = hi(sw1[k]);
      t[2 * k] = fmaf(hi(xs0), bl, lo(xs0) * al); t[2 * k + 1] = fmaf(hi(xs0), bh, lo(xs0) * ah);
      t[8 + 2 * k] = fmaf(hi(xs1), bl, lo(xs1) * al); t[8 + 2 * k + 1] = fmaf(hi(xs1), bh, lo(xs1) * ah);
      t[16 + 2 * k] = al + bl; t[16 + 2 * k + 1] = ah + bh;
    }
#pragma unroll
    for (int k = 0; k < 8; ++k) t[k] = fold16(fold32(t[k], t[16 + k]), fold32(t[8 + k], t[16 + k]));
    *(f32x4*)(ldsY + w * 512 + g * 128 + 8 * c) = f32x4{t[0], t[1], t[2], t[3]};
    *(f32x4*)(ldsY + w * 512 + g * 128 + 8 * c + 4) = f32x4{t[4], t[5], t[6], t[7]};
    if (tid == 0) *(unsigned long long*)(ldsC + 4) = ep;
    asm volatile("" :: "v"(ep));
    if (last_wave(ldsC, lane)) {
      stats();
      f32x2 z0 = {0.f, 0.f}, z1 = {0.f, 0.f}, cs = {0.f, 0.f};
#pragma unroll
      for (int ww = 0; ww < NWAVE; ++ww) {
        z0 += *(const f32x2*)(ldsY + ww * 512 + 2 * lane); z1 += *(const f32x2*)(ldsY + ww * 512 + 128 + 2 * lane); cs += *(const f32x2*)(ldsY + ww * 512 + 256 + 2 * lane); }
      st_unit(rs, WS_S1 + ((h * 8 + r8) * 256 + lane) * 16, (z0[0] - mean0 * cs[0]) * rstd0, (z0[1] - mean0 * cs[1]) * rstd0, tagA, tagB);
      st_unit(rs, WS_S1 + ((h * 8 + r8) * 256 + 64 + lane) * 16, (z1[0] - mean1 * cs[0]) * rstd1, (z1[1] - mean1 * cs[1]) * rstd1, tagA, tagB);
    }
  }
  MARK(33);
  unsigned due = tstart + (unsigned)T0X + (unsigned)(w * TPX / 8);

  auto point = [&](auto pc) __attribute__((always_inline)) {
    constexpr int p = decltype(pc)::value + 1;
    constexpr int fi = p - PFL_, wi = p - PWO_;
    PACE(p, (fi == 0 || fi == 2 ? XF1_ : 0) + (fi >= 0 && fi < 4 ? XF2_ : 0) + (wi >= 0 && wi < 16 ? XWO_ : 0));
    if constexpr (p == 4) qu = ld_unit(rs, qoff);
    if constexpr (p & 1) LOADV(p / 2); else LOADK(p / 2);
    if constexpr (fi == 0 || fi == 2) ffw[fi] = __builtin_amdgcn_raw_buffer_load_b128(rf, fxo, fi * (has_f1 ? F : D * 2), WNT_ ? 2 : 0);
    if constexpr (fi == 1 || fi == 3) ffw[fi] = __builtin_amdgcn_raw_buffer_load_b128(rf, has_f1 ? OOB : fxo + fi * D * 2, 0, WNT_ ? 2 : 0);
    if constexpr (wi >= 0 && wi < 16) wor[wi] = __builtin_amdgcn_raw_buffer_load_b128(rw, lo16, (wi ^ rot) * 1024, WNT_ ? 2 : 0);
    if constexpr (p == 12) fu = ld_unit(rs, fuoff);
    if constexpr (p == 26) {
      e0 = ld_unit(rs, is_fin ? dpoff : OOB); e1 = ld_unit(rs, is_fin ? dpoff + 16 : OOB);
      xo = ((const GAS unsigned short*)a.x)[bc * D + 2 * cu0 + c];
    }
    SB();
    if constexpr (p == 2) {
      wait_all(ldsC);
      stats();
      n00 = (lo(xs0) - mean0) * rstd0; n01 = (hi(xs0) - mean0) * rstd0; n10 = (lo(xs1) - mean1) * rstd1; n11 = (hi(xs1) - mean1) * rstd1;
      asm volatile("" : "+v"(n00), "+v"(n01), "+v"(n10), "+v"(n11));
    }
    if constexpr (p == 6) {
      await_unit(qu, rs, qoff, tagA, tagB);
      *(f32x2*)(ldsQ + w * 128 + 2 * lane) = f32x2{__uint_as_float(qu[0]), __uint_as_float(qu[2])};
      last_wave(ldsC + 1, lane);
    }
    if constexpr (p == 5) {
      if (has_f1) {
        float t[8];
#pragma unroll
        for (int k = 0; k < 4; ++k) {
          const float al = lo(ffw[0][k]), ah = hi(ffw[0][k]), bl = lo(ffw[2][k]), bh = hi(ffw[2][k]);
          t[2 * k] = fold32(fmaf(n01, bl, n00 * al), fmaf(n11, bl, n10 * al)); t[2 * k + 1] = fold32(fmaf(n01, bh, n00 * ah), fmaf(n11, bh, n10 * ah));
        }
        *(f32x4*)(ldsY + 4096 + w * 256 + (g >> 1) * 128 + 8 * c + 4 * (g & 1)) = f32x4{fold16(t[0], t[4]), fold16(t[1], t[5]), fold16(t[2], t[6]), fold16(t[3], t[7])};
        last_wave(ldsC + 2, lane);
      }
    }
    if constexpr (p == 8) {
      wait_all(ldsC + 1);
      f32x4 q0 = {0, 0, 0, 0}, q1 = {0, 0, 0, 0};
#pragma unroll
      for (int ww = 0; ww < NWAVE; ++ww) { q0 += *(const f32x4*)(ldsQ + ww * 128 + 8 * c); q1 += *(const f32x4*)(ldsQ + ww * 128 + 8 * c + 4); }
      qp[0] = pk_bf16(q0[0], q0[1]); qp[1] = pk_bf16(q0[2], q0[3]); qp[2] = pk_bf16(q1[0], q1[1]); qp[3] = pk_bf16(q1[2], q1[3]);
      group(IC<0>{});
    }
    if constexpr (p == 9) { group(IC<1>{}); group(IC<2>{}); }
    if constexpr (p == 10) group(IC<3>{});
    if constexpr (p >= 11 && (p & 1)) group(IC<p / 2 - 1>{});
    if constexpr (p == 7) {
      if (has_f1 && w < 2) {
        wait_all(ldsC + 2);
        f32x2 z = {0.f, 0.f};
#pragma unroll
        for (int ww = 0; ww < NWAVE; ++ww) z += *(const f32x2*)(ldsY + 4096 + ww * 256 + 2 * tid);
        st_unit(rs, WS_S1 + ((h * 8 + r8) * 256 + 128 + tid) * 16, z[0], z[1], tagA, tagB);
      }
    }
    if constexpr (p == 14) {
      if (has_f2) {
        await_unit(fu, rs, fuoff, tagA, tagB);
        float s0 = __uint_as_float(fu[0]), s1 = __uint_as_float(fu[2]);
        s0 += dpp<0x128>(s0); s1 += dpp<0x128>(s1); s0 += dpp<0x124>(s0); s1 += dpp<0x124>(s1); s0 += dpp<0x122>(s0); s1 += dpp<0x122>(s1);
        const float sel = (c & 2) ? s1 : s0;
        const auto gu = __builtin_amdgcn_permlane32_swap(__float_as_uint(sel), __float_as_uint(sel), false, false);
        const float hv = gelu_tanh(__uint_as_float(gu[0])) * __uint_as_float(gu[1]);
        float dd[16];
#pragma unroll
        for (int k = 0; k < 16; ++k) dd[k] = 0.f;
#pragma unroll
        for (int r = 0; r < 4; ++r) {
          const float h0 = __builtin_bit_cast(float, __builtin_amdgcn_readlane(__builtin_bit_cast(int, hv), ((r & 1) << 1) | (r >> 1)));
          const float h1 = __builtin_bit_cast(float, __builtin_amdgcn_readlane(__builtin_bit_cast(int, hv), 16 + (((r & 1) << 1) | (r >> 1))));
#pragma unroll
          for (int k = 0; k < 4; ++k) {
            const float wl = lo(ffw[r][k]), wh = hi(ffw[r][k]);
            dd[2 * k] = fmaf(h0, wl, dd[2 * k]); dd[2 * k + 1] = fmaf(h0, wh, dd[2 * k + 1]);
            dd[8 + 2 * k] = fmaf(h1, wl, dd[8 + 2 * k]); dd[8 + 2 * k + 1] = fmaf(h1, wh, dd[8 + 2 * k + 1]);
          }
        }
        *(f32x4*)(ldsY + w * 1024 + 8 * lane) = f32x4{dd[0], dd[1], dd[2], dd[3]};
        *(f32x4*)(ldsY + w * 1024 + 8 * lane + 4) = f32x4{dd[4], dd[5], dd[6], dd[7]};
        *(f32x4*)(ldsY + w * 1024 + 512 + 8 * lane) = f32x4{dd[8], dd[9], dd[10], dd[11]};
        *(f32x4*)(ldsY + w * 1024 + 512 + 8 * lane + 4) = f32x4{dd[12], dd[13], dd[14], dd[15]};
        last_wave(ldsC + 3, lane);
      }
    }
    if constexpr (p == 16) {
      if (has_f2) {
        wait_all(ldsC + 3);
        f32x2 dv = {0.f, 0.f};
#pragma unroll
        for (int ww = 0; ww < NWAVE; ++ww) dv += *(const f32x2*)(ldsY + ww * 1024 + 2 * tid);
        st_unit(rs, WS_DP + (cid * 512 + tid) * 16, dv[0], dv[1], tagA, tagB);
      }
    }
    if constexpr (p == PWO_ + 18) {
#pragma unroll
      for (int i = 0; i < 8; ++i) {
        const v4u r0 = wor[2 * i], r1 = wor[2 * i + 1];
        v4u e, o;
#pragma unroll
        for (int k = 0; k < 2; ++k) {
          e[2 * k] = __builtin_amdgcn_perm(r1[k], r0[k], 0x05040100u); e[2 * k + 1] = __builtin_amdgcn_perm(r1[k], r0[k], 0x07060302u);
          o[2 * k] = __builtin_amdgcn_perm(r1[2 + k], r0[2 + k], 0x05040100u); o[2 * k + 1] = __builtin_amdgcn_perm(r1[2 + k], r0[2 + k], 0x07060302u);
        }
        asm volatile("" : "+v"(e), "+v"(o));
        wor[2 * i] = e; wor[2 * i + 1] = o;
      }
    }
    SB();
  };
  static_for<2 * GROUPS - 1>(point);
  group(IC<GROUPS - 1>{});
  MARK(34);

  {
    const float mw = xrow_max(m);
    const float sc = __builtin_amdgcn_exp2f(m - mw);
    l = xrow_sum(l * sc);
#pragma unroll
    for (int k = 0; k < 8; ++k) acc[k] *= sc;
    *(f32x2*)(ldsM + w * 132 + 4 + 8 * c + 2 * g) = fold8(acc);
    if (lane == 0) *(f32x2*)(ldsM + w * 132) = f32x2{mw, l};
  }
  __syncthreads();
  MARK(35);
  const int pid = (b * H + h) * NSPLIT + s;
  {
    float M = -INFINITY, L = 0.f;
    f32x2 ml[8]; float av[8];
#pragma unroll
    for (int ww = 0; ww < NWAVE; ++ww) { ml[ww] = *(const f32x2*)(ldsM + ww * 132); av[ww] = ldsM[ww * 132 + 4 + 16 * w + (c ^ rot)]; }
#pragma unroll
    for (int ww = 0; ww < NWAVE; ++ww) M = fmaxf(M, ml[ww][0]);
    float o = 0.f;
#pragma unroll
    for (int ww = 0; ww < NWAVE; ++ww) { const float e = __builtin_amdgcn_exp2f(ml[ww][0] - M); L = fmaf(e, ml[ww][1], L); o = fmaf(e, av[ww], o); }
    if (tid == 448) st_unit(rs, WS_ML + pid * 16, M, L, tagA, tagB);
    if (DBG_ && tid == 0) st_unit(rs, WS_DBG + bid * 16, __uint_as_float(tstart), 0.f, tagA, tagB);
    const unsigned op = pk_bf16(o, dpp<0xB1>(o));
    float y[8];
#pragma unroll
    for (int k = 0; k < 8; ++k) y[k] = 0.f;
#pragma unroll
    for (int i = 0; i < 8; ++i) {
      const unsigned oi = __builtin_amdgcn_readlane(op, 2 * i);
#pragma unroll
      for (int k = 0; k < 4; ++k) { y[k] = dot2(wor[2 * i][k], oi, y[k]); y[4 + k] = dot2(wor[2 * i + 1][k], oi, y[4 + k]); }
    }
    *(f32x4*)(ldsY + w * 512 + 8 * lane) = f32x4{y[0], y[1], y[2], y[3]};
    *(f32x4*)(ldsY + w * 512 + 8 * lane + 4) = f32x4{y[4], y[5], y[6], y[7]};
  }
  __syncthreads();
  if (tid < 256) {
    f32x2 y = {0.f, 0.f};
#pragma unroll
    for (int ww = 0; ww < NWAVE; ++ww) y += *(const f32x2*)(ldsY + ww * 512 + 2 * tid);
    st_unit(rs, WS_Y + (pid * 256 + tid) * 16, y[0], y[1], tagA, tagB);
  }
  MARK(36);

  if (is_fin) {
    const int yoff = WS_Y + ((bc * 128 + gp) * 256 + cu0 + 2 * qd) * 16;
    const int mloff = WS_ML + (bc * 128 + (gp & ~3) + qd) * 16;
    const int otoff = WS_ML + ((1 - bc) * 128 + gp) * 16;
    await_unit(e0, rs, dpoff, tagA, tagB);
    await_unit(e1, rs, dpoff + 16, tagA, tagB);
    v4u y0, y1, ml;
    bool ok = false;
#pragma unroll 1
    for (unsigned spin = 0; spin < SPIN_CAP; ++spin) {
      asm volatile("" ::: "memory");
      if (!ok) {
        y0 = ld_unit(rs, yoff); y1 = ld_unit(rs, yoff + 16); ml = ld_unit(rs, mloff);
        const v4u ot = ld_unit(rs, cid == 0 ? otoff : mloff);
        ok = ((((y0[1] ^ tagA) | (y1[1] ^ tagA)) | ((ml[1] ^ tagA) | (ot[1] ^ tagA))) | (((y0[3] ^ tagB) | (y1[3] ^ tagB)) | ((ml[3] ^ tagB) | (ot[3] ^ tagB)))) == 0u;
      }
      if (__all(ok)) break;
      __builtin_amdgcn_s_sleep(1);
    }
    const float mq = __uint_as_float(ml[0]), lq = __uint_as_float(ml[2]);
    float M = fmaxf(mq, dpp<0xB1>(mq)); M = fmaxf(M, dpp<0x4E>(M));
    const float e = __builtin_amdgcn_exp2f(mq - M);
    float L = e * lq; L += dpp<0xB1>(L); L += dpp<0x4E>(L);
    const float eb0 = dpp<0x00>(e), eb1 = dpp<0x55>(e), eb2 = dpp<0xAA>(e), eb3 = dpp<0xFF>(e);
    const int sp = gp & 3;
    const float wt = (sp == 0 ? eb0 : sp == 1 ? eb1 : sp == 2 ? eb2 : eb3) / L;
    const bool hd = gp < NFFN;
    float v[8];
    v[0] = wt * __uint_as_float(y0[0]); v[1] = wt * __uint_as_float(y0[2]); v[2] = wt * __uint_as_float(y1[0]); v[3] = wt * __uint_as_float(y1[2]);
    v[4] = hd ? __uint_as_float(e0[0]) : 0.f; v[5] = hd ? __uint_as_float(e0[2]) : 0.f; v[6] = hd ? __uint_as_float(e1[0]) : 0.f; v[7] = hd ? __uint_as_float(e1[2]) : 0.f;
    f32x2 r = fold8(v);
    r[0] += dpp<0x124>(r[0]); r[1] += dpp<0x124>(r[1]);
    r[0] += dpp<0x128>(r[0]); r[1] += dpp<0x128>(r[1]);
    if (c < 4) *(f32x2*)(ldsF + w * 32 + c * 8 + 2 * g) = r;
    if (DBG_ && cid == 0 && w == 0) {
      int mn = 1 << 30, mx = -(1 << 30);
      for (int i = 0; i < 4; ++i) { v4u u = ld_unit(rs, WS_DBG + (i * 64 + lane) * 16); await_unit(u, rs, WS_DBG + (i * 64 + lane) * 16, tagA, tagB);
        const int d = (int)(u[0] - tstart); mn = min(mn, d); mx = max(mx, d); }
      for (int o = 1; o < 64; o <<= 1) { mn = min(mn, __shfl_xor(mn, o)); mx = max(mx, __shfl_xor(mx, o)); }
      unsigned d2 = __builtin_amdgcn_readfirstlane(tstart + (DBG_ == 1 ? mn : mx) + 100 * 1600);
      pace_wait(d2, 0);
    }
    __syncthreads();
    if (tid < 16) {
      float sv = 0.f, dv = 0.f;
#pragma unroll
      for (int ww = 0; ww < NWAVE; ++ww) { sv += ldsF[ww * 32 + (tid >> 2) * 8 + (tid & 3)]; dv += ldsF[ww * 32 + (tid >> 2) * 8 + 4 + (tid & 3)]; }
      const float gte = dv / (1.f + __expf(-dv));
      const float o = __uint_as_float((unsigned)xo << 16) + sv * gte;
      const __bf16 ob = (__bf16)o;
      ((GAS unsigned short*)a.out)[bc * D + 2 * cu0 + tid] = __builtin_bit_cast(unsigned short, ob);
      if (cid == 0 && tid == 0)
        __hip_atomic_store((GAS unsigned long long*)(a.ws + WS_EPOCH), ((unsigned long long)tagB << 32) | tagA, __ATOMIC_RELAXED, __HIP_MEMORY_SCOPE_AGENT);
    }
  }
}

extern "C" void kernel_launch(void* const* d_in, const int* in_sizes, int n_in, void* d_out, int out_size, void* d_ws, size_t ws_size,
                              hipStream_t stream) {
  Args a;
  a.x = (const unsigned short*)d_in[0];
  a.k = (const unsigned short*)d_in[1];
  a.v = (const unsigned short*)d_in[2];
  a.wq = (const unsigned short*)d_in[3];
  a.wo = (const unsigned short*)d_in[4];
  a.wg = (const unsigned short*)d_in[6];
  a.wu = (const unsigned short*)d_in[7];
  a.wd = (const unsigned short*)d_in[8];
  a.out = (unsigned short*)d_out;
  a.ws = (unsigned char*)d_ws;
  hipLaunchKernelGGL(k_all, dim3(256), dim3(NTHR), 0, stream, a);
}
```

```cpp
#include <hip/hip_runtime.h>
#include <hip/hip_bf16.h>
#include <stdint.h>

typedef unsigned v4u __attribute__((ext_vector_type(4)));
typedef float f32x4 __attribute__((ext_vector_type(4)));
typedef float f32x2 __attribute__((ext_vector_type(2)));
typedef __bf16 bf16x2 __attribute__((ext_vector_type(2)));
typedef __bf16 bf16x8 __attribute__((ext_vector_type(8)));
typedef unsigned v2u __attribute__((ext_vector_type(2)));
#define GAS __attribute__((address_space(1)))
#define CAS __attribute__((address_space(4)))

constexpr int D = 512, H = 32, SK = 8192, DH = 128, F = 1376;
constexpr int NSPLIT = 4, KEYS_PER_WG = SK / NSPLIT;
constexpr int NWAVE = 8, NTHR = NWAVE * 64;
constexpr int NFFN = F / 32;
constexpr int U = 4;
constexpr int NG = 5;
constexpr int GROUPS = KEYS_PER_WG / NWAVE / (4 * U);
#define PFL_ 1
#define PWO_ 10
#define PDLY_ 8
#define LATE_PRIO_ 400
#define XF1_ 54
#define XF2_ 12
#define XWO_ 8
#define KNT_ 1
#define VNT_ 1
#define WNT_ 1
#define TP_ 1800
#define T0_ 4000

constexpr int WS_EPOCH = 0;
constexpr int WS_ML = 4096;
constexpr int WS_Y = WS_ML + 256 * 16;
constexpr int WS_DP = WS_Y + 256 * 256 * 16;
constexpr int WS_S1 = WS_DP + NFFN * 512 * 16;
constexpr int WS_END = WS_S1 + 32 * 8 * 4 * 64 * 16;
constexpr int OOB = 0x7ffffff0;
constexpr unsigned SPIN_CAP = 400000;

#define SB() __builtin_amdgcn_sched_barrier(0)

__device__ __forceinline__ float dot2(unsigned k, unsigned q, float acc) {
  return __builtin_amdgcn_fdot2_f32_bf16(__builtin_bit_cast(bf16x2, k), __builtin_bit_cast(bf16x2, q), acc, false);
}
template <int CTRL> __device__ __forceinline__ float dpp(float x) {
  return __builtin_bit_cast(float, __builtin_amdgcn_mov_dpp(__builtin_bit_cast(int, x), CTRL, 0xf, 0xf, true));
}
__device__ __forceinline__ float row16_sum(float x) {
  x += dpp<0x128>(x);
  x += dpp<0x124>(x);
  x += dpp<0x122>(x);
  x += dpp<0x121>(x);
  return x;
}
__device__ __forceinline__ float fold32(float a, float b) {
  auto t = __builtin_amdgcn_permlane32_swap(__float_as_uint(a), __float_as_uint(b), false, false);
  return __uint_as_float(t[0]) + __uint_as_float(t[1]);
}
__device__ __forceinline__ float fold16(float a, float b) {
  auto t = __builtin_amdgcn_permlane16_swap(__float_as_uint(a), __float_as_uint(b), false, false);
  return __uint_as_float(t[0]) + __uint_as_float(t[1]);
}
__device__ __forceinline__ float xrow_sum(float x) { x = fold16(x, x); return fold32(x, x); }
__device__ __forceinline__ float xrow_max(float x) {
  auto s = __builtin_amdgcn_permlane16_swap(__float_as_uint(x), __float_as_uint(x), false, false);
  x = fmaxf(__uint_as_float(s[0]), __uint_as_float(s[1]));
  auto t = __builtin_amdgcn_permlane32_swap(__float_as_uint(x), __float_as_uint(x), false, false);
  return fmaxf(__uint_as_float(t[0]), __uint_as_float(t[1]));
}
__device__ __forceinline__ f32x2 fold8(const float (&v)[8]) {
  const float u0 = fold32(v[0], v[4]), u1 = fold32(v[1], v[5]), u2 = fold32(v[2], v[6]), u3 = fold32(v[3], v[7]);
  return f32x2{fold16(u0, u2), fold16(u1, u3)};
}
__device__ __forceinline__ float fold4(float v0, float v1, float v2, float v3) {
  float r = fold16(fold32(v0, v2), fold32(v1, v3));
  r += dpp<0x124>(r); r += dpp<0x128>(r);
  return r;
}
__device__ __forceinline__ float lo(unsigned u) { return __uint_as_float(u << 16); }
__device__ __forceinline__ float hi(unsigned u) { return __uint_as_float(u & 0xffff0000u); }
__device__ __forceinline__ unsigned pk_bf16(float a, float b) {
  bf16x2 r = {(__bf16)a, (__bf16)b};
  return __builtin_bit_cast(unsigned, r);
}
__device__ __forceinline__ unsigned sel4(v4u v, int g) { const unsigned a = (g & 1) ? v[1] : v[0], b = (g & 1) ? v[3] : v[2]; return (g & 2) ? b : a; }
__device__ __forceinline__ int ucol(int u) { return 64 * (u >> 5) + 4 * (u & 15) + 2 * ((u >> 4) & 1); }
template <int NT> __device__ __forceinline__ v4u ldp(const GAS v4u* p) { if (NT) return __builtin_nontemporal_load(p); else return *p; }
__device__ __forceinline__ float gelu_tanh(float x) {
  const float u = 0.7978845608028654f * (x + 0.044715f * x * x * x);
  const float e = __expf(2.f * u);
  const float t = 1.f - 2.f / (e + 1.f);
  return 0.5f * x * (1.f + t);
}
__device__ __forceinline__ void pace_wait(unsigned& due16, unsigned step16) {
  asm volatile("1:\n\ts_memrealtime s[90:91]\n\ts_waitcnt lgkmcnt(0)\n\ts_lshl_b32 s90, s90, 4\n\ts_sub_u32 s91, s90, %0\n\ts_cmp_ge_i32 s91, 0\n\ts_cbranch_scc1 2f\n\ts_sleep 2\n\ts_branch 1b\n"
               "2:\n\ts_add_u32 %0, %0, %1\n\ts_setprio 0\n\ts_cmp_lt_i32 s91, %2\n\ts_cbranch_scc1 3f\n\ts_setprio 2\n3:"
               : "+s"(due16) : "s"(step16), "i"(LATE_PRIO_) : "s90", "s91", "scc");
}
__device__ __forceinline__ unsigned now16() { return (unsigned)__builtin_amdgcn_s_memrealtime() << 4; }
#define MARK(i)
#define TPX TP_
#define T0X T0_
#define PACE(pt, x256) pace_wait(due, (unsigned)TPX * (256u + (x256)) / 256u)
constexpr int AUX_SC1 = 16;
__device__ __forceinline__ void st_unit(__amdgpu_buffer_rsrc_t rs, int off, float v0, float v1, unsigned ta, unsigned tb) {
  __builtin_amdgcn_raw_buffer_store_b128(v4u{__float_as_uint(v0), ta, __float_as_uint(v1), tb}, rs, off, 0, AUX_SC1);
}
__device__ __forceinline__ v4u ld_unit(__amdgpu_buffer_rsrc_t rs, int off) { return __builtin_amdgcn_raw_buffer_load_b128(rs, off, 0, AUX_SC1); }
__device__ __forceinline__ void await_unit(v4u& u, __amdgpu_buffer_rsrc_t rs, int off, unsigned ta, unsigned tb) {
  if (!__all(((u[1] ^ ta) | (u[3] ^ tb)) == 0u)) {
#pragma unroll 1
    for (unsigned spin = 0; spin < SPIN_CAP; ++spin) {
      __builtin_amdgcn_s_sleep(1);
      asm volatile("" ::: "memory");
      u = ld_unit(rs, off);
      if (__all(((u[1] ^ ta) | (u[3] ^ tb)) == 0u)) break;
    }
  }
}

__device__ __forceinline__ bool last_wave(unsigned* cnt, int lane) {
  asm volatile("s_waitcnt lgkmcnt(0)" ::: "memory");
  unsigned old = 0;
  if (lane == 0) old = __hip_atomic_fetch_add(cnt, 1u, __ATOMIC_RELAXED, __HIP_MEMORY_SCOPE_WORKGROUP);
  old = __builtin_amdgcn_readfirstlane(old);
  asm volatile("" ::: "memory");
  return old == NWAVE - 1;
}

__device__ __forceinline__ void wait_all(unsigned* cnt) {
  while (__builtin_amdgcn_readfirstlane(__hip_atomic_load(cnt, __ATOMIC_RELAXED, __HIP_MEMORY_SCOPE_WORKGROUP)) < NWAVE) __builtin_amdgcn_s_sleep(1);
  asm volatile("" ::: "memory");
}
template <int I> struct IC { static constexpr int value = I; };
template <int N, int I = 0, typename Fn> __device__ __forceinline__ void static_for(Fn& fn) { if constexpr (I < N) { fn(IC<I>{}); static_for<N, I + 1>(fn); } }

struct Args {
  const unsigned short* x; const unsigned short* wq;
  unsigned char* ws; const unsigned short* v; const unsigned short* wo; const unsigned short* wg;
  const unsigned short* wu; const unsigned short* wd; unsigned short* out; const unsigned short* k;
};

__global__ void __launch_bounds__(NTHR, 2) k_all(Args a) {
  __shared__ __attribute__((aligned(16))) float lds[8 * 1024 + 16 * 68 + 16 + 8 * 128 + 8 * 128 + 32 + 8];
  float* ldsY = lds;
  unsigned* ldsA = (unsigned*)(lds + 8 * 1024);
  float* ldsM = lds + 8 * 1024 + 16 * 68;
  float* ldsF = ldsM + 16;
  float* ldsQ = ldsF + 8 * 128;
  float* ldsS = ldsQ + 8 * 128;
  unsigned* ldsC = (unsigned*)(ldsS + 32);
  const unsigned tstart = now16();
  MARK(32);
  const int bid = blockIdx.x;
  const int xcd = bid & 7, j = bid >> 3;
  const int h = xcd * 4 + (j >> 3), b = (j >> 2) & 1, s = j & 3;
  const int tid = threadIdx.x, lane = tid & 63, w = __builtin_amdgcn_readfirstlane(tid >> 6);
  const int g = lane >> 4, c = lane & 15;
  const unsigned long long tptr = ((const CAS unsigned long long*)__builtin_amdgcn_kernarg_segment_ptr())[2 + w];
  if (tid == 0) *(v4u*)ldsC = v4u{0u, 0u, 0u, 0u};
  asm volatile("s_waitcnt lgkmcnt(0)" :: "s"(tptr), "s"(a.k), "s"(a.x), "s"(a.wq), "s"(tstart) : "memory");
  const int r8 = j & 7;
  const int fb = xcd * 3 + (j >> 3);
  const bool has_f1 = (j < 24) && (fb < 22);
  const bool is_fin = j >= 24;
  const int cid = xcd * 8 + (j - 24);
  const bool has_f2 = is_fin && cid < NFFN;
  const __amdgpu_buffer_rsrc_t rs = __builtin_amdgcn_make_buffer_rsrc((void*)a.ws, 0, WS_END, 0x00020000);

  const int row0 = 64 * r8 + 8 * w + 2 * g;
  v4u sw0, sw1;
  {
    const GAS v4u* p = (const GAS v4u*)(a.wq + (size_t)row0 * (H * DH) + h * DH + 8 * c);
    sw0 = ldp<WNT_>(p); sw1 = ldp<WNT_>(p + H * DH / 8);
  }
  const unsigned xst = *(const GAS unsigned*)(a.x + (lane >> 5) * D + 64 * w + 2 * (lane & 31));
  const unsigned long long ep = __hip_atomic_load((const GAS unsigned long long*)(tptr + ((unsigned)(b * H + h) * ((0x82u >> w) & 1u) << 21) + ((unsigned)h * ((0x04u >> w) & 1u) << 17)),
                                                  __ATOMIC_RELAXED, __HIP_MEMORY_SCOPE_AGENT);
  const __amdgpu_buffer_rsrc_t rk = __builtin_amdgcn_make_buffer_rsrc((void*)(a.k + (size_t)(b * H + h) * SK * DH), 0, SK * DH * 2, 0x00020000);
  const __amdgpu_buffer_rsrc_t rv = __builtin_amdgcn_make_buffer_rsrc((void*)(a.v + (size_t)(b * H + h) * SK * DH), 0, SK * DH * 2, 0x00020000);
  const int kvo = ((w & 1) << 13) | ((w >> 1) << 15) | ((s & 1) << 12) | ((s >> 1) << 14), lo16 = lane * 16;
#define GOFF(gi_) ((((gi_) & 3) << 17) | (((gi_) & 4) ? (1 << 19) | (1 << 14) : 0) | (((gi_) & 8) ? (1 << 20) | (1 << 12) : 0))
  v4u kr[NG][U], vr[NG][U];
#define LDK(gi_, u_) kr[(gi_) % NG][u_] = __builtin_amdgcn_raw_buffer_load_b128(rk, lo16 + (u_) * 1024, kvo ^ GOFF(gi_), KNT_ ? 2 : 0)
#define LDV(gi_, u_) vr[(gi_) % NG][u_] = __builtin_amdgcn_raw_buffer_load_b128(rv, lo16 + (u_) * 1024, kvo ^ GOFF(gi_), VNT_ ? 2 : 0)
#define LOADK(gi_) do { SB(); LDK(gi_, 0); LDK(gi_, 1); LDK(gi_, 2); LDK(gi_, 3); SB(); } while (0)
#define LOADV(gi_) do { SB(); LDV(gi_, 0); LDV(gi_, 1); LDV(gi_, 2); LDV(gi_, 3); SB(); } while (0)
  v4u xq0, xq1;
  asm volatile("s_load_dwordx4 %0, %2, 0x0\n\ts_load_dwordx4 %1, %2, 0x400" : "=&s"(xq0), "=&s"(xq1) : "s"(a.x + 64 * r8 + 8 * w));
  SB();
  __builtin_amdgcn_s_barrier();
  LOADK(0);

  constexpr float C2 = 0.08838834764831845f * 1.4426950408889634f;
  float m = -INFINITY, l = 0.f, acc[8];
#pragma unroll
  for (int k = 0; k < 8; ++k) acc[k] = 0.f;
  unsigned tagA, tagB, qp[4];
  float mean0, rstd0, mean1, rstd1, n00, n01, n10, n11;
  v2u wor[32]; v4u wb[4][4], ffw[4], fu, qu, e0, e1; unsigned short xo; float gte = 0.f;
  asm volatile("s_waitcnt lgkmcnt(0)" : "+s"(xq0), "+s"(xq1));
  const unsigned xs0 = sel4(xq0, g), xs1 = sel4(xq1, g);
  const __amdgpu_buffer_rsrc_t rf = __builtin_amdgcn_make_buffer_rsrc((void*)(!has_f1 ? a.wd : fb < 11 ? a.wg : a.wu), 0, (has_f1 || has_f2) ? D * F * 2 : 0, 0x00020000);
  int fxo;
  { const int fcol = 128 * (fb < 11 ? fb : fb - 11) + 8 * c; fxo = !has_f1 ? (32 * cid + 4 * w) * D * 2 + lo16 : fcol < F ? (row0 * F + fcol) * 2 : OOB - 4 * F * 2; }
  int fuoff;
  { const int fbs = (g >> 1) * 11 + (cid >> 2), hs = (fbs / 3) * 4 + fbs % 3;
    fuoff = has_f2 ? WS_S1 + ((hs * 8 + (c >> 1)) * 256 + (2 + (g & 1)) * 64 + 16 * (cid & 3) + 2 * w + (c & 1)) * 16 : OOB; }
  const __amdgpu_buffer_rsrc_t rw = __builtin_amdgcn_make_buffer_rsrc((void*)(a.wo + (size_t)h * DH * D), 0, DH * D * 2, 0x00020000);
  const int rot = ((h & 3) << 1) | ((h & 12) << 3), wvo = g * 8192 + w * 128 + c * 8;
  const int qoff = WS_S1 + ((h * 8 + w) * 256 + b * 64 + lane) * 16;
  const int bc = (cid >> 5) & 1, cu0 = (cid & 31) * 8;
  const int gp = tid >> 2, qd = tid & 3, ocol = bc * D + ucol(cu0 + (c >> 1)) + (c & 1);
  const int dpoff = WS_DP + ((gp < NFFN ? gp : 0) * 512 + bc * 256 + cu0 + 2 * qd) * 16;

  auto stats = [&]() __attribute__((always_inline)) {
    f32x4 st[8];
#pragma unroll
    for (int i = 0; i < 8; ++i) st[i] = *(const f32x4*)(ldsS + 4 * i);
    tagA = __builtin_amdgcn_readfirstlane(ldsC[4]) + 1u; tagB = __builtin_amdgcn_readfirstlane(ldsC[5]) + 0x9E3779B9u;
    mean0 = ((st[0][0] + st[0][2]) + (st[1][0] + st[1][2]) + (st[2][0] + st[2][2]) + (st[3][0] + st[3][2])) * 0.125f;
    mean1 = ((st[4][0] + st[4][2]) + (st[5][0] + st[5][2]) + (st[6][0] + st[6][2]) + (st[7][0] + st[7][2])) * 0.125f;
    float q0 = 0.f, q1 = 0.f, d0 = 0.f, d1 = 0.f;
#pragma unroll
    for (int i = 0; i < 4; ++i) {
      q0 += st[i][1] + st[i][3]; q1 += st[4 + i][1] + st[4 + i][3];
      const float a0 = st[i][0] - mean0, b0 = st[i][2] - mean0, a1 = st[4 + i][0] - mean1, b1 = st[4 + i][2] - mean1;
      d0 += fmaf(a0, a0, b0 * b0); d1 += fmaf(a1, a1, b1 * b1);
    }
    rstd0 = 1.f / sqrtf(fmaf(64.f, d0, q0) * (1.f / D) + 1e-5f);
    rstd1 = 1.f / sqrtf(fmaf(64.f, d1, q1) * (1.f / D) + 1e-5f);
  };
  auto group = [&](auto gic) __attribute__((always_inline)) {
    constexpr int n = decltype(gic)::value % NG;
    float sc[U];
#pragma unroll
    for (int u = 0; u < U; ++u) {
      float d = dot2(kr[n][u][0], qp[0], 0.f);
      d = dot2(kr[n][u][1], qp[1], d);
      d = dot2(kr[n][u][2], qp[2], d);
      d = dot2(kr[n][u][3], qp[3], d);
      sc[u] = row16_sum(d) * C2;
    }
    float mx = m;
#pragma unroll
    for (int u = 0; u < U; ++u) mx = fmaxf(mx, sc[u]);
    const float alpha = __builtin_amdgcn_exp2f(m - mx);
    m = mx;
    float p[U], ps = 0.f;
#pragma unroll
    for (int u = 0; u < U; ++u) { p[u] = __builtin_amdgcn_exp2f(sc[u] - mx); ps += p[u]; }
    l = fmaf(l, alpha, ps);
#pragma unroll
    for (int k = 0; k < 8; ++k) acc[k] *= alpha;
    asm volatile("" : "+v"(acc[0]), "+v"(acc[1]), "+v"(acc[2]), "+v"(acc[3]), "+v"(acc[4]), "+v"(acc[5]), "+v"(acc[6]), "+v"(acc[7]), "+v"(l), "+v"(p[0]), "+v"(p[1]), "+v"(p[2]), "+v"(p[3]));
    SB();
#pragma unroll
    for (int u = 0; u < U; ++u) {
#pragma unroll
      for (int k = 0; k < 4; ++k) { acc[2 * k] = fmaf(p[u], lo(vr[n][u][k]), acc[2 * k]); acc[2 * k + 1] = fmaf(p[u], hi(vr[n][u][k]), acc[2 * k + 1]); }
    }
    asm volatile("" : "+v"(acc[0]), "+v"(acc[1]), "+v"(acc[2]), "+v"(acc[3]), "+v"(acc[4]), "+v"(acc[5]), "+v"(acc[6]), "+v"(acc[7]), "+v"(m));
    SB();
  };

  {
    float sm = row16_sum(lo(xst) + hi(xst)); sm = fold16(sm, sm) * (1.f / 64);
    const float d0 = lo(xst) - sm, d1 = hi(xst) - sm;
    float m2 = row16_sum(fmaf(d0, d0, d1 * d1)); m2 = fold16(m2, m2);
    if ((lane & 31) == 0) *(f32x2*)(ldsS + (lane >> 5) * 16 + 2 * w) = f32x2{sm, m2};
    float t[24];
#pragma unroll
    for (int k = 0; k < 4; ++k) {
      const float al = lo(sw0[k]), ah = hi(sw0[k]), bl = lo(sw1[k]), bh = hi(sw1[k]);
      t[2 * k] = fmaf(hi(xs0), bl, lo(xs0) * al); t[2 * k + 1] = fmaf(hi(xs0), bh, lo(xs0) * ah);
      t[8 + 2 * k] = fmaf(hi(xs1), bl, lo(xs1) * al); t[8 + 2 * k + 1] = fmaf(hi(xs1), bh, lo(xs1) * ah);
      t[16 + 2 * k] = al + bl; t[16 + 2 * k + 1] = ah + bh;
    }
#pragma unroll
    for (int k = 0; k < 8; ++k) t[k] = fold16(fold32(t[k], t[16 + k]), fold32(t[8 + k], t[16 + k]));
    *(f32x4*)(ldsY + w * 512 + g * 128 + 8 * c) = f32x4{t[0], t[1], t[2], t[3]};
    *(f32x4*)(ldsY + w * 512 + g * 128 + 8 * c + 4) = f32x4{t[4], t[5], t[6], t[7]};
    if (tid == 0) *(unsigned long long*)(ldsC + 4) = ep;
    asm volatile("" :: "v"(ep));
    if (last_wave(ldsC, lane)) {
      stats();
      f32x2 z0 = {0.f, 0.f}, z1 = {0.f, 0.f}, cs = {0.f, 0.f};
#pragma unroll
      for (int ww = 0; ww < NWAVE; ++ww) {
        z0 += *(const f32x2*)(ldsY + ww * 512 + 2 * lane); z1 += *(const f32x2*)(ldsY + ww * 512 + 128 + 2 * lane); cs += *(const f32x2*)(ldsY + ww * 512 + 256 + 2 * lane); }
      st_unit(rs, WS_S1 + ((h * 8 + r8) * 256 + lane) * 16, (z0[0] - mean0 * cs[0]) * rstd0, (z0[1] - mean0 * cs[1]) * rstd0, tagA, tagB);
      st_unit(rs, WS_S1 + ((h * 8 + r8) * 256 + 64 + lane) * 16, (z1[0] - mean1 * cs[0]) * rstd1, (z1[1] - mean1 * cs[1]) * rstd1, tagA, tagB);
    }
  }
  MARK(33);
  unsigned due = tstart + (unsigned)T0X + (unsigned)(w * TPX / 8);

  auto point = [&](auto pc) __attribute__((always_inline)) {
    constexpr int p = decltype(pc)::value + 1;
    constexpr int fi = p - PFL_, wi = p - PWO_;
    PACE(p, (fi == 0 || fi == 2 ? XF1_ : 0) + (fi >= 0 && fi < 4 ? XF2_ : 0) + (wi >= 0 && wi < 16 ? XWO_ : 0));
    if constexpr (p == 4) qu = ld_unit(rs, qoff);
    if constexpr (p & 1) LOADV(p / 2); else LOADK(p / 2);
    if constexpr (fi == 0 || fi == 2) ffw[fi] = __builtin_amdgcn_raw_buffer_load_b128(rf, fxo, fi * (has_f1 ? F : D * 2), WNT_ ? 2 : 0);
    if constexpr (fi == 1 || fi == 3) ffw[fi] = __builtin_amdgcn_raw_buffer_load_b128(rf, has_f1 ? OOB : fxo + fi * D * 2, 0, WNT_ ? 2 : 0);
    if constexpr (wi >= 0 && wi < 16) {
      const int so = ((32 * (wi >> 2) + 2 * (wi & 3)) ^ rot) * 1024;
      wor[2 * wi] = __builtin_amdgcn_raw_buffer_load_b64(rw, wvo, so, WNT_ ? 2 : 0); wor[2 * wi + 1] = __builtin_amdgcn_raw_buffer_load_b64(rw, wvo + 1024, so, WNT_ ? 2 : 0);
    }
    if constexpr (p == 12) fu = ld_unit(rs, fuoff);
    if constexpr (p == 20) {
      e0 = ld_unit(rs, is_fin ? dpoff : OOB); e1 = ld_unit(rs, is_fin ? dpoff + 16 : OOB);
      xo = ((const GAS unsigned short*)a.x)[ocol];
    }
    SB();
    if constexpr (p == 2) {
      wait_all(ldsC);
      stats();
      n00 = (lo(xs0) - mean0) * rstd0; n01 = (hi(xs0) - mean0) * rstd0; n10 = (lo(xs1) - mean1) * rstd1; n11 = (hi(xs1) - mean1) * rstd1;
      asm volatile("" : "+v"(n00), "+v"(n01), "+v"(n10), "+v"(n11));
    }
    if constexpr (p == 6) {
      await_unit(qu, rs, qoff, tagA, tagB);
      *(f32x2*)(ldsQ + w * 128 + 2 * lane) = f32x2{__uint_as_float(qu[0]), __uint_as_float(qu[2])};
      last_wave(ldsC + 1, lane);
    }
    if constexpr (p == 5) {
      if (has_f1) {
        float t[8];
#pragma unroll
        for (int k = 0; k < 4; ++k) {
          const float al = lo(ffw[0][k]), ah = hi(ffw[0][k]), bl = lo(ffw[2][k]), bh = hi(ffw[2][k]);
          t[2 * k] = fold32(fmaf(n01, bl, n00 * al), fmaf(n11, bl, n10 * al)); t[2 * k + 1] = fold32(fmaf(n01, bh, n00 * ah), fmaf(n11, bh, n10 * ah));
        }
        *(f32x4*)(ldsY + 4096 + w * 256 + (g >> 1) * 128 + 8 * c + 4 * (g & 1)) = f32x4{fold16(t[0], t[4]), fold16(t[1], t[5]), fold16(t[2], t[6]), fold16(t[3], t[7])};
        last_wave(ldsC + 2, lane);
      }
    }
    if constexpr (p == 8) {
      wait_all(ldsC + 1);
      f32x4 q0 = {0, 0, 0, 0}, q1 = {0, 0, 0, 0};
#pragma unroll
      for (int ww = 0; ww < NWAVE; ++ww) { q0 += *(const f32x4*)(ldsQ + ww * 128 + 8 * c); q1 += *(const f32x4*)(ldsQ + ww * 128 + 8 * c + 4); }
      qp[0] = pk_bf16(q0[0], q0[1]); qp[1] = pk_bf16(q0[2], q0[3]); qp[2] = pk_bf16(q1[0], q1[1]); qp[3] = pk_bf16(q1[2], q1[3]);
      group(IC<0>{});
    }
    if constexpr (p == 9) { group(IC<1>{}); group(IC<2>{}); }
    if constexpr (p == 10) group(IC<3>{});
    if constexpr (p >= 11 && (p & 1)) group(IC<p / 2 - 1>{});
    if constexpr (p == 7) {
      if (has_f1 && w < 2) {
        wait_all(ldsC + 2);
        f32x2 z = {0.f, 0.f};
#pragma unroll
        for (int ww = 0; ww < NWAVE; ++ww) z += *(const f32x2*)(ldsY + 4096 + ww * 256 + 2 * tid);
        st_unit(rs, WS_S1 + ((h * 8 + r8) * 256 + 128 + tid) * 16, z[0], z[1], tagA, tagB);
      }
    }
    if constexpr (p == 14) {
      if (has_f2) {
        await_unit(fu, rs, fuoff, tagA, tagB);
        float s0 = __uint_as_float(fu[0]), s1 = __uint_as_float(fu[2]);
        s0 += dpp<0x128>(s0); s1 += dpp<0x128>(s1); s0 += dpp<0x124>(s0); s1 += dpp<0x124>(s1); s0 += dpp<0x122>(s0); s1 += dpp<0x122>(s1);
        const float sel = (c & 2) ? s1 : s0;
        const auto gu = __builtin_amdgcn_permlane32_swap(__float_as_uint(sel), __float_as_uint(sel), false, false);
        const float hv = gelu_tanh(__uint_as_float(gu[0])) * __uint_as_float(gu[1]);
        float dd[16];
#pragma unroll
        for (int k = 0; k < 16; ++k) dd[k] = 0.f;
#pragma unroll
        for (int r = 0; r < 4; ++r) {
          const float h0 = __builtin_bit_cast(float, __builtin_amdgcn_readlane(__builtin_bit_cast(int, hv), ((r & 1) << 1) | (r >> 1)));
          const float h1 = __builtin_bit_cast(float, __builtin_amdgcn_readlane(__builtin_bit_cast(int, hv), 16 + (((r & 1) << 1) | (r >> 1))));
#pragma unroll
          for (int k = 0; k < 4; ++k) {
            const float wl = lo(ffw[r][k]), wh = hi(ffw[r][k]);
            dd[2 * k] = fmaf(h0, wl, dd[2 * k]); dd[2 * k + 1] = fmaf(h0, wh, dd[2 * k + 1]);
            dd[8 + 2 * k] = fmaf(h1, wl, dd[8 + 2 * k]); dd[8 + 2 * k + 1] = fmaf(h1, wh, dd[8 + 2 * k + 1]);
          }
        }
        *(f32x4*)(ldsY + w * 1024 + 8 * lane) = f32x4{dd[0], dd[1], dd[2], dd[3]};
        *(f32x4*)(ldsY + w * 1024 + 8 * lane + 4) = f32x4{dd[4], dd[5], dd[6], dd[7]};
        *(f32x4*)(ldsY + w * 1024 + 512 + 8 * lane) = f32x4{dd[8], dd[9], dd[10], dd[11]};
        *(f32x4*)(ldsY + w * 1024 + 512 + 8 * lane + 4) = f32x4{dd[12], dd[13], dd[14], dd[15]};
        last_wave(ldsC + 3, lane);
      }
    }
    if constexpr (p == 16) {
      if (has_f2) {
        wait_all(ldsC + 3);
        f32x2 dv = {0.f, 0.f};
#pragma unroll
        for (int ww = 0; ww < NWAVE; ++ww) dv += *(const f32x2*)(ldsY + ww * 1024 + (tid & 256) * 2 + ucol(tid & 255));
        st_unit(rs, WS_DP + (cid * 512 + tid) * 16, dv[0], dv[1], tagA, tagB);
      }
    }
    if constexpr (p == PWO_ + 18) {
#pragma unroll
      for (int i = 0; i < 16; ++i) {
        const v2u r0 = wor[2 * i], r1 = wor[2 * i + 1];
        wb[0][i >> 2][i & 3] = __builtin_amdgcn_perm(r1[0], r0[0], 0x05040100u); wb[1][i >> 2][i & 3] = __builtin_amdgcn_perm(r1[0], r0[0], 0x07060302u);
        wb[2][i >> 2][i & 3] = __builtin_amdgcn_perm(r1[1], r0[1], 0x05040100u); wb[3][i >> 2][i & 3] = __builtin_amdgcn_perm(r1[1], r0[1], 0x07060302u);
      }
#pragma unroll
      for (int i = 0; i < 4; ++i) asm volatile("" : "+v"(wb[i][0]), "+v"(wb[i][1]), "+v"(wb[i][2]), "+v"(wb[i][3]));
    }
    if constexpr (p == 22) {
      if (is_fin) {
        await_unit(e0, rs, dpoff, tagA, tagB);
        await_unit(e1, rs, dpoff + 16, tagA, tagB);
        const bool hd = gp < NFFN;
        float r = fold4(hd ? __uint_as_float(e0[0]) : 0.f, hd ? __uint_as_float(e0[2]) : 0.f, hd ? __uint_as_float(e1[0]) : 0.f, hd ? __uint_as_float(e1[2]) : 0.f);
        if (c < 4) ldsF[w * 16 + 4 * c + g] = r;
        last_wave(ldsC + 2, lane);
      }
    }
    if constexpr (p == 24) {
      if (is_fin && w == 0) {
        wait_all(ldsC + 2);
        const float dv = ldsF[c] + ldsF[16 + c] + ldsF[32 + c];
        gte = dv / (1.f + __expf(-dv));
        asm volatile("" : "+v"(gte));
      }
    }
    SB();
  };
  static_for<2 * GROUPS - 1>(point);
  group(IC<GROUPS - 1>{});
  MARK(34);

  {
    const float mw = xrow_max(m);
    const float sc = __builtin_amdgcn_exp2f(m - mw);
    l = xrow_sum(l * sc);
#pragma unroll
    for (int k = 0; k < 8; ++k) acc[k] *= sc;
    const f32x2 o2 = fold8(acc);
    const unsigned oh = pk_bf16(o2[0], o2[1]), ol = pk_bf16(o2[0] - lo(oh), o2[1] - hi(oh));
    const int ai = w * 68 + ((4 * c + g) ^ (rot >> 1));
    ldsA[ai] = oh; ldsA[8 * 68 + ai] = ol;
    if (lane == 0) { ldsM[w] = mw; ldsM[8 + w] = l; }
  }
  __syncthreads();
  MARK(35);
  const int pid = (b * H + h) * NSPLIT + s;
  {
    v4u af[4];
#pragma unroll
    for (int ks = 0; ks < 4; ++ks) af[ks] = *(const v4u*)(ldsA + c * 68 + 16 * ks + 4 * g);
    const f32x4 ma = *(const f32x4*)(ldsM + 4 * (g & 1)), mb = *(const f32x4*)(ldsM + 4 - 4 * (g & 1)), la = *(const f32x4*)(ldsM + 8 + 4 * (g & 1));
    f32x4 d[4];
#pragma unroll
    for (int t = 0; t < 4; ++t) d[t] = f32x4{0.f, 0.f, 0.f, 0.f};
#pragma unroll
    for (int ks = 0; ks < 4; ++ks)
#pragma unroll
      for (int t = 0; t < 4; ++t) d[t] = __builtin_amdgcn_mfma_f32_16x16x32_bf16(__builtin_bit_cast(bf16x8, af[ks]), __builtin_bit_cast(bf16x8, wb[t][ks]), d[t], 0, 0, 0);
    const float M = fmaxf(fmaxf(fmaxf(ma[0], ma[1]), fmaxf(ma[2], ma[3])), fmaxf(fmaxf(mb[0], mb[1]), fmaxf(mb[2], mb[3])));
    f32x4 e;
#pragma unroll
    for (int i = 0; i < 4; ++i) e[i] = __builtin_amdgcn_exp2f(ma[i] - M);
    float L = fmaf(e[0], la[0], e[1] * la[1]) + fmaf(e[2], la[2], e[3] * la[3]);
    L = fold16(L, L);
    if (tid == 448) st_unit(rs, WS_ML + pid * 16, M, L, tagA, tagB);
    float y[4];
#pragma unroll
    for (int t = 0; t < 4; ++t) y[t] = fmaf(e[0], d[t][0], e[1] * d[t][1]) + fmaf(e[2], d[t][2], e[3] * d[t][3]);
    float u0 = fold32(y[0], y[2]), u1 = fold32(y[1], y[3]);
    u0 = fold16(u0, u0); u1 = fold16(u1, u1);
    if ((g & 1) == 0) st_unit(rs, WS_Y + (pid * 256 + 32 * w + 16 * (g >> 1) + c) * 16, u0, u1, tagA, tagB);
  }
  MARK(36);

  if (is_fin) {
    const int yoff = WS_Y + ((bc * 128 + gp) * 256 + cu0 + 2 * qd) * 16;
    const int mloff = WS_ML + (bc * 128 + (gp & ~3) + qd) * 16;
    const int otoff = WS_ML + ((1 - bc) * 128 + gp) * 16;
    __builtin_amdgcn_s_barrier();
    if (PDLY_) __builtin_amdgcn_s_sleep(PDLY_);
    MARK(38);
    v4u y0, y1, ml;
    bool ok = false;
#pragma unroll 1
    for (unsigned spin = 0; spin < SPIN_CAP; ++spin) {
      asm volatile("" ::: "memory");
      if (!ok) {
        y0 = ld_unit(rs, yoff); y1 = ld_unit(rs, yoff + 16); ml = ld_unit(rs, mloff);
        const v4u ot = ld_unit(rs, cid == 0 ? otoff : mloff);
        ok = ((((y0[1] ^ tagA) | (y1[1] ^ tagA)) | ((ml[1] ^ tagA) | (ot[1] ^ tagA))) | (((y0[3] ^ tagB) | (y1[3] ^ tagB)) | ((ml[3] ^ tagB) | (ot[3] ^ tagB)))) == 0u;
      }
      if (__all(ok)) break;
      __builtin_amdgcn_s_sleep(1);
    }
    MARK(39);
    const float mq = __uint_as_float(ml[0]), lq = __uint_as_float(ml[2]);
    float M = fmaxf(mq, dpp<0xB1>(mq)); M = fmaxf(M, dpp<0x4E>(M));
    const float e = __builtin_amdgcn_exp2f(mq - M);
    float L = e * lq; L += dpp<0xB1>(L); L += dpp<0x4E>(L);
    const float eb0 = dpp<0x00>(e), eb1 = dpp<0x55>(e), eb2 = dpp<0xAA>(e), eb3 = dpp<0xFF>(e);
    const int sp = gp & 3;
    const float wt = (sp == 0 ? eb0 : sp == 1 ? eb1 : sp == 2 ? eb2 : eb3) * __builtin_amdgcn_rcpf(L);
    const float r = fold4(wt * __uint_as_float(y0[0]), wt * __uint_as_float(y0[2]), wt * __uint_as_float(y1[0]), wt * __uint_as_float(y1[2]));
    if (c < 4) ldsF[128 + w * 16 + 4 * c + g] = r;
    __syncthreads();
    if (tid < 16) {
      float sv = 0.f;
#pragma unroll
      for (int ww = 0; ww < NWAVE; ++ww) sv += ldsF[128 + ww * 16 + tid];
      const float o = __uint_as_float((unsigned)xo << 16) + sv * gte;
      const __bf16 ob = (__bf16)o;
      ((GAS unsigned short*)a.out)[ocol] = __builtin_bit_cast(unsigned short, ob);
      if (cid == 0 && tid == 0)
        __hip_atomic_store((GAS unsigned long long*)(a.ws + WS_EPOCH), ((unsigned long long)tagB << 32) | tagA, __ATOMIC_RELAXED, __HIP_MEMORY_SCOPE_AGENT);
    }
  }
}

extern "C" void kernel_launch(void* const* d_in, const int* in_sizes, int n_in, void* d_out, int out_size, void* d_ws, size_t ws_size,
                              hipStream_t stream) {
  Args a;
  a.x = (const unsigned short*)d_in[0];
  a.k = (const unsigned short*)d_in[1];
  a.v = (const unsigned short*)d_in[2];
  a.wq = (const unsigned short*)d_in[3];
  a.wo = (const unsigned short*)d_in[4];
  a.wg = (const unsigned short*)d_in[6];
  a.wu = (const unsigned short*)d_in[7];
  a.wd = (const unsigned short*)d_in[8];
  a.out = (unsigned short*)d_out;
  a.ws = (unsigned char*)d_ws;
  hipLaunchKernelGGL(k_all, dim3(256), dim3(NTHR), 0, stream, a);
}
```

```cpp
#include <hip/hip_runtime.h>
#include <hip/hip_bf16.h>
#include <stdint.h>

typedef unsigned v4u __attribute__((ext_vector_type(4)));
typedef float f32x4 __attribute__((ext_vector_type(4)));
typedef float f32x2 __attribute__((ext_vector_type(2)));
typedef __bf16 bf16x2 __attribute__((ext_vector_type(2)));
typedef __bf16 bf16x8 __attribute__((ext_vector_type(8)));
typedef unsigned v2u __attribute__((ext_vector_type(2)));
#define GAS __attribute__((address_space(1)))
#define CAS __attribute__((address_space(4)))

constexpr int D = 512, H = 32, SK = 8192, DH = 128, F = 1376;
constexpr int NSPLIT = 4, KEYS_PER_WG = SK / NSPLIT;
constexpr int NWAVE = 8, NTHR = NWAVE * 64;
constexpr int NFFN = F / 32;
constexpr int U = 4;
constexpr int NG = 5;
constexpr int GROUPS = KEYS_PER_WG / NWAVE / (4 * U);
#define PFL_ 1
#define PWO_ 10
#define DIST_ 2
#define PDLY_ 8
#define LATE_PRIO_ 400
#define XF1_ 54
#define XF2_ 12
#define XWO_ 8
#define KNT_ 1
#define VNT_ 1
#define WNT_ 1
#define ONT_ 0
#define TP_ 1800
#define T0_ 4000

constexpr int WS_EPOCH = 0;
constexpr int WS_ML = 4096;
#define MLV_ 0
constexpr int WS_Y = WS_ML + 65 * 128 * 16;
constexpr int WS_DP = WS_Y + 256 * 256 * 16;
constexpr int WS_S1 = WS_DP + NFFN * 512 * 16;
constexpr int WS_END = WS_S1 + 32 * 8 * 4 * 64 * 16;
constexpr int OOB = 0x7ffffff0;
constexpr unsigned SPIN_CAP = 400000;

#define SB() __builtin_amdgcn_sched_barrier(0)

__device__ __forceinline__ float dot2(unsigned k, unsigned q, float acc) {
  return __builtin_amdgcn_fdot2_f32_bf16(__builtin_bit_cast(bf16x2, k), __builtin_bit_cast(bf16x2, q), acc, false);
}
template <int CTRL> __device__ __forceinline__ float dpp(float x) {
  return __builtin_bit_cast(float, __builtin_amdgcn_mov_dpp(__builtin_bit_cast(int, x), CTRL, 0xf, 0xf, true));
}
__device__ __forceinline__ float row16_sum(float x) {
  x += dpp<0x128>(x);
  x += dpp<0x124>(x);
  x += dpp<0x122>(x);
  x += dpp<0x121>(x);
  return x;
}
__device__ __forceinline__ float fold32(float a, float b) {
  auto t = __builtin_amdgcn_permlane32_swap(__float_as_uint(a), __float_as_uint(b), false, false);
  return __uint_as_float(t[0]) + __uint_as_float(t[1]);
}
__device__ __forceinline__ float fold16(float a, float b) {
  auto t = __builtin_amdgcn_permlane16_swap(__float_as_uint(a), __float_as_uint(b), false, false);
  return __uint_as_float(t[0]) + __uint_as_float(t[1]);
}
__device__ __forceinline__ float xrow_sum(float x) { x = fold16(x, x); return fold32(x, x); }
__device__ __forceinline__ float xrow_max(float x) {
  auto s = __builtin_amdgcn_permlane16_swap(__float_as_uint(x), __float_as_uint(x), false, false);
  x = fmaxf(__uint_as_float(s[0]), __uint_as_float(s[1]));
  auto t = __builtin_amdgcn_permlane32_swap(__float_as_uint(x), __float_as_uint(x), false, false);
  return fmaxf(__uint_as_float(t[0]), __uint_as_float(t[1]));
}
__device__ __forceinline__ f32x2 fold8(const float (&v)[8]) {
  const float u0 = fold32(v[0], v[4]), u1 = fold32(v[1], v[5]), u2 = fold32(v[2], v[6]), u3 = fold32(v[3], v[7]);
  return f32x2{fold16(u0, u2), fold16(u1, u3)};
}
__device__ __forceinline__ float fold4(float v0, float v1, float v2, float v3) {
  float r = fold16(fold32(v0, v2), fold32(v1, v3));
  r += dpp<0x124>(r); r += dpp<0x128>(r);
  return r;
}
__device__ __forceinline__ float lo(unsigned u) { return __uint_as_float(u << 16); }
__device__ __forceinline__ float hi(unsigned u) { return __uint_as_float(u & 0xffff0000u); }
__device__ __forceinline__ unsigned pk_bf16(float a, float b) {
  bf16x2 r = {(__bf16)a, (__bf16)b};
  return __builtin_bit_cast(unsigned, r);
}
__device__ __forceinline__ unsigned sel4(v4u v, int g) { const unsigned a = (g & 1) ? v[1] : v[0], b = (g & 1) ? v[3] : v[2]; return (g & 2) ? b : a; }
__device__ __forceinline__ int ucol(int u) { return 64 * (u >> 5) + 4 * (u & 15) + 2 * ((u >> 4) & 1); }
template <int NT> __device__ __forceinline__ v4u ldp(const GAS v4u* p) { if (NT) return __builtin_nontemporal_load(p); else return *p; }
__device__ __forceinline__ float gelu_tanh(float x) {
  const float u = 0.7978845608028654f * (x + 0.044715f * x * x * x);
  const float e = __expf(2.f * u);
  const float t = 1.f - 2.f / (e + 1.f);
  return 0.5f * x * (1.f + t);
}
__device__ __forceinline__ void pace_wait(unsigned& due16, unsigned step16) {
  asm volatile("1:\n\ts_memrealtime s[90:91]\n\ts_waitcnt lgkmcnt(0)\n\ts_lshl_b32 s90, s90, 4\n\ts_sub_u32 s91, s90, %0\n\ts_cmp_ge_i32 s91, 0\n\ts_cbranch_scc1 2f\n\ts_sleep 2\n\ts_branch 1b\n"
               "2:\n\ts_add_u32 %0, %0, %1\n\ts_setprio 0\n\ts_cmp_lt_i32 s91, %2\n\ts_cbranch_scc1 3f\n\ts_setprio 2\n3:"
               : "+s"(due16) : "s"(step16), "i"(LATE_PRIO_) : "s90", "s91", "scc");
}
__device__ __forceinline__ unsigned now16() { return (unsigned)__builtin_amdgcn_s_memrealtime() << 4; }
#define MARK(i)
#define TPX TP_
#define T0X T0_
#define PACE(pt, x256) pace_wait(due, (unsigned)TPX * (256u + (x256)) / 256u)
constexpr int AUX_SC1 = 16;
__device__ __forceinline__ void st_unit(__amdgpu_buffer_rsrc_t rs, int off, float v0, float v1, unsigned ta, unsigned tb) {
  __builtin_amdgcn_raw_buffer_store_b128(v4u{__float_as_uint(v0), ta, __float_as_uint(v1), tb}, rs, off, 0, AUX_SC1);
}
__device__ __forceinline__ v4u ld_unit(__amdgpu_buffer_rsrc_t rs, int off) { return __builtin_amdgcn_raw_buffer_load_b128(rs, off, 0, AUX_SC1); }
__device__ __forceinline__ void await_unit(v4u& u, __amdgpu_buffer_rsrc_t rs, int off, unsigned ta, unsigned tb) {
  if (!__all(((u[1] ^ ta) | (u[3] ^ tb)) == 0u)) {
#pragma unroll 1
    for (unsigned spin = 0; spin < SPIN_CAP; ++spin) {
      __builtin_amdgcn_s_sleep(1);
      asm volatile("" ::: "memory");
      u = ld_unit(rs, off);
      if (__all(((u[1] ^ ta) | (u[3] ^ tb)) == 0u)) break;
    }
  }
}

__device__ __forceinline__ bool last_wave(unsigned* cnt, int lane) {
  asm volatile("s_waitcnt lgkmcnt(0)" ::: "memory");
  unsigned old = 0;
  if (lane == 0) old = __hip_atomic_fetch_add(cnt, 1u, __ATOMIC_RELAXED, __HIP_MEMORY_SCOPE_WORKGROUP);
  old = __builtin_amdgcn_readfirstlane(old);
  asm volatile("" ::: "memory");
  return old == NWAVE - 1;
}

__device__ __forceinline__ void wait_all(unsigned* cnt) {
  while (__builtin_amdgcn_readfirstlane(__hip_atomic_load(cnt, __ATOMIC_RELAXED, __HIP_MEMORY_SCOPE_WORKGROUP)) < NWAVE) __builtin_amdgcn_s_sleep(1);
  asm volatile("" ::: "memory");
}
template <int I> struct IC { static constexpr int value = I; };
template <int N, int I = 0, typename Fn> __device__ __forceinline__ void static_for(Fn& fn) { if constexpr (I < N) { fn(IC<I>{}); static_for<N, I + 1>(fn); } }

struct Args {
  const unsigned short* x; const unsigned short* wq;
  unsigned char* ws; const unsigned short* v; const unsigned short* wo; const unsigned short* wg;
  const unsigned short* wu; const unsigned short* wd; unsigned short* out; const unsigned short* k;
};

__global__ void __launch_bounds__(NTHR, 2) k_all(Args a) {
  __shared__ __attribute__((aligned(16))) float lds[8 * 1024 + 16 * 68 + 16 + 8 * 128 + 8 * 128 + 32 + 8];
  float* ldsY = lds;
  unsigned* ldsA = (unsigned*)(lds + 8 * 1024);
  float* ldsM = lds + 8 * 1024 + 16 * 68;
  float* ldsF = ldsM + 16;
  float* ldsQ = ldsF + 8 * 128;
  float* ldsS = ldsQ + 8 * 128;
  unsigned* ldsC = (unsigned*)(ldsS + 32);
  const unsigned tstart = now16();
  MARK(32);
  const int bid = blockIdx.x;
  const int xcd = bid & 7, j = bid >> 3;
  const int h = xcd * 4 + (j >> 3), b = (j >> 2) & 1, s = j & 3;
  const int tid = threadIdx.x, lane = tid & 63, w = __builtin_amdgcn_readfirstlane(tid >> 6);
  const int g = lane >> 4, c = lane & 15;
  const unsigned long long tptr = ((const CAS unsigned long long*)__builtin_amdgcn_kernarg_segment_ptr())[2 + w];
  if (tid == 0) *(v4u*)ldsC = v4u{0u, 0u, 0u, 0u};
  asm volatile("s_waitcnt lgkmcnt(0)" :: "s"(tptr), "s"(a.k), "s"(a.x), "s"(a.wq), "s"(tstart) : "memory");
  const int r8 = j & 7;
  const int fb = xcd * 3 + (j >> 3);
  const bool has_f1 = (j < 24) && (fb < 22);
  const bool is_fin = j >= 24;
  const int cid = xcd * 8 + (j - 24);
  const bool has_f2 = is_fin && cid < NFFN;
  const __amdgpu_buffer_rsrc_t rs = __builtin_amdgcn_make_buffer_rsrc((void*)a.ws, 0, WS_END, 0x00020000);

  const int row0 = 64 * r8 + 8 * w + 2 * g;
  v4u sw0, sw1;
  {
    const GAS v4u* p = (const GAS v4u*)(a.wq + (size_t)row0 * (H * DH) + h * DH + 8 * c);
    sw0 = ldp<WNT_>(p); sw1 = ldp<WNT_>(p + H * DH / 8);
  }
  const unsigned xst = *(const GAS unsigned*)(a.x + (lane >> 5) * D + 64 * w + 2 * (lane & 31));
  const unsigned long long ep = __hip_atomic_load((const GAS unsigned long long*)(tptr + ((unsigned)(b * H + h) * ((0x82u >> w) & 1u) << 21) + ((unsigned)h * ((0x04u >> w) & 1u) << 17)),
                                                  __ATOMIC_RELAXED, __HIP_MEMORY_SCOPE_AGENT);
  const __amdgpu_buffer_rsrc_t rk = __builtin_amdgcn_make_buffer_rsrc((void*)(a.k + (size_t)(b * H + h) * SK * DH), 0, SK * DH * 2, 0x00020000);
  const __amdgpu_buffer_rsrc_t rv = __builtin_amdgcn_make_buffer_rsrc((void*)(a.v + (size_t)(b * H + h) * SK * DH), 0, SK * DH * 2, 0x00020000);
  const int kvo = ((w & 1) << 13) | ((w >> 1) << 15) | ((s & 1) << 12) | ((s >> 1) << 14), lo16 = lane * 16;
#define GOFF(gi_) ((((gi_) & 3) << 17) | (((gi_) & 4) ? (1 << 19) | (1 << 14) : 0) | (((gi_) & 8) ? (1 << 20) | (1 << 12) : 0))
  v4u kr[NG][U], vr[NG][U];
#define LDK(gi_, u_) kr[(gi_) % NG][u_] = __builtin_amdgcn_raw_buffer_load_b128(rk, lo16 + (u_) * 1024, kvo ^ GOFF(gi_), KNT_ ? 2 : 0)
#define LDV(gi_, u_) vr[(gi_) % NG][u_] = __builtin_amdgcn_raw_buffer_load_b128(rv, lo16 + (u_) * 1024, kvo ^ GOFF(gi_), VNT_ ? 2 : 0)
#define LOADK(gi_) do { SB(); LDK(gi_, 0); LDK(gi_, 1); LDK(gi_, 2); LDK(gi_, 3); SB(); } while (0)
#define LOADV(gi_) do { SB(); LDV(gi_, 0); LDV(gi_, 1); LDV(gi_, 2); LDV(gi_, 3); SB(); } while (0)
  v4u xq0, xq1;
  asm volatile("s_load_dwordx4 %0, %2, 0x0\n\ts_load_dwordx4 %1, %2, 0x400" : "=&s"(xq0), "=&s"(xq1) : "s"(a.x + 64 * r8 + 8 * w));
  SB();
  __builtin_amdgcn_s_barrier();
  LOADK(0);

  constexpr float C2 = 0.08838834764831845f * 1.4426950408889634f;
  float m = -INFINITY, l = 0.f, acc[8];
#pragma unroll
  for (int k = 0; k < 8; ++k) acc[k] = 0.f;
  unsigned tagA, tagB, qp[4];
  float mean0, rstd0, mean1, rstd1, n00, n01, n10, n11;
  v2u wor[32]; v4u wb[4][4], ffw[4], fu, qu, e0, e1; unsigned short xo; float gte = 0.f;
  asm volatile("s_waitcnt lgkmcnt(0)" : "+s"(xq0), "+s"(xq1));
  const unsigned xs0 = sel4(xq0, g), xs1 = sel4(xq1, g);
  const __amdgpu_buffer_rsrc_t rf = __builtin_amdgcn_make_buffer_rsrc((void*)(!has_f1 ? a.wd : fb < 11 ? a.wg : a.wu), 0, (has_f1 || has_f2) ? D * F * 2 : 0, 0x00020000);
  int fxo;
  { const int fcol = 128 * (fb < 11 ? fb : fb - 11) + 8 * c; fxo = !has_f1 ? (32 * cid + 4 * w) * D * 2 + lo16 : fcol < F ? (row0 * F + fcol) * 2 : OOB - 4 * F * 2; }
  int fuoff;
  { const int fbs = (g >> 1) * 11 + (cid >> 2), hs = (fbs / 3) * 4 + fbs % 3;
    fuoff = has_f2 ? WS_S1 + ((hs * 8 + (c >> 1)) * 256 + (2 + (g & 1)) * 64 + 16 * (cid & 3) + 2 * w + (c & 1)) * 16 : OOB; }
  const __amdgpu_buffer_rsrc_t rw = __builtin_amdgcn_make_buffer_rsrc((void*)(a.wo + (size_t)h * DH * D), 0, DH * D * 2, 0x00020000);
  const int rot = ((h & 3) << 1) | ((h & 12) << 3), wvo = g * 8192 + w * 128 + c * 8;
  const int qoff = WS_S1 + ((h * 8 + w) * 256 + b * 64 + lane) * 16;
  const int bc = (cid >> 5) & 1, cu0 = (cid & 31) * 8;
  const int gp = tid >> 2, qd = tid & 3, ocol = bc * D + ucol(cu0 + (c >> 1)) + (c & 1);
  const int dpoff = WS_DP + ((gp < NFFN ? gp : 0) * 512 + bc * 256 + cu0 + 2 * qd) * 16;

  auto stats = [&]() __attribute__((always_inline)) {
    f32x4 st[8];
#pragma unroll
    for (int i = 0; i < 8; ++i) st[i] = *(const f32x4*)(ldsS + 4 * i);
    tagA = __builtin_amdgcn_readfirstlane(ldsC[4]) + 1u; tagB = __builtin_amdgcn_readfirstlane(ldsC[5]) + 0x9E3779B9u;
    mean0 = ((st[0][0] + st[0][2]) + (st[1][0] + st[1][2]) + (st[2][0] + st[2][2]) + (st[3][0] + st[3][2])) * 0.125f;
    mean1 = ((st[4][0] + st[4][2]) + (st[5][0] + st[5][2]) + (st[6][0] + st[6][2]) + (st[7][0] + st[7][2])) * 0.125f;
    float q0 = 0.f, q1 = 0.f, d0 = 0.f, d1 = 0.f;
#pragma unroll
    for (int i = 0; i < 4; ++i) {
      q0 += st[i][1] + st[i][3]; q1 += st[4 + i][1] + st[4 + i][3];
      const float a0 = st[i][0] - mean0, b0 = st[i][2] - mean0, a1 = st[4 + i][0] - mean1, b1 = st[4 + i][2] - mean1;
      d0 += fmaf(a0, a0, b0 * b0); d1 += fmaf(a1, a1, b1 * b1);
    }
    rstd0 = 1.f / sqrtf(fmaf(64.f, d0, q0) * (1.f / D) + 1e-5f);
    rstd1 = 1.f / sqrtf(fmaf(64.f, d1, q1) * (1.f / D) + 1e-5f);
  };
  auto group = [&](auto gic) __attribute__((always_inline)) {
    constexpr int n = decltype(gic)::value % NG;
    float sc[U];
#pragma unroll
    for (int u = 0; u < U; ++u) {
      float d = dot2(kr[n][u][0], qp[0], 0.f);
      d = dot2(kr[n][u][1], qp[1], d);
      d = dot2(kr[n][u][2], qp[2], d);
      d = dot2(kr[n][u][3], qp[3], d);
      sc[u] = row16_sum(d) * C2;
    }
    float mx = m;
#pragma unroll
    for (int u = 0; u < U; ++u) mx = fmaxf(mx, sc[u]);
    const float alpha = __builtin_amdgcn_exp2f(m - mx);
    m = mx;
    float p[U], ps = 0.f;
#pragma unroll
    for (int u = 0; u < U; ++u) { p[u] = __builtin_amdgcn_exp2f(sc[u] - mx); ps += p[u]; }
    l = fmaf(l, alpha, ps);
#pragma unroll
    for (int k = 0; k < 8; ++k) acc[k] *= alpha;
    asm volatile("" : "+v"(acc[0]), "+v"(acc[1]), "+v"(acc[2]), "+v"(acc[3]), "+v"(acc[4]), "+v"(acc[5]), "+v"(acc[6]), "+v"(acc[7]), "+v"(l), "+v"(p[0]), "+v"(p[1]), "+v"(p[2]), "+v"(p[3]));
    SB();
#pragma unroll
    for (int u = 0; u < U; ++u) {
#pragma unroll
      for (int k = 0; k < 4; ++k) { acc[2 * k] = fmaf(p[u], lo(vr[n][u][k]), acc[2 * k]); acc[2 * k + 1] = fmaf(p[u], hi(vr[n][u][k]), acc[2 * k + 1]); }
    }
    asm volatile("" : "+v"(acc[0]), "+v"(acc[1]), "+v"(acc[2]), "+v"(acc[3]), "+v"(acc[4]), "+v"(acc[5]), "+v"(acc[6]), "+v"(acc[7]), "+v"(m));
    SB();
  };

  {
    float sm = row16_sum(lo(xst) + hi(xst)); sm = fold16(sm, sm) * (1.f / 64);
    const float d0 = lo(xst) - sm, d1 = hi(xst) - sm;
    float m2 = row16_sum(fmaf(d0, d0, d1 * d1)); m2 = fold16(m2, m2);
    if ((lane & 31) == 0) *(f32x2*)(ldsS + (lane >> 5) * 16 + 2 * w) = f32x2{sm, m2};
    float t[24];
#pragma unroll
    for (int k = 0; k < 4; ++k) {
      const float al = lo(sw0[k]), ah = hi(sw0[k]), bl = lo(sw1[k]), bh = hi(sw1[k]);
      t[2 * k] = fmaf(hi(xs0), bl, lo(xs0) * al); t[2 * k + 1] = fmaf(hi(xs0), bh, lo(xs0) * ah);
      t[8 + 2 * k] = fmaf(hi(xs1), bl, lo(xs1) * al); t[8 + 2 * k + 1] = fmaf(hi(xs1), bh, lo(xs1) * ah);
      t[16 + 2 * k] = al + bl; t[16 + 2 * k + 1] = ah + bh;
    }
#pragma unroll
    for (int k = 0; k < 8; ++k) t[k] = fold16(fold32(t[k], t[16 + k]), fold32(t[8 + k], t[16 + k]));
    *(f32x4*)(ldsY + w * 512 + g * 128 + 8 * c) = f32x4{t[0], t[1], t[2], t[3]};
    *(f32x4*)(ldsY + w * 512 + g * 128 + 8 * c + 4) = f32x4{t[4], t[5], t[6], t[7]};
    if (tid == 0) *(unsigned long long*)(ldsC + 4) = ep;
    asm volatile("" :: "v"(ep));
    if (last_wave(ldsC, lane)) {
      stats();
      f32x2 z0 = {0.f, 0.f}, z1 = {0.f, 0.f}, cs = {0.f, 0.f};
#pragma unroll
      for (int ww = 0; ww < NWAVE; ++ww) {
        z0 += *(const f32x2*)(ldsY + ww * 512 + 2 * lane); z1 += *(const f32x2*)(ldsY + ww * 512 + 128 + 2 * lane); cs += *(const f32x2*)(ldsY + ww * 512 + 256 + 2 * lane); }
      st_unit(rs, WS_S1 + ((h * 8 + r8) * 256 + lane) * 16, (z0[0] - mean0 * cs[0]) * rstd0, (z0[1] - mean0 * cs[1]) * rstd0, tagA, tagB);
      st_unit(rs, WS_S1 + ((h * 8 + r8) * 256 + 64 + lane) * 16, (z1[0] - mean1 * cs[0]) * rstd1, (z1[1] - mean1 * cs[1]) * rstd1, tagA, tagB);
    }
  }
  MARK(33);
  unsigned due = tstart + (unsigned)T0X + (unsigned)(w * TPX / 8);

  auto point = [&](auto pc) __attribute__((always_inline)) {
    constexpr int p = decltype(pc)::value + 1;
    constexpr int fi = p - PFL_, wi = p - PWO_;
    PACE(p, (fi == 0 || fi == 2 ? XF1_ : 0) + (fi >= 0 && fi < 4 ? XF2_ : 0) + (wi >= 0 && wi < 16 ? XWO_ : 0));
    if constexpr (p == 4) qu = ld_unit(rs, qoff);
    if constexpr (p & 1) LOADV(p / 2); else LOADK(p / 2);
    if constexpr (fi == 0 || fi == 2) ffw[fi] = __builtin_amdgcn_raw_buffer_load_b128(rf, fxo, fi * (has_f1 ? F : D * 2), WNT_ ? 2 : 0);
    if constexpr (fi == 1 || fi == 3) ffw[fi] = __builtin_amdgcn_raw_buffer_load_b128(rf, has_f1 ? OOB : fxo + fi * D * 2, 0, WNT_ ? 2 : 0);
    if constexpr (wi >= 0 && wi < 16) {
      const int so = ((32 * (wi >> 2) + 2 * (wi & 3)) ^ rot) * 1024;
      wor[2 * wi] = __builtin_amdgcn_raw_buffer_load_b64(rw, wvo, so, ONT_ ? 2 : 0); wor[2 * wi + 1] = __builtin_amdgcn_raw_buffer_load_b64(rw, wvo + 1024, so, ONT_ ? 2 : 0);
    }
    if constexpr (p == 12) fu = ld_unit(rs, fuoff);
    if constexpr (p == 20) {
      e0 = ld_unit(rs, is_fin ? dpoff : OOB); e1 = ld_unit(rs, is_fin ? dpoff + 16 : OOB);
      xo = ((const GAS unsigned short*)a.x)[ocol];
    }
    SB();
    if constexpr (p == 2) {
      wait_all(ldsC);
      stats();
      n00 = (lo(xs0) - mean0) * rstd0; n01 = (hi(xs0) - mean0) * rstd0; n10 = (lo(xs1) - mean1) * rstd1; n11 = (hi(xs1) - mean1) * rstd1;
      asm volatile("" : "+v"(n00), "+v"(n01), "+v"(n10), "+v"(n11));
    }
    if constexpr (p == 6) {
      await_unit(qu, rs, qoff, tagA, tagB);
      *(f32x2*)(ldsQ + w * 128 + 2 * lane) = f32x2{__uint_as_float(qu[0]), __uint_as_float(qu[2])};
      last_wave(ldsC + 1, lane);
    }
    if constexpr (p == 5) {
      if (has_f1) {
        float t[8];
#pragma unroll
        for (int k = 0; k < 4; ++k) {
          const float al = lo(ffw[0][k]), ah = hi(ffw[0][k]), bl = lo(ffw[2][k]), bh = hi(ffw[2][k]);
          t[2 * k] = fold32(fmaf(n01, bl, n00 * al), fmaf(n11, bl, n10 * al)); t[2 * k + 1] = fold32(fmaf(n01, bh, n00 * ah), fmaf(n11, bh, n10 * ah));
        }
        *(f32x4*)(ldsY + 4096 + w * 256 + (g >> 1) * 128 + 8 * c + 4 * (g & 1)) = f32x4{fold16(t[0], t[4]), fold16(t[1], t[5]), fold16(t[2], t[6]), fold16(t[3], t[7])};
        last_wave(ldsC + 2, lane);
      }
    }
    if constexpr (p == 8) {
      wait_all(ldsC + 1);
      f32x4 q0 = {0, 0, 0, 0}, q1 = {0, 0, 0, 0};
#pragma unroll
      for (int ww = 0; ww < NWAVE; ++ww) { q0 += *(const f32x4*)(ldsQ + ww * 128 + 8 * c); q1 += *(const f32x4*)(ldsQ + ww * 128 + 8 * c + 4); }
      qp[0] = pk_bf16(q0[0], q0[1]); qp[1] = pk_bf16(q0[2], q0[3]); qp[2] = pk_bf16(q1[0], q1[1]); qp[3] = pk_bf16(q1[2], q1[3]);
      group(IC<0>{});
    }
    if constexpr (p == 9) group(IC<1>{});
    if constexpr (p == 10) group(IC<2>{});
    if constexpr (p >= 11 && p < 30 && (p & 1)) group(IC<p / 2 - 2>{});
    if constexpr (p >= 30) group(IC<p - 17>{});
    if constexpr (p == 7) {
      if (has_f1 && w < 2) {
        wait_all(ldsC + 2);
        f32x2 z = {0.f, 0.f};
#pragma unroll
        for (int ww = 0; ww < NWAVE; ++ww) z += *(const f32x2*)(ldsY + 4096 + ww * 256 + 2 * tid);
        st_unit(rs, WS_S1 + ((h * 8 + r8) * 256 + 128 + tid) * 16, z[0], z[1], tagA, tagB);
      }
    }
    if constexpr (p == 14) {
      if (has_f2) {
        await_unit(fu, rs, fuoff, tagA, tagB);
        float s0 = __uint_as_float(fu[0]), s1 = __uint_as_float(fu[2]);
        s0 += dpp<0x128>(s0); s1 += dpp<0x128>(s1); s0 += dpp<0x124>(s0); s1 += dpp<0x124>(s1); s0 += dpp<0x122>(s0); s1 += dpp<0x122>(s1);
        const float sel = (c & 2) ? s1 : s0;
        const auto gu = __builtin_amdgcn_permlane32_swap(__float_as_uint(sel), __float_as_uint(sel), false, false);
        const float hv = gelu_tanh(__uint_as_float(gu[0])) * __uint_as_float(gu[1]);
        float dd[16];
#pragma unroll
        for (int k = 0; k < 16; ++k) dd[k] = 0.f;
#pragma unroll
        for (int r = 0; r < 4; ++r) {
          const float h0 = __builtin_bit_cast(float, __builtin_amdgcn_readlane(__builtin_bit_cast(int, hv), ((r & 1) << 1) | (r >> 1)));
          const float h1 = __builtin_bit_cast(float, __builtin_amdgcn_readlane(__builtin_bit_cast(int, hv), 16 + (((r & 1) << 1) | (r >> 1))));
#pragma unroll
          for (int k = 0; k < 4; ++k) {
            const float wl = lo(ffw[r][k]), wh = hi(ffw[r][k]);
            dd[2 * k] = fmaf(h0, wl, dd[2 * k]); dd[2 * k + 1] = fmaf(h0, wh, dd[2 * k + 1]);
            dd[8 + 2 * k] = fmaf(h1, wl, dd[8 + 2 * k]); dd[8 + 2 * k + 1] = fmaf(h1, wh, dd[8 + 2 * k + 1]);
          }
        }
        *(f32x4*)(ldsY + w * 1024 + 8 * lane) = f32x4{dd[0], dd[1], dd[2], dd[3]};
        *(f32x4*)(ldsY + w * 1024 + 8 * lane + 4) = f32x4{dd[4], dd[5], dd[6], dd[7]};
        *(f32x4*)(ldsY + w * 1024 + 512 + 8 * lane) = f32x4{dd[8], dd[9], dd[10], dd[11]};
        *(f32x4*)(ldsY + w * 1024 + 512 + 8 * lane + 4) = f32x4{dd[12], dd[13], dd[14], dd[15]};
        last_wave(ldsC + 3, lane);
      }
    }
    if constexpr (p == 16) {
      if (has_f2) {
        wait_all(ldsC + 3);
        f32x2 dv = {0.f, 0.f};
#pragma unroll
        for (int ww = 0; ww < NWAVE; ++ww) dv += *(const f32x2*)(ldsY + ww * 1024 + (tid & 256) * 2 + ucol(tid & 255));
        st_unit(rs, WS_DP + (cid * 512 + tid) * 16, dv[0], dv[1], tagA, tagB);
      }
    }
    if constexpr (p == PWO_ + 18) {
#pragma unroll
      for (int i = 0; i < 16; ++i) {
        const v2u r0 = wor[2 * i], r1 = wor[2 * i + 1];
        wb[0][i >> 2][i & 3] = __builtin_amdgcn_perm(r1[0], r0[0], 0x05040100u); wb[1][i >> 2][i & 3] = __builtin_amdgcn_perm(r1[0], r0[0], 0x07060302u);
        wb[2][i >> 2][i & 3] = __builtin_amdgcn_perm(r1[1], r0[1], 0x05040100u); wb[3][i >> 2][i & 3] = __builtin_amdgcn_perm(r1[1], r0[1], 0x07060302u);
      }
#pragma unroll
      for (int i = 0; i < 4; ++i) asm volatile("" : "+v"(wb[i][0]), "+v"(wb[i][1]), "+v"(wb[i][2]), "+v"(wb[i][3]));
    }
    if constexpr (p == 22) {
      if (is_fin) {
        await_unit(e0, rs, dpoff, tagA, tagB);
        await_unit(e1, rs, dpoff + 16, tagA, tagB);
        const bool hd = gp < NFFN;
        float r = fold4(hd ? __uint_as_float(e0[0]) : 0.f, hd ? __uint_as_float(e0[2]) : 0.f, hd ? __uint_as_float(e1[0]) : 0.f, hd ? __uint_as_float(e1[2]) : 0.f);
        if (c < 4) ldsF[w * 16 + 4 * c + g] = r;
        last_wave(ldsC + 2, lane);
      }
    }
    if constexpr (p == 24) {
      if (is_fin && w == 0) {
        wait_all(ldsC + 2);
        const float dv = ldsF[c] + ldsF[16 + c] + ldsF[32 + c];
        gte = dv / (1.f + __expf(-dv));
        asm volatile("" : "+v"(gte));
      }
    }
    SB();
  };
  static_for<2 * GROUPS - 1>(point);
  group(IC<GROUPS - 1>{});
  MARK(34);

  {
    const float mw = xrow_max(m);
    const float sc = __builtin_amdgcn_exp2f(m - mw);
    l = xrow_sum(l * sc);
#pragma unroll
    for (int k = 0; k < 8; ++k) acc[k] *= sc;
    const f32x2 o2 = fold8(acc);
    const unsigned oh = pk_bf16(o2[0], o2[1]), ol = pk_bf16(o2[0] - lo(oh), o2[1] - hi(oh));
    const int ai = w * 68 + ((4 * c + g) ^ (rot >> 1));
    ldsA[ai] = oh; ldsA[8 * 68 + ai] = ol;
    if (lane == 0) { ldsM[w] = mw; ldsM[8 + w] = l; }
  }
  __syncthreads();
  MARK(35);
  const int pid = (b * H + h) * NSPLIT + s;
  {
    v4u af[4];
#pragma unroll
    for (int ks = 0; ks < 4; ++ks) af[ks] = *(const v4u*)(ldsA + c * 68 + 16 * ks + 4 * g);
    const f32x4 ma = *(const f32x4*)(ldsM + 4 * (g & 1)), mb = *(const f32x4*)(ldsM + 4 - 4 * (g & 1)), la = *(const f32x4*)(ldsM + 8 + 4 * (g & 1));
    f32x4 d[4];
#pragma unroll
    for (int t = 0; t < 4; ++t) d[t] = f32x4{0.f, 0.f, 0.f, 0.f};
#pragma unroll
    for (int ks = 0; ks < 4; ++ks)
#pragma unroll
      for (int t = 0; t < 4; ++t) d[t] = __builtin_amdgcn_mfma_f32_16x16x32_bf16(__builtin_bit_cast(bf16x8, af[ks]), __builtin_bit_cast(bf16x8, wb[t][ks]), d[t], 0, 0, 0);
    const float M = fmaxf(fmaxf(fmaxf(ma[0], ma[1]), fmaxf(ma[2], ma[3])), fmaxf(fmaxf(mb[0], mb[1]), fmaxf(mb[2], mb[3])));
    f32x4 e;
#pragma unroll
    for (int i = 0; i < 4; ++i) e[i] = __builtin_amdgcn_exp2f(ma[i] - M);
    float L = fmaf(e[0], la[0], e[1] * la[1]) + fmaf(e[2], la[2], e[3] * la[3]);
    L = fold16(L, L);
    if (MLV_ == 0) { if (tid == 448) st_unit(rs, WS_ML + pid * 16, M, L, tagA, tagB); }
    else if (MLV_ == 1) { if (tid == 448) st_unit(rs, WS_ML + pid * 128, M, L, tagA, tagB); }
    else if (w == 7 && lane < 33 && (lane < 32 || b == 1)) st_unit(rs, WS_ML + ((lane < 32 ? b * 32 + lane : 64) * 128 + (pid & 127)) * 16, M, L, tagA, tagB);
    float y[4];
#pragma unroll
    for (int t = 0; t < 4; ++t) y[t] = fmaf(e[0], d[t][0], e[1] * d[t][1]) + fmaf(e[2], d[t][2], e[3] * d[t][3]);
    float u0 = fold32(y[0], y[2]), u1 = fold32(y[1], y[3]);
    u0 = fold16(u0, u0); u1 = fold16(u1, u1);
    if ((g & 1) == 0) st_unit(rs, WS_Y + (pid * 256 + 32 * w + 16 * (g >> 1) + c) * 16, u0, u1, tagA, tagB);
  }
  MARK(36);

  if (is_fin) {
    const int yoff = WS_Y + ((bc * 128 + gp) * 256 + cu0 + 2 * qd) * 16;
    const int mloff = WS_ML + (MLV_ == 2 ? (cid * 128 + (gp & ~3) + qd) * 16 : (bc * 128 + (gp & ~3) + qd) * (MLV_ == 1 ? 128 : 16));
    const int otoff = WS_ML + (MLV_ == 2 ? (64 * 128 + gp) * 16 : ((1 - bc) * 128 + gp) * (MLV_ == 1 ? 128 : 16));
    __builtin_amdgcn_s_barrier();
    if (PDLY_) __builtin_amdgcn_s_sleep(PDLY_);
    MARK(38);
    v4u y0, y1, ml;
    bool ok = false;
#pragma unroll 1
    for (unsigned spin = 0; spin < SPIN_CAP; ++spin) {
      asm volatile("" ::: "memory");
      if (!ok) {
        y0 = ld_unit(rs, yoff); y1 = ld_unit(rs, yoff + 16); ml = ld_unit(rs, mloff);
        v4u ot = ld_unit(rs, cid == 0 ? otoff : OOB); if (cid != 0) ot = ml;
        ok = ((((y0[1] ^ tagA) | (y1[1] ^ tagA)) | ((ml[1] ^ tagA) | (ot[1] ^ tagA))) | (((y0[3] ^ tagB) | (y1[3] ^ tagB)) | ((ml[3] ^ tagB) | (ot[3] ^ tagB)))) == 0u;
      }
      if (__all(ok)) break;
      __builtin_amdgcn_s_sleep(1);
    }
    MARK(39);
    const float mq = __uint_as_float(ml[0]), lq = __uint_as_float(ml[2]);
    float M = fmaxf(mq, dpp<0xB1>(mq)); M = fmaxf(M, dpp<0x4E>(M));
    const float e = __builtin_amdgcn_exp2f(mq - M);
    float L = e * lq; L += dpp<0xB1>(L); L += dpp<0x4E>(L);
    const float eb0 = dpp<0x00>(e), eb1 = dpp<0x55>(e), eb2 = dpp<0xAA>(e), eb3 = dpp<0xFF>(e);
    const int sp = gp & 3;
    const float wt = (sp == 0 ? eb0 : sp == 1 ? eb1 : sp == 2 ? eb2 : eb3) * __builtin_amdgcn_rcpf(L);
    const float r = fold4(wt * __uint_as_float(y0[0]), wt * __uint_as_float(y0[2]), wt * __uint_as_float(y1[0]), wt * __uint_as_float(y1[2]));
    if (c < 4) ldsF[128 + w * 16 + 4 * c + g] = r;
    __syncthreads();
    if (tid < 16) {
      float sv = 0.f;
#pragma unroll
      for (int ww = 0; ww < NWAVE; ++ww) sv += ldsF[128 + ww * 16 + tid];
      const float o = __uint_as_float((unsigned)xo << 16) + sv * gte;
      const __bf16 ob = (__bf16)o;
      ((GAS unsigned short*)a.out)[ocol] = __builtin_bit_cast(unsigned short, ob);
      if (cid == 0 && tid == 0)
        __hip_atomic_store((GAS unsigned long long*)(a.ws + WS_EPOCH), ((unsigned long long)tagB << 32) | tagA, __ATOMIC_RELAXED, __HIP_MEMORY_SCOPE_AGENT);
    }
  }
}

extern "C" void kernel_launch(void* const* d_in, const int* in_sizes, int n_in, void* d_out, int out_size, void* d_ws, size_t ws_size,
                              hipStream_t stream) {
  Args a;
  a.x = (const unsigned short*)d_in[0];
  a.k = (const unsigned short*)d_in[1];
  a.v = (const unsigned short*)d_in[2];
  a.wq = (const unsigned short*)d_in[3];
  a.wo = (const unsigned short*)d_in[4];
  a.wg = (const unsigned short*)d_in[6];
  a.wu = (const unsigned short*)d_in[7];
  a.wd = (const unsigned short*)d_in[8];
  a.out = (unsigned short*)d_out;
  a.ws = (unsigned char*)d_ws;
  hipLaunchKernelGGL(k_all, dim3(256), dim3(NTHR), 0, stream, a);
}
```

```cpp
#include <hip/hip_runtime.h>
#include <hip/hip_bf16.h>
#include <stdint.h>

typedef unsigned v4u __attribute__((ext_vector_type(4)));
typedef float f32x4 __attribute__((ext_vector_type(4)));
typedef float f32x2 __attribute__((ext_vector_type(2)));
typedef __bf16 bf16x2 __attribute__((ext_vector_type(2)));
typedef __bf16 bf16x8 __attribute__((ext_vector_type(8)));
typedef unsigned v2u __attribute__((ext_vector_type(2)));
#define GAS __attribute__((address_space(1)))
#define CAS __attribute__((address_space(4)))

constexpr int D = 512, H = 32, SK = 8192, DH = 128, F = 1376;
constexpr int NSPLIT = 4, KEYS_PER_WG = SK / NSPLIT;
constexpr int NWAVE = 8, NTHR = NWAVE * 64;
constexpr int NFFN = F / 32;
constexpr int U = 4;
constexpr int GROUPS = KEYS_PER_WG / NWAVE / (4 * U);
#define PWO_ 1
constexpr int won(int p) { return p <= PWO_ ? 0 : p >= 28 ? 32 : (p - PWO_) * 32 / (28 - PWO_); }
#define PDLY_ 8
#define LATE_PRIO_ 400
#define KNT_ 1
#define VNT_ 1
#define WNT_ 1
#define ONT_ 0
#define TP_ 1760
#define T0_ 4800

constexpr int WS_EPOCH = 0;
constexpr int WS_ML = 4096;
constexpr int WS_Y = WS_ML + 256 * 16;
constexpr int WS_DP = WS_Y + 256 * 256 * 16;
constexpr int WS_S1 = WS_DP + NFFN * 512 * 16;
constexpr int WS_END = WS_S1 + 32 * 8 * 4 * 64 * 16;
constexpr int OOB = 0x7ffffff0;
constexpr unsigned SPIN_CAP = 400000;

#define SB() __builtin_amdgcn_sched_barrier(0)

__device__ __forceinline__ float dot2(unsigned k, unsigned q, float acc) {
  return __builtin_amdgcn_fdot2_f32_bf16(__builtin_bit_cast(bf16x2, k), __builtin_bit_cast(bf16x2, q), acc, false);
}
template <int CTRL> __device__ __forceinline__ float dpp(float x) {
  return __builtin_bit_cast(float, __builtin_amdgcn_mov_dpp(__builtin_bit_cast(int, x), CTRL, 0xf, 0xf, true));
}
__device__ __forceinline__ float row16_sum(float x) {
  x += dpp<0x128>(x);
  x += dpp<0x124>(x);
  x += dpp<0x122>(x);
  x += dpp<0x121>(x);
  return x;
}
__device__ __forceinline__ float fold32(float a, float b) {
  auto t = __builtin_amdgcn_permlane32_swap(__float_as_uint(a), __float_as_uint(b), false, false);
  return __uint_as_float(t[0]) + __uint_as_float(t[1]);
}
__device__ __forceinline__ float fold16(float a, float b) {
  auto t = __builtin_amdgcn_permlane16_swap(__float_as_uint(a), __float_as_uint(b), false, false);
  return __uint_as_float(t[0]) + __uint_as_float(t[1]);
}
__device__ __forceinline__ float xrow_sum(float x) { x = fold16(x, x); return fold32(x, x); }
__device__ __forceinline__ float xrow_max(float x) {
  auto s = __builtin_amdgcn_permlane16_swap(__float_as_uint(x), __float_as_uint(x), false, false);
  x = fmaxf(__uint_as_float(s[0]), __uint_as_float(s[1]));
  auto t = __builtin_amdgcn_permlane32_swap(__float_as_uint(x), __float_as_uint(x), false, false);
  return fmaxf(__uint_as_float(t[0]), __uint_as_float(t[1]));
}
__device__ __forceinline__ f32x2 fold8(const float (&v)[8]) {
  const float u0 = fold32(v[0], v[4]), u1 = fold32(v[1], v[5]), u2 = fold32(v[2], v[6]), u3 = fold32(v[3], v[7]);
  return f32x2{fold16(u0, u2), fold16(u1, u3)};
}
__device__ __forceinline__ float fold4(float v0, float v1, float v2, float v3) {
  float r = fold16(fold32(v0, v2), fold32(v1, v3));
  r += dpp<0x124>(r); r += dpp<0x128>(r);
  return r;
}
__device__ __forceinline__ float lo(unsigned u) { return __uint_as_float(u << 16); }
__device__ __forceinline__ float hi(unsigned u) { return __uint_as_float(u & 0xffff0000u); }
__device__ __forceinline__ unsigned pk_bf16(float a, float b) {
  bf16x2 r = {(__bf16)a, (__bf16)b};
  return __builtin_bit_cast(unsigned, r);
}
__device__ __forceinline__ unsigned sel4(v4u v, int g) { const unsigned a = (g & 1) ? v[1] : v[0], b = (g & 1) ? v[3] : v[2]; return (g & 2) ? b : a; }
__device__ __forceinline__ int ucol(int u) { return 64 * (u >> 5) + 4 * (u & 15) + 2 * ((u >> 4) & 1); }
template <int NT> __device__ __forceinline__ v4u ldp(const GAS v4u* p) { if (NT) return __builtin_nontemporal_load(p); else return *p; }
__device__ __forceinline__ float gelu_tanh(float x) {
  const float u = 0.7978845608028654f * (x + 0.044715f * x * x * x);
  const float e = __expf(2.f * u);
  const float t = 1.f - 2.f / (e + 1.f);
  return 0.5f * x * (1.f + t);
}
__device__ __forceinline__ void pace_wait(unsigned& due16, unsigned step16) {
  asm volatile("1:\n\ts_memrealtime s[90:91]\n\ts_waitcnt lgkmcnt(0)\n\ts_lshl_b32 s90, s90, 4\n\ts_sub_u32 s91, s90, %0\n\ts_cmp_ge_i32 s91, 0\n\ts_cbranch_scc1 2f\n\ts_sleep 2\n\ts_branch 1b\n"
               "2:\n\ts_add_u32 %0, %0, %1\n\ts_setprio 0\n\ts_cmp_lt_i32 s91, %2\n\ts_cbranch_scc1 3f\n\ts_setprio 2\n3:"
               : "+s"(due16) : "s"(step16), "i"(LATE_PRIO_) : "s90", "s91", "scc");
}
__device__ __forceinline__ unsigned now16() { return (unsigned)__builtin_amdgcn_s_memrealtime() << 4; }
#define MARK(i)
#define TPX TP_
#define T0X T0_
#define PACE(pt) pace_wait(due, (unsigned)TPX)
constexpr int AUX_SC1 = 16;
__device__ __forceinline__ void st_unit(__amdgpu_buffer_rsrc_t rs, int off, float v0, float v1, unsigned ta, unsigned tb) {
  __builtin_amdgcn_raw_buffer_store_b128(v4u{__float_as_uint(v0), ta, __float_as_uint(v1), tb}, rs, off, 0, AUX_SC1);
}
__device__ __forceinline__ v4u ld_unit(__amdgpu_buffer_rsrc_t rs, int off) { return __builtin_amdgcn_raw_buffer_load_b128(rs, off, 0, AUX_SC1); }
__device__ __forceinline__ void await_unit(v4u& u, __amdgpu_buffer_rsrc_t rs, int off, unsigned ta, unsigned tb) {
  if (!__all(((u[1] ^ ta) | (u[3] ^ tb)) == 0u)) {
#pragma unroll 1
    for (unsigned spin = 0; spin < SPIN_CAP; ++spin) {
      __builtin_amdgcn_s_sleep(1);
      asm volatile("" ::: "memory");
      u = ld_unit(rs, off);
      if (__all(((u[1] ^ ta) | (u[3] ^ tb)) == 0u)) break;
    }
  }
}

__device__ __forceinline__ bool last_wave(unsigned* cnt, int lane) {
  asm volatile("s_waitcnt lgkmcnt(0)" ::: "memory");
  unsigned old = 0;
  if (lane == 0) old = __hip_atomic_fetch_add(cnt, 1u, __ATOMIC_RELAXED, __HIP_MEMORY_SCOPE_WORKGROUP);
  old = __builtin_amdgcn_readfirstlane(old);
  asm volatile("" ::: "memory");
  return old == NWAVE - 1;
}

__device__ __forceinline__ void wait_all(unsigned* cnt) {
  while (__builtin_amdgcn_readfirstlane(__hip_atomic_load(cnt, __ATOMIC_RELAXED, __HIP_MEMORY_SCOPE_WORKGROUP)) < NWAVE) __builtin_amdgcn_s_sleep(1);
  asm volatile("" ::: "memory");
}
template <int I> struct IC { static constexpr int value = I; };
template <int N, int I = 0, typename Fn> __device__ __forceinline__ void static_for(Fn& fn) { if constexpr (I < N) { fn(IC<I>{}); static_for<N, I + 1>(fn); } }

struct Args {
  const unsigned short* x; const unsigned short* wq;
  unsigned char* ws; const unsigned short* v; const unsigned short* wo; const unsigned short* wg;
  const unsigned short* wu; const unsigned short* wd; unsigned short* out; const unsigned short* k;
};

__global__ void __launch_bounds__(NTHR, 2) k_all(Args a) {
  __shared__ __attribute__((aligned(16))) float lds[8 * 1024 + 16 * 68 + 16 + 8 * 128 + 8 * 128 + 32 + 12];
  float* ldsY = lds;
  unsigned* ldsA = (unsigned*)(lds + 8 * 1024);
  float* ldsM = lds + 8 * 1024 + 16 * 68;
  float* ldsF = ldsM + 16;
  float* ldsQ = ldsF + 8 * 128;
  float* ldsS = ldsQ + 8 * 128;
  unsigned* ldsC = (unsigned*)(ldsS + 32);
  const unsigned tstart = now16();
  MARK(32);
  const int bid = blockIdx.x;
  const int xcd = bid & 7, j = bid >> 3;
  const int h = xcd * 4 + (j >> 3), b = (j >> 2) & 1, s = j & 3;
  const int tid = threadIdx.x, lane = tid & 63, w = __builtin_amdgcn_readfirstlane(tid >> 6);
  const int g = lane >> 4, c = lane & 15;
  const unsigned long long tptr = ((const CAS unsigned long long*)__builtin_amdgcn_kernarg_segment_ptr())[2 + w];
  if (tid < 2) *(v4u*)(ldsC + 4 * tid) = v4u{0u, 0u, 0u, 0u};
  asm volatile("s_waitcnt lgkmcnt(0)" :: "s"(tptr), "s"(a.k), "s"(a.x), "s"(a.wq), "s"(tstart) : "memory");
  const int r8 = j & 7;
  const int fb = xcd * 3 + (j >> 3);
  const bool has_f1 = (j < 24) && (fb < 22);
  const bool is_fin = j >= 24;
  const int cid = xcd * 8 + (j - 24);
  const bool has_f2 = is_fin && cid < NFFN;
  const __amdgpu_buffer_rsrc_t rs = __builtin_amdgcn_make_buffer_rsrc((void*)a.ws, 0, WS_END, 0x00020000);

  const int row0 = 64 * r8 + 8 * w + 2 * g, lo16 = lane * 16;
  v4u sw0, sw1;
  {
    const GAS v4u* p = (const GAS v4u*)(a.wq + (size_t)row0 * (H * DH) + h * DH + 8 * c);
    sw0 = ldp<WNT_>(p); sw1 = ldp<WNT_>(p + H * DH / 8);
  }
  const unsigned xst = *(const GAS unsigned*)(a.x + (lane >> 5) * D + 64 * w + 2 * (lane & 31));
  const unsigned long long ep = __hip_atomic_load((const GAS unsigned long long*)(tptr + ((unsigned)(b * H + h) * ((0x82u >> w) & 1u) << 21) + ((unsigned)h * ((0x04u >> w) & 1u) << 17)),
                                                  __ATOMIC_RELAXED, __HIP_MEMORY_SCOPE_AGENT);
  v4u xq0, xq1;
  asm volatile("s_load_dwordx4 %0, %2, 0x0\n\ts_load_dwordx4 %1, %2, 0x400" : "=&s"(xq0), "=&s"(xq1) : "s"(a.x + 64 * r8 + 8 * w));
  const __amdgpu_buffer_rsrc_t rf = __builtin_amdgcn_make_buffer_rsrc((void*)(!has_f1 ? a.wd : fb < 11 ? a.wg : a.wu), 0, (has_f1 || has_f2) ? D * F * 2 : 0, 0x00020000);
  const int fcol = 128 * (fb < 11 ? fb : fb - 11) + 8 * c;
  const int fxo = !has_f1 ? (32 * cid + 4 * w) * D * 2 + lo16 : fcol < F ? (row0 * F + fcol) * 2 : OOB - 4 * F * 2, fxo2 = has_f1 ? OOB - 4096 : fxo;
  const __amdgpu_buffer_rsrc_t rw = __builtin_amdgcn_make_buffer_rsrc((void*)(a.wo + (size_t)h * DH * D), 0, DH * D * 2, 0x00020000);
  SB();
  __builtin_amdgcn_s_barrier();
  v4u ffw[4];
  ffw[0] = __builtin_amdgcn_raw_buffer_load_b128(rf, fxo, 0, WNT_ ? 2 : 0);
  ffw[1] = __builtin_amdgcn_raw_buffer_load_b128(rf, fxo, has_f1 ? F * 2 : D * 2, WNT_ ? 2 : 0);
  const v4u pf0 = __builtin_amdgcn_raw_buffer_load_b128(rw, lo16, (16 * r8 + 2 * w) * 1024, 0), pf1 = __builtin_amdgcn_raw_buffer_load_b128(rw, lo16 + 1024, (16 * r8 + 2 * w) * 1024, 0);
  ffw[2] = __builtin_amdgcn_raw_buffer_load_b128(rf, fxo2 + 2048, 0, WNT_ ? 2 : 0);
  ffw[3] = __builtin_amdgcn_raw_buffer_load_b128(rf, fxo2 + 3072, 0, WNT_ ? 2 : 0);
  SB();

  const __amdgpu_buffer_rsrc_t rk = __builtin_amdgcn_make_buffer_rsrc((void*)(a.k + (size_t)(b * H + h) * SK * DH), 0, SK * DH * 2, 0x00020000);
  const __amdgpu_buffer_rsrc_t rv = __builtin_amdgcn_make_buffer_rsrc((void*)(a.v + (size_t)(b * H + h) * SK * DH), 0, SK * DH * 2, 0x00020000);
  const int kvo = ((w & 1) << 13) | ((w >> 1) << 15) | ((s & 1) << 12) | ((s >> 1) << 14);
#define GOFF(gi_) ((((gi_) & 3) << 17) | (((gi_) & 4) ? (1 << 19) | (1 << 14) : 0) | (((gi_) & 8) ? (1 << 20) | (1 << 12) : 0))
  v4u rg[2 * GROUPS][U];

  constexpr float C2 = 0.08838834764831845f * 1.4426950408889634f;
  float sp[4] = {0.f, 0.f, 0.f, 0.f};
  float mw = 0.f, acc[8];
#pragma unroll
  for (int k = 0; k < 8; ++k) acc[k] = 0.f;
  unsigned tagA, tagB, qp[4];
  float mean0, rstd0, mean1, rstd1, n00, n01, n10, n11;
  v2u wor[32]; v4u wb[4][4], fu, qu, mu, e0, e1; unsigned short xo; float gte = 0.f;
  asm volatile("s_waitcnt lgkmcnt(0)" : "+s"(xq0), "+s"(xq1));
  const unsigned xs0 = sel4(xq0, g), xs1 = sel4(xq1, g);
  int fuoff;
  { const int fbs = (g >> 1) * 11 + (cid >> 2), hs = (fbs / 3) * 4 + fbs % 3;
    fuoff = has_f2 ? WS_S1 + ((hs * 8 + w) * 256 + (2 + (g & 1)) * 64 + 16 * (cid & 3) + c) * 16 : OOB; }
  const int wvo = g * 8192 + w * 128 + c * 8;
  const int qoff = WS_S1 + ((h * 8 + w) * 256 + b * 64 + lane) * 16;
  const int pid = (b * H + h) * NSPLIT + s, muoff = WS_ML + ((pid & ~3) + (lane & 3)) * 16;
  const int bc = (cid >> 5) & 1, cu0 = (cid & 31) * 8;
  const int gp = tid >> 2, qd = tid & 3, ocol = bc * D + ucol(cu0 + (c >> 1)) + (c & 1);
  const int dpoff = WS_DP + ((gp < NFFN ? gp : 0) * 512 + bc * 256 + cu0 + 2 * qd) * 16;

  auto stats = [&]() __attribute__((always_inline)) {
    f32x4 st[8];
#pragma unroll
    for (int i = 0; i < 8; ++i) st[i] = *(const f32x4*)(ldsS + 4 * i);
    tagA = __builtin_amdgcn_readfirstlane(ldsC[8]) + 1u; tagB = __builtin_amdgcn_readfirstlane(ldsC[9]) + 0x9E3779B9u;
    mean0 = ((st[0][0] + st[0][2]) + (st[1][0] + st[1][2]) + (st[2][0] + st[2][2]) + (st[3][0] + st[3][2])) * 0.125f;
    mean1 = ((st[4][0] + st[4][2]) + (st[5][0] + st[5][2]) + (st[6][0] + st[6][2]) + (st[7][0] + st[7][2])) * 0.125f;
    float q0 = 0.f, q1 = 0.f, d0 = 0.f, d1 = 0.f;
#pragma unroll
    for (int i = 0; i < 4; ++i) {
      q0 += st[i][1] + st[i][3]; q1 += st[4 + i][1] + st[4 + i][3];
      const float a0 = st[i][0] - mean0, b0 = st[i][2] - mean0, a1 = st[4 + i][0] - mean1, b1 = st[4 + i][2] - mean1;
      d0 += fmaf(a0, a0, b0 * b0); d1 += fmaf(a1, a1, b1 * b1);
    }
    rstd0 = 1.f / sqrtf(fmaf(64.f, d0, q0) * (1.f / D) + 1e-5f);
    rstd1 = 1.f / sqrtf(fmaf(64.f, d1, q1) * (1.f / D) + 1e-5f);
  };
  auto qk = [&](auto gic) __attribute__((always_inline)) {
    constexpr int gi = decltype(gic)::value;
    float d[U];
#pragma unroll
    for (int u = 0; u < U; ++u) d[u] = dot2(rg[gi][u][3], qp[3], dot2(rg[gi][u][2], qp[2], dot2(rg[gi][u][1], qp[1], dot2(rg[gi][u][0], qp[0], 0.f))));
    const bool o1 = c & 1, o2 = c & 2;
    const float x0 = (o1 ? d[1] : d[0]) + dpp<0xB1>(o1 ? d[0] : d[1]), x1 = (o1 ? d[3] : d[2]) + dpp<0xB1>(o1 ? d[2] : d[3]);
    float x = (o2 ? x1 : x0) + dpp<0x4E>(o2 ? x0 : x1);
    x += dpp<0x124>(x); x += dpp<0x128>(x);
    sp[gi / 4] = __builtin_bit_cast(float, __builtin_amdgcn_update_dpp(__builtin_bit_cast(int, sp[gi / 4]), __builtin_bit_cast(int, x), 0xE4, 0xf, 1 << (gi % 4), false));
    asm volatile("" : "+v"(sp[gi / 4]));
    SB();
  };
  auto pv = [&](auto gic) __attribute__((always_inline)) {
    constexpr int gi = decltype(gic)::value;
    const float p0 = dpp<0x150 + 4 * (gi % 4)>(sp[gi / 4]), p1 = dpp<0x151 + 4 * (gi % 4)>(sp[gi / 4]), p2 = dpp<0x152 + 4 * (gi % 4)>(sp[gi / 4]), p3 = dpp<0x153 + 4 * (gi % 4)>(sp[gi / 4]);
    const float p[4] = {p0, p1, p2, p3};
#pragma unroll
    for (int u = 0; u < U; ++u) {
#pragma unroll
      for (int k = 0; k < 4; ++k) { acc[2 * k] = fmaf(p[u], lo(rg[GROUPS + gi][u][k]), acc[2 * k]); acc[2 * k + 1] = fmaf(p[u], hi(rg[GROUPS + gi][u][k]), acc[2 * k + 1]); }
    }
    asm volatile("" : "+v"(acc[0]), "+v"(acc[1]), "+v"(acc[2]), "+v"(acc[3]), "+v"(acc[4]), "+v"(acc[5]), "+v"(acc[6]), "+v"(acc[7]));
    SB();
  };

  {
    float sm = row16_sum(lo(xst) + hi(xst)); sm = fold16(sm, sm) * (1.f / 64);
    const float d0 = lo(xst) - sm, d1 = hi(xst) - sm;
    float m2 = row16_sum(fmaf(d0, d0, d1 * d1)); m2 = fold16(m2, m2);
    if ((lane & 31) == 0) *(f32x2*)(ldsS + (lane >> 5) * 16 + 2 * w) = f32x2{sm, m2};
    float t[24];
#pragma unroll
    for (int k = 0; k < 4; ++k) {
      const float al = lo(sw0[k]), ah = hi(sw0[k]), bl = lo(sw1[k]), bh = hi(sw1[k]);
      t[2 * k] = fmaf(hi(xs0), bl, lo(xs0) * al); t[2 * k + 1] = fmaf(hi(xs0), bh, lo(xs0) * ah);
      t[8 + 2 * k] = fmaf(hi(xs1), bl, lo(xs1) * al); t[8 + 2 * k + 1] = fmaf(hi(xs1), bh, lo(xs1) * ah);
      t[16 + 2 * k] = al + bl; t[16 + 2 * k + 1] = ah + bh;
    }
#pragma unroll
    for (int k = 0; k < 8; ++k) t[k] = fold16(fold32(t[k], t[16 + k]), fold32(t[8 + k], t[16 + k]));
    *(f32x4*)(ldsY + w * 512 + g * 128 + 8 * c) = f32x4{t[0], t[1], t[2], t[3]};
    *(f32x4*)(ldsY + w * 512 + g * 128 + 8 * c + 4) = f32x4{t[4], t[5], t[6], t[7]};
    if (tid == 0) *(unsigned long long*)(ldsC + 8) = ep;
    asm volatile("" :: "v"(ep));
    if (last_wave(ldsC, lane)) {
      stats();
      f32x2 z0 = {0.f, 0.f}, z1 = {0.f, 0.f}, cs = {0.f, 0.f};
#pragma unroll
      for (int ww = 0; ww < NWAVE; ++ww) {
        z0 += *(const f32x2*)(ldsY + ww * 512 + 2 * lane); z1 += *(const f32x2*)(ldsY + ww * 512 + 128 + 2 * lane); cs += *(const f32x2*)(ldsY + ww * 512 + 256 + 2 * lane); }
      st_unit(rs, WS_S1 + ((h * 8 + r8) * 256 + lane) * 16, (z0[0] - mean0 * cs[0]) * rstd0, (z0[1] - mean0 * cs[1]) * rstd0, tagA, tagB);
      st_unit(rs, WS_S1 + ((h * 8 + r8) * 256 + 64 + lane) * 16, (z1[0] - mean1 * cs[0]) * rstd1, (z1[1] - mean1 * cs[1]) * rstd1, tagA, tagB);
    }
  }
  MARK(33);
  unsigned due = tstart + (unsigned)T0X + (unsigned)(w * TPX / 8);

  auto point = [&](auto pc) __attribute__((always_inline)) {
    constexpr int p = decltype(pc)::value;
    PACE(p);
    SB();
#pragma unroll
    for (int u = 0; u < U; ++u) rg[p][u] = __builtin_amdgcn_raw_buffer_load_b128(p < GROUPS ? rk : rv, lo16 + u * 1024, kvo ^ GOFF(p % GROUPS), 2);
    SB();
    if constexpr (p == 3) qu = ld_unit(rs, qoff);
    if constexpr (p == 8) fu = ld_unit(rs, fuoff);
    if constexpr (p == 29) mu = ld_unit(rs, muoff);
#pragma unroll
    for (int i = won(p); i < won(p + 1); ++i) wor[i] = __builtin_amdgcn_raw_buffer_load_b64(rw, wvo, (32 * (i >> 3) + (i & 7)) * 1024, 0);
    if constexpr (p == 22) {
      e0 = ld_unit(rs, is_fin ? dpoff : OOB); e1 = ld_unit(rs, is_fin ? dpoff + 16 : OOB);
      xo = ((const GAS unsigned short*)a.x)[ocol];
    }
    SB();
    if constexpr (p == 1) {
      wait_all(ldsC);
      stats();
      n00 = (lo(xs0) - mean0) * rstd0; n01 = (hi(xs0) - mean0) * rstd0; n10 = (lo(xs1) - mean1) * rstd1; n11 = (hi(xs1) - mean1) * rstd1;
      asm volatile("" : "+v"(n00), "+v"(n01), "+v"(n10), "+v"(n11));
    }
    if constexpr (p == 2) {
      asm volatile("" :: "v"(pf0), "v"(pf1));
      if (has_f1) {
        float t[8];
#pragma unroll
        for (int k = 0; k < 4; ++k) {
          const float al = lo(ffw[0][k]), ah = hi(ffw[0][k]), bl = lo(ffw[1][k]), bh = hi(ffw[1][k]);
          t[2 * k] = fold32(fmaf(n01, bl, n00 * al), fmaf(n11, bl, n10 * al)); t[2 * k + 1] = fold32(fmaf(n01, bh, n00 * ah), fmaf(n11, bh, n10 * ah));
        }
        *(f32x4*)(ldsY + 4096 + w * 256 + (g >> 1) * 128 + 8 * c + 4 * (g & 1)) = f32x4{fold16(t[0], t[4]), fold16(t[1], t[5]), fold16(t[2], t[6]), fold16(t[3], t[7])};
        last_wave(ldsC + 2, lane);
      }
    }
    if constexpr (p == 4) {
      if (has_f1 && w < 2) {
        wait_all(ldsC + 2);
        f32x2 z = {0.f, 0.f};
#pragma unroll
        for (int ww = 0; ww < NWAVE; ++ww) z += *(const f32x2*)(ldsY + 4096 + ww * 256 + 2 * tid);
        st_unit(rs, WS_S1 + ((h * 8 + r8) * 256 + 128 + tid) * 16, z[0], z[1], tagA, tagB);
      }
    }
    if constexpr (p == 5) {
      await_unit(qu, rs, qoff, tagA, tagB);
      *(f32x2*)(ldsQ + w * 128 + 2 * lane) = f32x2{__uint_as_float(qu[0]), __uint_as_float(qu[2])};
      last_wave(ldsC + 1, lane);
    }
    if constexpr (p == 7) {
      wait_all(ldsC + 1);
      f32x4 q0 = {0, 0, 0, 0}, q1 = {0, 0, 0, 0};
#pragma unroll
      for (int ww = 0; ww < NWAVE; ++ww) { q0 += *(const f32x4*)(ldsQ + ww * 128 + 8 * c); q1 += *(const f32x4*)(ldsQ + ww * 128 + 8 * c + 4); }
      qp[0] = pk_bf16(q0[0], q0[1]); qp[1] = pk_bf16(q0[2], q0[3]); qp[2] = pk_bf16(q1[0], q1[1]); qp[3] = pk_bf16(q1[2], q1[3]);
      qk(IC<0>{});
    }
    if constexpr (p == 8) { qk(IC<1>{}); qk(IC<2>{}); qk(IC<3>{}); }
    if constexpr (p == 9) qk(IC<4>{});
    if constexpr (p >= 9 && p < GROUPS + 4) qk(IC<p - 4>{});
    if constexpr (p == 11) {
      if (has_f2) {
        await_unit(fu, rs, fuoff, tagA, tagB);
        *(f32x2*)(ldsF + w * 128 + g * 32 + 2 * c) = f32x2{__uint_as_float(fu[0]), __uint_as_float(fu[2])};
        last_wave(ldsC + 5, lane);
      }
    }
    if constexpr (p == 13) {
      if (has_f2) {
        wait_all(ldsC + 5);
        float gs = 0.f, us = 0.f;
        const int hx = ((lane >> 2) & 1) * 32 + 4 * w + (lane & 3);
#pragma unroll
        for (int ww = 0; ww < NWAVE; ++ww) { gs += ldsF[ww * 128 + hx]; us += ldsF[ww * 128 + 64 + hx]; }
        const float hv = gelu_tanh(gs) * us;
        float dd[16];
#pragma unroll
        for (int k = 0; k < 16; ++k) dd[k] = 0.f;
#pragma unroll
        for (int r = 0; r < 4; ++r) {
          const float h0 = __builtin_bit_cast(float, __builtin_amdgcn_readlane(__builtin_bit_cast(int, hv), r));
          const float h1 = __builtin_bit_cast(float, __builtin_amdgcn_readlane(__builtin_bit_cast(int, hv), 4 + r));
#pragma unroll
          for (int k = 0; k < 4; ++k) {
            const float wl = lo(ffw[r][k]), wh = hi(ffw[r][k]);
            dd[2 * k] = fmaf(h0, wl, dd[2 * k]); dd[2 * k + 1] = fmaf(h0, wh, dd[2 * k + 1]);
            dd[8 + 2 * k] = fmaf(h1, wl, dd[8 + 2 * k]); dd[8 + 2 * k + 1] = fmaf(h1, wh, dd[8 + 2 * k + 1]);
          }
        }
        *(f32x4*)(ldsY + w * 1024 + 8 * lane) = f32x4{dd[0], dd[1], dd[2], dd[3]};
        *(f32x4*)(ldsY + w * 1024 + 8 * lane + 4) = f32x4{dd[4], dd[5], dd[6], dd[7]};
        *(f32x4*)(ldsY + w * 1024 + 512 + 8 * lane) = f32x4{dd[8], dd[9], dd[10], dd[11]};
        *(f32x4*)(ldsY + w * 1024 + 512 + 8 * lane + 4) = f32x4{dd[12], dd[13], dd[14], dd[15]};
        last_wave(ldsC + 3, lane);
      }
    }
    if constexpr (p == 15) {
      if (has_f2) {
        wait_all(ldsC + 3);
        f32x2 dv = {0.f, 0.f};
#pragma unroll
        for (int ww = 0; ww < NWAVE; ++ww) dv += *(const f32x2*)(ldsY + ww * 1024 + (tid & 256) * 2 + ucol(tid & 255));
        st_unit(rs, WS_DP + (cid * 512 + tid) * 16, dv[0], dv[1], tagA, tagB);
      }
    }
    if constexpr (p == 25) {
      if (is_fin) {
        await_unit(e0, rs, dpoff, tagA, tagB);
        await_unit(e1, rs, dpoff + 16, tagA, tagB);
        const bool hd = gp < NFFN;
        const float r = fold4(hd ? __uint_as_float(e0[0]) : 0.f, hd ? __uint_as_float(e0[2]) : 0.f, hd ? __uint_as_float(e1[0]) : 0.f, hd ? __uint_as_float(e1[2]) : 0.f);
        if (c < 4) ldsF[w * 16 + 4 * c + g] = r;
        last_wave(ldsC + 2, lane);
      }
    }
    if constexpr (p == GROUPS + 3) {
      float x = fmaxf(fmaxf(sp[0], sp[1]), fmaxf(sp[2], sp[3]));
      x = fmaxf(x, dpp<0x128>(x)); x = fmaxf(x, dpp<0x124>(x)); x = fmaxf(x, dpp<0x122>(x)); x = fmaxf(x, dpp<0x121>(x));
      mw = xrow_max(x) * C2;
#pragma unroll
      for (int i = 0; i < 4; ++i) sp[i] = __builtin_amdgcn_exp2f(fmaf(sp[i], C2, -mw));
      const float lw = xrow_sum(row16_sum((sp[0] + sp[1]) + (sp[2] + sp[3])));
      if (lane == 0) { ldsM[w] = mw; ldsM[8 + w] = lw; }
      last_wave(ldsC + 4, lane);
    }
    if constexpr (p == 21) {
      if (w == 0) {
        wait_all(ldsC + 4);
        const float mi = ldsM[lane & 7], li = ldsM[8 + (lane & 7)];
        float M = fmaxf(mi, dpp<0x124>(mi)); M = fmaxf(M, dpp<0x122>(M)); M = fmaxf(M, dpp<0x121>(M));
        float L = __builtin_amdgcn_exp2f(mi - M) * li; L += dpp<0x124>(L); L += dpp<0x122>(L); L += dpp<0x121>(L);
        if (lane == 0) st_unit(rs, WS_ML + pid * 16, M, L, tagA, tagB);
      }
    }
    if constexpr (p == 27) {
      if (is_fin && w == 0) {
        wait_all(ldsC + 2);
        const float dv = ldsF[c] + ldsF[16 + c] + ldsF[32 + c];
        gte = dv / (1.f + __expf(-dv));
        asm volatile("" : "+v"(gte));
      }
    }
    if constexpr (p == 31) {
      await_unit(mu, rs, muoff, tagA, tagB);
      const float mi = __uint_as_float(mu[0]);
      float M = fmaxf(mi, dpp<0xB1>(mi)); M = fmaxf(M, dpp<0x4E>(M));
      float L = __builtin_amdgcn_exp2f(mi - M) * __uint_as_float(mu[2]); L += dpp<0xB1>(L); L += dpp<0x4E>(L);
      mw = __builtin_amdgcn_exp2f(mw - M) / L;
      asm volatile("" : "+v"(mw));
    }
    if constexpr (p >= 20 && p < 28) pv(IC<p - 20>{});
    if constexpr (p == 28 || p == 29) { pv(IC<2 * p - 48>{}); pv(IC<2 * p - 47>{}); }
    if constexpr (p == 30) pv(IC<12>{});
    if constexpr (p == 31) { pv(IC<13>{}); pv(IC<14>{}); pv(IC<15>{}); }
    if constexpr (p == 30) {
#pragma unroll
      for (int i = 0; i < 16; ++i) {
        const v2u r0 = wor[2 * i], r1 = wor[2 * i + 1];
        wb[0][i >> 2][i & 3] = __builtin_amdgcn_perm(r1[0], r0[0], 0x05040100u); wb[1][i >> 2][i & 3] = __builtin_amdgcn_perm(r1[0], r0[0], 0x07060302u);
        wb[2][i >> 2][i & 3] = __builtin_amdgcn_perm(r1[1], r0[1], 0x05040100u); wb[3][i >> 2][i & 3] = __builtin_amdgcn_perm(r1[1], r0[1], 0x07060302u);
      }
#pragma unroll
      for (int i = 0; i < 4; ++i) asm volatile("" : "+v"(wb[i][0]), "+v"(wb[i][1]), "+v"(wb[i][2]), "+v"(wb[i][3]));
    }
    SB();
  };
  static_for<2 * GROUPS>(point);
  MARK(34);

  {
    const f32x2 o2 = fold8(acc) * mw;
    const unsigned oh = pk_bf16(o2[0], o2[1]), ol = pk_bf16(o2[0] - lo(oh), o2[1] - hi(oh));
    ldsA[w * 68 + 4 * c + g] = oh; ldsA[(8 + w) * 68 + 4 * c + g] = ol;
  }
  __syncthreads();
  MARK(35);
  {
    v4u af[4];
#pragma unroll
    for (int ks = 0; ks < 4; ++ks) af[ks] = *(const v4u*)(ldsA + c * 68 + 16 * ks + 4 * g);
    f32x4 d[4];
#pragma unroll
    for (int t = 0; t < 4; ++t) d[t] = f32x4{0.f, 0.f, 0.f, 0.f};
#pragma unroll
    for (int ks = 0; ks < 4; ++ks)
#pragma unroll
      for (int t = 0; t < 4; ++t) d[t] = __builtin_amdgcn_mfma_f32_16x16x32_bf16(__builtin_bit_cast(bf16x8, af[ks]), __builtin_bit_cast(bf16x8, wb[t][ks]), d[t], 0, 0, 0);
    float y[4];
#pragma unroll
    for (int t = 0; t < 4; ++t) y[t] = (d[t][0] + d[t][1]) + (d[t][2] + d[t][3]);
    float u0 = fold32(y[0], y[2]), u1 = fold32(y[1], y[3]);
    u0 = fold16(u0, u0); u1 = fold16(u1, u1);
    if ((g & 1) == 0) st_unit(rs, WS_Y + (pid * 256 + 32 * w + 16 * (g >> 1) + c) * 16, u0, u1, tagA, tagB);
  }
  MARK(36);

  if (is_fin) {
    const int yoff = WS_Y + ((bc * 128 + gp) * 256 + cu0 + 2 * qd) * 16;
    __builtin_amdgcn_s_barrier();
    if (PDLY_) __builtin_amdgcn_s_sleep(PDLY_);
    MARK(38);
    v4u y0, y1;
    bool ok = false;
#pragma unroll 1
    for (unsigned spin = 0; spin < SPIN_CAP; ++spin) {
      asm volatile("" ::: "memory");
      if (!ok) {
        y0 = ld_unit(rs, yoff); y1 = ld_unit(rs, yoff + 16);
        ok = (((y0[1] ^ tagA) | (y1[1] ^ tagA)) | ((y0[3] ^ tagB) | (y1[3] ^ tagB))) == 0u;
      }
      if (__all(ok)) break;
      __builtin_amdgcn_s_sleep(1);
    }
    MARK(39);
    const float r = fold4(__uint_as_float(y0[0]), __uint_as_float(y0[2]), __uint_as_float(y1[0]), __uint_as_float(y1[2]));
    if (c < 4) ldsF[128 + w * 16 + 4 * c + g] = r;
    __syncthreads();
    if (tid < 16) {
      float sv = 0.f;
#pragma unroll
      for (int ww = 0; ww < NWAVE; ++ww) sv += ldsF[128 + ww * 16 + tid];
      const float o = __uint_as_float((unsigned)xo << 16) + sv * gte;
      const __bf16 ob = (__bf16)o;
      ((GAS unsigned short*)a.out)[ocol] = __builtin_bit_cast(unsigned short, ob);
      if (cid == 0 && tid == 0)
        __hip_atomic_store((GAS unsigned long long*)(a.ws + WS_EPOCH), ((unsigned long long)tagB << 32) | tagA, __ATOMIC_RELAXED, __HIP_MEMORY_SCOPE_AGENT);
    }
  }
}

extern "C" void kernel_launch(void* const* d_in, const int* in_sizes, int n_in, void* d_out, int out_size, void* d_ws, size_t ws_size,
                              hipStream_t stream) {
  Args a;
  a.x = (const unsigned short*)d_in[0];
  a.k = (const unsigned short*)d_in[1];
  a.v = (const unsigned short*)d_in[2];
  a.wq = (const unsigned short*)d_in[3];
  a.wo = (const unsigned short*)d_in[4];
  a.wg = (const unsigned short*)d_in[6];
  a.wu = (const unsigned short*)d_in[7];
  a.wd = (const unsigned short*)d_in[8];
  a.out = (unsigned short*)d_out;
  a.ws = (unsigned char*)d_ws;
  hipLaunchKernelGGL(k_all, dim3(256), dim3(NTHR), 0, stream, a);
}
```

```cpp
#include <hip/hip_runtime.h>
#include <hip/hip_bf16.h>
#include <stdint.h>

typedef unsigned v4u __attribute__((ext_vector_type(4)));
typedef float f32x4 __attribute__((ext_vector_type(4)));
typedef float f32x2 __attribute__((ext_vector_type(2)));
typedef __bf16 bf16x2 __attribute__((ext_vector_type(2)));
typedef __bf16 bf16x8 __attribute__((ext_vector_type(8)));
typedef unsigned v2u __attribute__((ext_vector_type(2)));
#define GAS __attribute__((address_space(1)))
#define CAS __attribute__((address_space(4)))

constexpr int D = 512, H = 32, SK = 8192, DH = 128, F = 1376;
constexpr int NSPLIT = 4, KEYS_PER_WG = SK / NSPLIT;
constexpr int NWAVE = 8, NTHR = NWAVE * 64;
constexpr int NFFN = F / 32;
constexpr int U = 4;
constexpr int GROUPS = KEYS_PER_WG / NWAVE / (4 * U);
#define PWO_ 1
constexpr int won(int p) { return p <= PWO_ ? 0 : p >= 28 ? 32 : (p - PWO_) * 32 / (28 - PWO_); }
#define ABLWO_ 0
#define WOAUX_ 0
#define GAP_ 0
#define GAPAT(p) ((p) == 4 || (p) == 18)
#define PDLY_ 8
#define LATE_PRIO_ 400
#define KNT_ 1
#define VNT_ 1
#define WNT_ 1
#define ONT_ 0
#define TP_ 1780
#define T0_ 4000

constexpr int WS_EPOCH = 0;
constexpr int WS_ML = 4096;
constexpr int WS_Y = WS_ML + 256 * 16;
constexpr int WS_DP = WS_Y + 256 * 256 * 16;
constexpr int WS_S1 = WS_DP + NFFN * 512 * 16;
constexpr int WS_END = WS_S1 + 32 * 8 * 4 * 64 * 16;
constexpr int OOB = 0x7ffffff0;
constexpr unsigned SPIN_CAP = 400000;

#define SB() __builtin_amdgcn_sched_barrier(0)

__device__ __forceinline__ float dot2(unsigned k, unsigned q, float acc) {
  return __builtin_amdgcn_fdot2_f32_bf16(__builtin_bit_cast(bf16x2, k), __builtin_bit_cast(bf16x2, q), acc, false);
}
template <int CTRL> __device__ __forceinline__ float dpp(float x) {
  return __builtin_bit_cast(float, __builtin_amdgcn_mov_dpp(__builtin_bit_cast(int, x), CTRL, 0xf, 0xf, true));
}
__device__ __forceinline__ float row16_sum(float x) {
  x += dpp<0x128>(x);
  x += dpp<0x124>(x);
  x += dpp<0x122>(x);
  x += dpp<0x121>(x);
  return x;
}
__device__ __forceinline__ float fold32(float a, float b) {
  auto t = __builtin_amdgcn_permlane32_swap(__float_as_uint(a), __float_as_uint(b), false, false);
  return __uint_as_float(t[0]) + __uint_as_float(t[1]);
}
__device__ __forceinline__ float fold16(float a, float b) {
  auto t = __builtin_amdgcn_permlane16_swap(__float_as_uint(a), __float_as_uint(b), false, false);
  return __uint_as_float(t[0]) + __uint_as_float(t[1]);
}
__device__ __forceinline__ float xrow_sum(float x) { x = fold16(x, x); return fold32(x, x); }
__device__ __forceinline__ float xrow_max(float x) {
  auto s = __builtin_amdgcn_permlane16_swap(__float_as_uint(x), __float_as_uint(x), false, false);
  x = fmaxf(__uint_as_float(s[0]), __uint_as_float(s[1]));
  auto t = __builtin_amdgcn_permlane32_swap(__float_as_uint(x), __float_as_uint(x), false, false);
  return fmaxf(__uint_as_float(t[0]), __uint_as_float(t[1]));
}
__device__ __forceinline__ f32x2 fold8(const float (&v)[8]) {
  const float u0 = fold32(v[0], v[4]), u1 = fold32(v[1], v[5]), u2 = fold32(v[2], v[6]), u3 = fold32(v[3], v[7]);
  return f32x2{fold16(u0, u2), fold16(u1, u3)};
}
__device__ __forceinline__ float fold4(float v0, float v1, float v2, float v3) {
  float r = fold16(fold32(v0, v2), fold32(v1, v3));
  r += dpp<0x124>(r); r += dpp<0x128>(r);
  return r;
}
__device__ __forceinline__ float lo(unsigned u) { return __uint_as_float(u << 16); }
__device__ __forceinline__ float hi(unsigned u) { return __uint_as_float(u & 0xffff0000u); }
__device__ __forceinline__ unsigned pk_bf16(float a, float b) {
  bf16x2 r = {(__bf16)a, (__bf16)b};
  return __builtin_bit_cast(unsigned, r);
}
__device__ __forceinline__ unsigned sel4(v4u v, int g) { const unsigned a = (g & 1) ? v[1] : v[0], b = (g & 1) ? v[3] : v[2]; return (g & 2) ? b : a; }
__device__ __forceinline__ int ucol(int u) { return 64 * (u >> 5) + 4 * (u & 15) + 2 * ((u >> 4) & 1); }
template <int NT> __device__ __forceinline__ v4u ldp(const GAS v4u* p) { if (NT) return __builtin_nontemporal_load(p); else return *p; }
__device__ __forceinline__ float gelu_tanh(float x) {
  const float u = 0.7978845608028654f * (x + 0.044715f * x * x * x);
  const float e = __expf(2.f * u);
  const float t = 1.f - 2.f * __builtin_amdgcn_rcpf(e + 1.f);
  return 0.5f * x * (1.f + t);
}
__device__ __forceinline__ void pace_wait(unsigned& due16, unsigned step16) {
  asm volatile("1:\n\ts_memrealtime s[90:91]\n\ts_waitcnt lgkmcnt(0)\n\ts_lshl_b32 s90, s90, 4\n\ts_sub_u32 s91, s90, %0\n\ts_cmp_ge_i32 s91, 0\n\ts_cbranch_scc1 2f\n\ts_sleep 2\n\ts_branch 1b\n"
               "2:\n\ts_add_u32 %0, %0, %1\n\ts_setprio 0\n\ts_cmp_lt_i32 s91, %2\n\ts_cbranch_scc1 3f\n\ts_setprio 2\n3:"
               : "+s"(due16) : "s"(step16), "i"(LATE_PRIO_) : "s90", "s91", "scc");
}
__device__ __forceinline__ unsigned now16() { return (unsigned)__builtin_amdgcn_s_memrealtime() << 4; }
#define MARK(i)
#define TPX TP_
#define T0X T0_
#define FLG 0
#define SPRD 0
#define PFC_ 1
#define GAPX GAP_
#define PACE(pt) pace_wait(due, (unsigned)TPX)
constexpr int AUX_SC1 = 16;
__device__ __forceinline__ void st_unit(__amdgpu_buffer_rsrc_t rs, int off, float v0, float v1, unsigned ta, unsigned tb) {
  __builtin_amdgcn_raw_buffer_store_b128(v4u{__float_as_uint(v0), ta, __float_as_uint(v1), tb}, rs, off, 0, AUX_SC1);
}
__device__ __forceinline__ v4u ld_unit(__amdgpu_buffer_rsrc_t rs, int off) { return __builtin_amdgcn_raw_buffer_load_b128(rs, off, 0, AUX_SC1); }
__device__ __forceinline__ void await_unit(v4u& u, __amdgpu_buffer_rsrc_t rs, int off, unsigned ta, unsigned tb) {
  if (!__all(((u[1] ^ ta) | (u[3] ^ tb)) == 0u)) {
#pragma unroll 1
    for (unsigned spin = 0; spin < SPIN_CAP; ++spin) {
      __builtin_amdgcn_s_sleep(1);
      asm volatile("" ::: "memory");
      u = ld_unit(rs, off);
      if (__all(((u[1] ^ ta) | (u[3] ^ tb)) == 0u)) break;
    }
  }
}

__device__ __forceinline__ bool last_wave(unsigned* cnt, int lane) {
  asm volatile("s_waitcnt lgkmcnt(0)" ::: "memory");
  unsigned old = 0;
  if (lane == 0) old = __hip_atomic_fetch_add(cnt, 1u, __ATOMIC_RELAXED, __HIP_MEMORY_SCOPE_WORKGROUP);
  old = __builtin_amdgcn_readfirstlane(old);
  asm volatile("" ::: "memory");
  return old == NWAVE - 1;
}

__device__ __forceinline__ void wait_all(unsigned* cnt, unsigned n = NWAVE) {
  while (__builtin_amdgcn_readfirstlane(__hip_atomic_load(cnt, __ATOMIC_RELAXED, __HIP_MEMORY_SCOPE_WORKGROUP)) < n) __builtin_amdgcn_s_sleep(1);
  asm volatile("" ::: "memory");
}
template <int I> struct IC { static constexpr int value = I; };
template <int N, int I = 0, typename Fn> __device__ __forceinline__ void static_for(Fn& fn) { if constexpr (I < N) { fn(IC<I>{}); static_for<N, I + 1>(fn); } }

struct Args {
  const unsigned short* x; const unsigned short* wq;
  unsigned char* ws; const unsigned short* v; const unsigned short* wo; const unsigned short* wg;
  const unsigned short* wu; const unsigned short* wd; unsigned short* out; const unsigned short* k;
};

__global__ void __launch_bounds__(NTHR, 2) k_all(Args a) {
  __shared__ __attribute__((aligned(16))) float lds[8 * 1024 + 16 * 68 + 16 + 8 * 128 + 8 * 128 + 32 + 16];
  float* ldsY = lds;
  unsigned* ldsA = (unsigned*)(lds + 8 * 1024);
  float* ldsM = lds + 8 * 1024 + 16 * 68;
  float* ldsF = ldsM + 16;
  float* ldsQ = ldsF + 8 * 128;
  float* ldsS = ldsQ + 8 * 128;
  unsigned* ldsC = (unsigned*)(ldsS + 32);
  const unsigned tstart = now16();
  MARK(32);
  const int bid = blockIdx.x;
  const int xcd = bid & 7, j = bid >> 3;
  const int h = xcd * 4 + (j >> 3), b = (j >> 2) & 1, s = j & 3;
  const int tid = threadIdx.x, lane = tid & 63, w = __builtin_amdgcn_readfirstlane(tid >> 6);
  const int g = lane >> 4, c = lane & 15;
  const unsigned long long tptr = ((const CAS unsigned long long*)__builtin_amdgcn_kernarg_segment_ptr())[2 + w];
  if (tid < 2) *(v4u*)(ldsC + 4 * tid) = v4u{0u, 0u, 0u, 0u};
  asm volatile("s_waitcnt lgkmcnt(0)" :: "s"(tptr), "s"(a.k), "s"(a.x), "s"(a.wq), "s"(tstart) : "memory");
  const int r8 = j & 7;
  const int fb = xcd * 3 + (j >> 3);
  const bool has_f1 = (j < 24) && (fb < 22) && !(FLG & 1);
  const bool is_fin = j >= 24;
  const int cid = xcd * 8 + (j - 24);
  const bool has_f2 = is_fin && cid < NFFN && !(FLG & 1);
  const __amdgpu_buffer_rsrc_t rs = __builtin_amdgcn_make_buffer_rsrc((void*)a.ws, 0, WS_END, 0x00020000);

  const int row0 = 64 * r8 + 8 * w + 2 * g, lo16 = lane * 16;
  v4u sw0, sw1;
  {
    const GAS v4u* p = (const GAS v4u*)(a.wq + (size_t)row0 * (H * DH) + h * DH + 8 * c);
    sw0 = ldp<WNT_>(p); sw1 = ldp<WNT_>(p + H * DH / 8);
  }
  const unsigned xst = *(const GAS unsigned*)(a.x + (lane >> 5) * D + 64 * w + 2 * (lane & 31));
  const unsigned long long ep = __hip_atomic_load((const GAS unsigned long long*)(tptr + ((unsigned)(b * H + h) * ((0x82u >> w) & 1u) << 21) + ((unsigned)h * ((0x04u >> w) & 1u) << 17)),
                                                  __ATOMIC_RELAXED, __HIP_MEMORY_SCOPE_AGENT);
  v4u xq0, xq1;
  asm volatile("s_load_dwordx4 %0, %2, 0x0\n\ts_load_dwordx4 %1, %2, 0x400" : "=&s"(xq0), "=&s"(xq1) : "s"(a.x + 64 * r8 + 8 * w));
  const __amdgpu_buffer_rsrc_t rf = __builtin_amdgcn_make_buffer_rsrc((void*)(!has_f1 ? a.wd : fb < 11 ? a.wg : a.wu), 0, (has_f1 || has_f2) ? D * F * 2 : 0, 0x00020000);
  const int fcol = 128 * (fb < 11 ? fb : fb - 11) + 8 * c;
  const int fxo = !has_f1 ? (32 * cid + 4 * w) * D * 2 + lo16 : fcol < F ? (row0 * F + fcol) * 2 : OOB - 4 * F * 2, fxo2 = has_f1 ? OOB - 4096 : fxo;
  const __amdgpu_buffer_rsrc_t rw = __builtin_amdgcn_make_buffer_rsrc((void*)(a.wo + (size_t)h * DH * D), 0, DH * D * 2, 0x00020000);
  SB();
  __builtin_amdgcn_s_barrier();
  v4u ffw[4];
  ffw[0] = __builtin_amdgcn_raw_buffer_load_b128(rf, fxo, 0, WNT_ ? 2 : 0);
  ffw[1] = __builtin_amdgcn_raw_buffer_load_b128(rf, fxo, has_f1 ? F * 2 : D * 2, WNT_ ? 2 : 0);
  const v4u pf0 = __builtin_amdgcn_raw_buffer_load_b128(rw, lo16, (16 * r8 + 2 * w) * 1024, 0), pf1 = __builtin_amdgcn_raw_buffer_load_b128(rw, lo16 + 1024, (16 * r8 + 2 * w) * 1024, 0);
  unsigned long long pc0; unsigned csz;
  asm volatile("s_getpc_b64 %0\n.Lk_all_pc0:\n\ts_mov_b32 %1, .Lk_all_end-.Lk_all_pc0" : "=s"(pc0), "=s"(csz));
  const v4u pfc = __builtin_amdgcn_raw_buffer_load_b128(__builtin_amdgcn_make_buffer_rsrc((void*)(pc0 & ~127ull), 0, (w == 3 && PFC_) ? (csz & ~15u) : 0u, 0x00020000), lo16, j * 1024, 0);
  ffw[2] = __builtin_amdgcn_raw_buffer_load_b128(rf, fxo2 + 2048, 0, WNT_ ? 2 : 0);
  ffw[3] = __builtin_amdgcn_raw_buffer_load_b128(rf, fxo2 + 3072, 0, WNT_ ? 2 : 0);
  SB();

  const __amdgpu_buffer_rsrc_t rk = __builtin_amdgcn_make_buffer_rsrc((void*)(a.k + (size_t)(b * H + h) * SK * DH), 0, SK * DH * 2, 0x00020000);
  const __amdgpu_buffer_rsrc_t rv = __builtin_amdgcn_make_buffer_rsrc((void*)(a.v + (size_t)(b * H + h) * SK * DH), 0, SK * DH * 2, 0x00020000);
  const int kvo = ((w & 1) << 13) | ((w >> 1) << 15) | ((s & 1) << 12) | ((s >> 1) << 14);
#define GOFF(gi_) ((((gi_) & 3) << 17) | (((gi_) & 4) ? (1 << 19) | (1 << 14) : 0) | (((gi_) & 8) ? (1 << 20) | (1 << 12) : 0))
  v4u rg[2 * GROUPS][U];

  constexpr float C2 = 0.08838834764831845f * 1.4426950408889634f;
  float sp[4] = {0.f, 0.f, 0.f, 0.f};
  float mw = 0.f, acc[8];
#pragma unroll
  for (int k = 0; k < 8; ++k) acc[k] = 0.f;
  unsigned tagA, tagB, qp[4];
  v2u wor[32]; v4u wb[4][4], fu, qu, mu, e0, e1; unsigned short xo; float gte = 0.f;
  asm volatile("s_waitcnt lgkmcnt(0)" : "+s"(xq0), "+s"(xq1));
  const unsigned xs0 = sel4(xq0, g), xs1 = sel4(xq1, g);
  int fuoff;
  { const int fbs = (g >> 1) * 11 + (cid >> 2), hs = (fbs / 3) * 4 + fbs % 3;
    fuoff = has_f2 ? WS_S1 + ((hs * 8 + w) * 256 + ((2 + (g & 1)) * 64 ^ (w & 1) * 128) + 16 * (cid & 3) + c) * 16 : OOB; }
  const int wvo = g * 8192 + w * 128 + c * 8;
  const int qoff = WS_S1 + ((h * 8 + w) * 256 + (w & 1) * 128 + b * 64 + lane) * 16;
  const int pid = (b * H + h) * NSPLIT + s, muoff = WS_ML + ((pid & ~3) + (lane & 3)) * 16;
  const int bc = (cid >> 5) & 1, cu0 = (cid & 31) * 8;
  const int gp = tid >> 2, qd = tid & 3, ocol = bc * D + ucol(cu0 + (c >> 1)) + (c & 1);
  const int dpoff = WS_DP + ((gp < NFFN ? gp : 0) * 512 + bc * 256 + cu0 + 2 * qd) * 16;

  auto qk = [&](auto gic) __attribute__((always_inline)) {
    constexpr int gi = decltype(gic)::value;
    float d[U];
#pragma unroll
    for (int u = 0; u < U; ++u) d[u] = dot2(rg[gi][u][3], qp[3], dot2(rg[gi][u][2], qp[2], dot2(rg[gi][u][1], qp[1], dot2(rg[gi][u][0], qp[0], 0.f))));
    const bool o1 = c & 1, o2 = c & 2;
    const float x0 = (o1 ? d[1] : d[0]) + dpp<0xB1>(o1 ? d[0] : d[1]), x1 = (o1 ? d[3] : d[2]) + dpp<0xB1>(o1 ? d[2] : d[3]);
    float x = (o2 ? x1 : x0) + dpp<0x4E>(o2 ? x0 : x1);
    x += dpp<0x124>(x); x += dpp<0x128>(x);
    sp[gi / 4] = __builtin_bit_cast(float, __builtin_amdgcn_update_dpp(__builtin_bit_cast(int, sp[gi / 4]), __builtin_bit_cast(int, x), 0xE4, 0xf, 1 << (gi % 4), false));
    asm volatile("" : "+v"(sp[gi / 4]));
    SB();
  };
  auto pv = [&](auto gic) __attribute__((always_inline)) {
    constexpr int gi = decltype(gic)::value;
    const float p0 = dpp<0x150 + 4 * (gi % 4)>(sp[gi / 4]), p1 = dpp<0x151 + 4 * (gi % 4)>(sp[gi / 4]), p2 = dpp<0x152 + 4 * (gi % 4)>(sp[gi / 4]), p3 = dpp<0x153 + 4 * (gi % 4)>(sp[gi / 4]);
    const float p[4] = {p0, p1, p2, p3};
#pragma unroll
    for (int u = 0; u < U; ++u) {
#pragma unroll
      for (int k = 0; k < 4; ++k) { acc[2 * k] = fmaf(p[u], lo(rg[GROUPS + gi][u][k]), acc[2 * k]); acc[2 * k + 1] = fmaf(p[u], hi(rg[GROUPS + gi][u][k]), acc[2 * k + 1]); }
    }
    asm volatile("" : "+v"(acc[0]), "+v"(acc[1]), "+v"(acc[2]), "+v"(acc[3]), "+v"(acc[4]), "+v"(acc[5]), "+v"(acc[6]), "+v"(acc[7]));
    SB();
  };

  {
    float sm = row16_sum(lo(xst) + hi(xst)); sm = fold16(sm, sm) * (1.f / 64);
    const float d0 = lo(xst) - sm, d1 = hi(xst) - sm;
    float m2 = row16_sum(fmaf(d0, d0, d1 * d1)); m2 = fold16(m2, m2);
    if ((lane & 31) == 0) *(f32x2*)(ldsS + (lane >> 5) * 16 + 2 * w) = f32x2{sm, m2};
    float t[24];
#pragma unroll
    for (int k = 0; k < 4; ++k) {
      const float al = lo(sw0[k]), ah = hi(sw0[k]), bl = lo(sw1[k]), bh = hi(sw1[k]);
      t[2 * k] = fmaf(hi(xs0), bl, lo(xs0) * al); t[2 * k + 1] = fmaf(hi(xs0), bh, lo(xs0) * ah);
      t[8 + 2 * k] = fmaf(hi(xs1), bl, lo(xs1) * al); t[8 + 2 * k + 1] = fmaf(hi(xs1), bh, lo(xs1) * ah);
      t[16 + 2 * k] = al + bl; t[16 + 2 * k + 1] = ah + bh;
    }
#pragma unroll
    for (int k = 0; k < 8; ++k) t[k] = fold16(fold32(t[k], t[16 + k]), fold32(t[8 + k], t[16 + k]));
    *(f32x4*)(ldsY + w * 512 + g * 128 + 8 * c) = f32x4{t[0], t[1], t[2], t[3]};
    *(f32x4*)(ldsY + w * 512 + g * 128 + 8 * c + 4) = f32x4{t[4], t[5], t[6], t[7]};
    if (tid == 0) *(unsigned long long*)(ldsC + 8) = ep;
    asm volatile("" :: "v"(ep));
    if (last_wave(ldsC, lane)) {
      f32x4 st[8];
#pragma unroll
      for (int i = 0; i < 8; ++i) st[i] = *(const f32x4*)(ldsS + 4 * i);
      tagA = __builtin_amdgcn_readfirstlane(ldsC[8]) + 1u; tagB = __builtin_amdgcn_readfirstlane(ldsC[9]) + 0x9E3779B9u;
      const float mean0 = ((st[0][0] + st[0][2]) + (st[1][0] + st[1][2]) + (st[2][0] + st[2][2]) + (st[3][0] + st[3][2])) * 0.125f;
      const float mean1 = ((st[4][0] + st[4][2]) + (st[5][0] + st[5][2]) + (st[6][0] + st[6][2]) + (st[7][0] + st[7][2])) * 0.125f;
      float q0 = 0.f, q1 = 0.f, d0 = 0.f, d1 = 0.f;
#pragma unroll
      for (int i = 0; i < 4; ++i) {
        q0 += st[i][1] + st[i][3]; q1 += st[4 + i][1] + st[4 + i][3];
        const float a0 = st[i][0] - mean0, b0 = st[i][2] - mean0, a1 = st[4 + i][0] - mean1, b1 = st[4 + i][2] - mean1;
        d0 += fmaf(a0, a0, b0 * b0); d1 += fmaf(a1, a1, b1 * b1);
      }
      const float rstd0 = __builtin_amdgcn_rsqf(fmaf(64.f, d0, q0) * (1.f / D) + 1e-5f), rstd1 = __builtin_amdgcn_rsqf(fmaf(64.f, d1, q1) * (1.f / D) + 1e-5f);
      if (lane == 0) *(f32x4*)(ldsC + 12) = f32x4{mean0, rstd0, mean1, rstd1};
      last_wave(ldsC, lane);
      f32x2 z0 = {0.f, 0.f}, z1 = {0.f, 0.f}, cs = {0.f, 0.f};
#pragma unroll
      for (int ww = 0; ww < NWAVE; ++ww) {
        z0 += *(const f32x2*)(ldsY + ww * 512 + 2 * lane); z1 += *(const f32x2*)(ldsY + ww * 512 + 128 + 2 * lane); cs += *(const f32x2*)(ldsY + ww * 512 + 256 + 2 * lane); }
      st_unit(rs, WS_S1 + ((h * 8 + r8) * 256 + (r8 & 1) * 128 + lane) * 16, (z0[0] - mean0 * cs[0]) * rstd0, (z0[1] - mean0 * cs[1]) * rstd0, tagA, tagB);
      st_unit(rs, WS_S1 + ((h * 8 + r8) * 256 + (r8 & 1) * 128 + 64 + lane) * 16, (z1[0] - mean1 * cs[0]) * rstd1, (z1[1] - mean1 * cs[1]) * rstd1, tagA, tagB);
    }
  }
  MARK(33);
  unsigned due = tstart + (unsigned)T0X + (unsigned)(w * TPX / 8);

  auto point = [&](auto pc) __attribute__((always_inline)) {
    constexpr int p = decltype(pc)::value;
    PACE(p);
    if constexpr (GAPAT(p)) due = __builtin_amdgcn_readfirstlane(due + GAPX);
    SB();
#pragma unroll
    for (int u = 0; u < U; ++u) rg[p][u] = __builtin_amdgcn_raw_buffer_load_b128(p < GROUPS ? rk : rv, lo16 + u * 1024, kvo ^ GOFF(p % GROUPS), 2);
    SB();
    if constexpr (p == 3) qu = ld_unit(rs, qoff);
    if constexpr (p == 11) fu = ld_unit(rs, fuoff);
    if constexpr (p == 29) mu = ld_unit(rs, muoff);
#pragma unroll
    for (int i = won(p); i < won(p + 1); ++i) wor[i] = __builtin_amdgcn_raw_buffer_load_b64(rw, wvo + ABLWO_, (32 * (i >> 3) + (i & 7)) * 1024, WOAUX_);
    if constexpr (p == 25) {
      e0 = ld_unit(rs, is_fin ? dpoff : OOB); e1 = ld_unit(rs, is_fin ? dpoff + 16 : OOB);
      xo = ((const GAS unsigned short*)a.x)[ocol];
    }
    SB();
    if constexpr (p == 1) {
      wait_all(ldsC, NWAVE + 1);
      tagA = __builtin_amdgcn_readfirstlane(ldsC[8]) + 1u; tagB = __builtin_amdgcn_readfirstlane(ldsC[9]) + 0x9E3779B9u;
      asm volatile("" :: "v"(pf0), "v"(pf1), "v"(pfc));
      if (has_f1) {
        const f32x4 ln = *(const f32x4*)(ldsC + 12);
        const float n00 = (lo(xs0) - ln[0]) * ln[1], n01 = (hi(xs0) - ln[0]) * ln[1], n10 = (lo(xs1) - ln[2]) * ln[3], n11 = (hi(xs1) - ln[2]) * ln[3];
        float t[8];
#pragma unroll
        for (int k = 0; k < 4; ++k) {
          const float al = lo(ffw[0][k]), ah = hi(ffw[0][k]), bl = lo(ffw[1][k]), bh = hi(ffw[1][k]);
          t[2 * k] = fold32(fmaf(n01, bl, n00 * al), fmaf(n11, bl, n10 * al)); t[2 * k + 1] = fold32(fmaf(n01, bh, n00 * ah), fmaf(n11, bh, n10 * ah));
        }
        *(f32x4*)(ldsY + 4096 + w * 256 + (g >> 1) * 128 + 8 * c + 4 * (g & 1)) = f32x4{fold16(t[0], t[4]), fold16(t[1], t[5]), fold16(t[2], t[6]), fold16(t[3], t[7])};
        last_wave(ldsC + 2, lane);
      }
    }
    if constexpr (p == 3) {
      if (has_f1 && w < 2) {
        wait_all(ldsC + 2);
        f32x2 z = {0.f, 0.f};
#pragma unroll
        for (int ww = 0; ww < NWAVE; ++ww) z += *(const f32x2*)(ldsY + 4096 + ww * 256 + 2 * tid);
        st_unit(rs, WS_S1 + ((h * 8 + r8) * 256 + (~r8 & 1) * 128 + tid) * 16, z[0], z[1], tagA, tagB);
      }
    }
    if constexpr (p == 5) {
      await_unit(qu, rs, qoff, tagA, tagB);
      *(f32x2*)(ldsQ + w * 128 + 2 * lane) = f32x2{__uint_as_float(qu[0]), __uint_as_float(qu[2])};
      last_wave(ldsC + 1, lane);
    }
    if constexpr (p == 7) {
      wait_all(ldsC + 1);
      f32x4 q0 = {0, 0, 0, 0}, q1 = {0, 0, 0, 0};
#pragma unroll
      for (int ww = 0; ww < NWAVE; ++ww) { q0 += *(const f32x4*)(ldsQ + ww * 128 + 8 * c); q1 += *(const f32x4*)(ldsQ + ww * 128 + 8 * c + 4); }
      qp[0] = pk_bf16(q0[0], q0[1]); qp[1] = pk_bf16(q0[2], q0[3]); qp[2] = pk_bf16(q1[0], q1[1]); qp[3] = pk_bf16(q1[2], q1[3]);
      qk(IC<0>{});
    }
    if constexpr (p == 8) { qk(IC<1>{}); qk(IC<2>{}); qk(IC<3>{}); }
    if constexpr (p == 9) qk(IC<4>{});
    if constexpr (p >= 9 && p < GROUPS + 4) qk(IC<p - 4>{});
    if constexpr (p == 14) {
      if (has_f2) {
        await_unit(fu, rs, fuoff, tagA, tagB);
        *(f32x2*)(ldsF + w * 128 + g * 32 + 2 * c) = f32x2{__uint_as_float(fu[0]), __uint_as_float(fu[2])};
        last_wave(ldsC + 5, lane);
      }
    }
    if constexpr (p == 16) {
      if (has_f2) {
        wait_all(ldsC + 5);
        float gs = 0.f, us = 0.f;
        const int hx = ((lane >> 2) & 1) * 32 + 4 * w + (lane & 3);
#pragma unroll
        for (int ww = 0; ww < NWAVE; ++ww) { gs += ldsF[ww * 128 + hx]; us += ldsF[ww * 128 + 64 + hx]; }
        const float hv = gelu_tanh(gs) * us;
        float dd[16];
#pragma unroll
        for (int k = 0; k < 16; ++k) dd[k] = 0.f;
#pragma unroll
        for (int r = 0; r < 4; ++r) {
          const float h0 = __builtin_bit_cast(float, __builtin_amdgcn_readlane(__builtin_bit_cast(int, hv), r));
          const float h1 = __builtin_bit_cast(float, __builtin_amdgcn_readlane(__builtin_bit_cast(int, hv), 4 + r));
#pragma unroll
          for (int k = 0; k < 4; ++k) {
            const float wl = lo(ffw[r][k]), wh = hi(ffw[r][k]);
            dd[2 * k] = fmaf(h0, wl, dd[2 * k]); dd[2 * k + 1] = fmaf(h0, wh, dd[2 * k + 1]);
            dd[8 + 2 * k] = fmaf(h1, wl, dd[8 + 2 * k]); dd[8 + 2 * k + 1] = fmaf(h1, wh, dd[8 + 2 * k + 1]);
          }
        }
        *(f32x4*)(ldsY + w * 1024 + 8 * lane) = f32x4{dd[0], dd[1], dd[2], dd[3]};
        *(f32x4*)(ldsY + w * 1024 + 8 * lane + 4) = f32x4{dd[4], dd[5], dd[6], dd[7]};
        *(f32x4*)(ldsY + w * 1024 + 512 + 8 * lane) = f32x4{dd[8], dd[9], dd[10], dd[11]};
        *(f32x4*)(ldsY + w * 1024 + 512 + 8 * lane + 4) = f32x4{dd[12], dd[13], dd[14], dd[15]};
        last_wave(ldsC + 3, lane);
      }
    }
    if constexpr (p == 18) {
      if (has_f2) {
        wait_all(ldsC + 3);
        f32x2 dv = {0.f, 0.f};
#pragma unroll
        for (int ww = 0; ww < NWAVE; ++ww) dv += *(const f32x2*)(ldsY + ww * 1024 + (tid & 256) * 2 + ucol(tid & 255));
        st_unit(rs, WS_DP + (cid * 512 + tid) * 16, dv[0], dv[1], tagA, tagB);
      }
    }
    if constexpr (p == 27) {
      if (is_fin && (FLG & 1)) last_wave(ldsC + 2, lane);
      if (is_fin && !(FLG & 1)) {
        await_unit(e0, rs, dpoff, tagA, tagB);
        await_unit(e1, rs, dpoff + 16, tagA, tagB);
        const bool hd = gp < NFFN;
        const float r = fold4(hd ? __uint_as_float(e0[0]) : 0.f, hd ? __uint_as_float(e0[2]) : 0.f, hd ? __uint_as_float(e1[0]) : 0.f, hd ? __uint_as_float(e1[2]) : 0.f);
        if (c < 4) ldsF[w * 16 + 4 * c + g] = r;
        last_wave(ldsC + 2, lane);
      }
    }
    if constexpr (p == GROUPS + 3) {
      float x = fmaxf(fmaxf(sp[0], sp[1]), fmaxf(sp[2], sp[3]));
      x = fmaxf(x, dpp<0x128>(x)); x = fmaxf(x, dpp<0x124>(x)); x = fmaxf(x, dpp<0x122>(x)); x = fmaxf(x, dpp<0x121>(x));
      mw = xrow_max(x) * C2;
#pragma unroll
      for (int i = 0; i < 4; ++i) sp[i] = __builtin_amdgcn_exp2f(fmaf(sp[i], C2, -mw));
      const float lw = xrow_sum(row16_sum((sp[0] + sp[1]) + (sp[2] + sp[3])));
      if (lane == 0) { ldsM[w] = mw; ldsM[8 + w] = lw; }
      last_wave(ldsC + 4, lane);
    }
    if constexpr (p == 21) {
      if (w == 0) {
        wait_all(ldsC + 4);
        const float mi = ldsM[lane & 7], li = ldsM[8 + (lane & 7)];
        float M = fmaxf(mi, dpp<0x124>(mi)); M = fmaxf(M, dpp<0x122>(M)); M = fmaxf(M, dpp<0x121>(M));
        float L = __builtin_amdgcn_exp2f(mi - M) * li; L += dpp<0x124>(L); L += dpp<0x122>(L); L += dpp<0x121>(L);
        if (lane == 0) st_unit(rs, WS_ML + pid * 16, M, L, tagA, tagB);
      }
    }
    if constexpr (p == 30) {
      if (is_fin && w == 0) {
        wait_all(ldsC + 2);
        const float dv = ldsF[c] + ldsF[16 + c] + ldsF[32 + c];
        gte = dv * __builtin_amdgcn_rcpf(1.f + __expf(-dv));
        asm volatile("" : "+v"(gte));
      }
    }
    if constexpr (p == 31) {
      await_unit(mu, rs, muoff, tagA, tagB);
      const float mi = __uint_as_float(mu[0]);
      float M = fmaxf(mi, dpp<0xB1>(mi)); M = fmaxf(M, dpp<0x4E>(M));
      float L = __builtin_amdgcn_exp2f(mi - M) * __uint_as_float(mu[2]); L += dpp<0xB1>(L); L += dpp<0x4E>(L);
      mw = __builtin_amdgcn_exp2f(mw - M) * __builtin_amdgcn_rcpf(L);
      asm volatile("" : "+v"(mw));
    }
    if constexpr (p >= 20 && p < 28) pv(IC<p - 20>{});
    if constexpr (p == 28 || p == 29) { pv(IC<2 * p - 48>{}); pv(IC<2 * p - 47>{}); }
    if constexpr (p == 30) pv(IC<12>{});
    if constexpr (p == 31) { pv(IC<13>{}); pv(IC<14>{}); pv(IC<15>{}); }
    if constexpr (p == 30) {
#pragma unroll
      for (int i = 0; i < 16; ++i) {
        const v2u r0 = wor[2 * i], r1 = wor[2 * i + 1];
        wb[0][i >> 2][i & 3] = __builtin_amdgcn_perm(r1[0], r0[0], 0x05040100u); wb[1][i >> 2][i & 3] = __builtin_amdgcn_perm(r1[0], r0[0], 0x07060302u);
        wb[2][i >> 2][i & 3] = __builtin_amdgcn_perm(r1[1], r0[1], 0x05040100u); wb[3][i >> 2][i & 3] = __builtin_amdgcn_perm(r1[1], r0[1], 0x07060302u);
      }
#pragma unroll
      for (int i = 0; i < 4; ++i) asm volatile("" : "+v"(wb[i][0]), "+v"(wb[i][1]), "+v"(wb[i][2]), "+v"(wb[i][3]));
    }
    SB();
  };
  static_for<2 * GROUPS>(point);
  MARK(34);

  {
    const f32x2 o2 = fold8(acc) * mw;
    const unsigned oh = pk_bf16(o2[0], o2[1]), ol = pk_bf16(o2[0] - lo(oh), o2[1] - hi(oh));
    ldsA[w * 68 + 4 * c + g] = oh; ldsA[(8 + w) * 68 + 4 * c + g] = ol;
  }
  __syncthreads();
  MARK(35);
  {
    v4u af[4];
#pragma unroll
    for (int ks = 0; ks < 4; ++ks) af[ks] = *(const v4u*)(ldsA + c * 68 + 16 * ks + 4 * g);
    f32x4 d[4];
#pragma unroll
    for (int t = 0; t < 4; ++t) d[t] = f32x4{0.f, 0.f, 0.f, 0.f};
#pragma unroll
    for (int ks = 0; ks < 4; ++ks)
#pragma unroll
      for (int t = 0; t < 4; ++t) d[t] = __builtin_amdgcn_mfma_f32_16x16x32_bf16(__builtin_bit_cast(bf16x8, af[ks]), __builtin_bit_cast(bf16x8, wb[t][ks]), d[t], 0, 0, 0);
    float y[4];
#pragma unroll
    for (int t = 0; t < 4; ++t) y[t] = (d[t][0] + d[t][1]) + (d[t][2] + d[t][3]);
    float u0 = fold32(y[0], y[2]), u1 = fold32(y[1], y[3]);
    u0 = fold16(u0, u0); u1 = fold16(u1, u1);
    if ((g & 1) == 0) st_unit(rs, WS_Y + (pid * 256 + 32 * w + 16 * (g >> 1) + c) * 16, u0, u1, tagA, tagB);
  }
  MARK(36);

  if (is_fin) {
    const int yoff = WS_Y + ((bc * 128 + gp) * 256 + cu0 + 2 * qd) * 16;
    __builtin_amdgcn_s_barrier();
    if (PDLY_) __builtin_amdgcn_s_sleep(PDLY_);
    MARK(38);
    v4u y0, y1;
    bool ok = false;
#pragma unroll 1
    for (unsigned spin = 0; spin < SPIN_CAP; ++spin) {
      asm volatile("" ::: "memory");
      if (!ok) {
        y0 = ld_unit(rs, yoff); y1 = ld_unit(rs, yoff + 16);
        ok = (((y0[1] ^ tagA) | (y1[1] ^ tagA)) | ((y0[3] ^ tagB) | (y1[3] ^ tagB))) == 0u;
      }
      if (__all(ok)) break;
      __builtin_amdgcn_s_sleep(1);
    }
    MARK(39);
    const float r = fold4(__uint_as_float(y0[0]), __uint_as_float(y0[2]), __uint_as_float(y1[0]), __uint_as_float(y1[2]));
    if (c < 4) ldsF[128 + w * 16 + 4 * c + g] = r;
    __syncthreads();
    if (tid < 16) {
      float sv = 0.f;
#pragma unroll
      for (int ww = 0; ww < NWAVE; ++ww) sv += ldsF[128 + ww * 16 + tid];
      const float o = __uint_as_float((unsigned)xo << 16) + sv * gte;
      const __bf16 ob = (__bf16)o;
      ((GAS unsigned short*)a.out)[ocol] = __builtin_bit_cast(unsigned short, ob);
      if (cid == 0 && tid == 0)
        __hip_atomic_store((GAS unsigned long long*)(a.ws + WS_EPOCH), ((unsigned long long)tagB << 32) | tagA, __ATOMIC_RELAXED, __HIP_MEMORY_SCOPE_AGENT);
    }
  }
  asm volatile(".Lk_all_end:");
}

extern "C" void kernel_launch(void* const* d_in, const int* in_sizes, int n_in, void* d_out, int out_size, void* d_ws, size_t ws_size,
                              hipStream_t stream) {
  Args a;
  a.x = (const unsigned short*)d_in[0];
  a.k = (const unsigned short*)d_in[1];
  a.v = (const unsigned short*)d_in[2];
  a.wq = (const unsigned short*)d_in[3];
  a.wo = (const unsigned short*)d_in[4];
  a.wg = (const unsigned short*)d_in[6];
  a.wu = (const unsigned short*)d_in[7];
  a.wd = (const unsigned short*)d_in[8];
  a.out = (unsigned short*)d_out;
  a.ws = (unsigned char*)d_ws;
  hipLaunchKernelGGL(k_all, dim3(256), dim3(NTHR), 0, stream, a);
}
```

```cpp
#include <hip/hip_runtime.h>
#include <hip/hip_bf16.h>
#include <stdint.h>

typedef unsigned v4u __attribute__((ext_vector_type(4)));
typedef float f32x4 __attribute__((ext_vector_type(4)));
typedef float f32x2 __attribute__((ext_vector_type(2)));
typedef __bf16 bf16x2 __attribute__((ext_vector_type(2)));
typedef __bf16 bf16x8 __attribute__((ext_vector_type(8)));
typedef unsigned v2u __attribute__((ext_vector_type(2)));
#define GAS __attribute__((address_space(1)))
#define CAS __attribute__((address_space(4)))

constexpr int D = 512, H = 32, SK = 8192, DH = 128, F = 1376;
constexpr int NSPLIT = 4, KEYS_PER_WG = SK / NSPLIT;
constexpr int NWAVE = 8, NTHR = NWAVE * 64;
constexpr int NFFN = F / 32;
constexpr int U = 4;
constexpr int GROUPS = KEYS_PER_WG / NWAVE / (4 * U);
#define PWO_ 1
constexpr int won(int p) { return p <= PWO_ ? 0 : p >= 28 ? 32 : (p - PWO_) * 32 / (28 - PWO_); }
#define ABLWO_ 0
#define WOAUX_ 0
#define GAP_ 0
#define GAPAT(p) ((p) == 4 || (p) == 18)
#define PDLY_ 8
#define LATE_PRIO_ 400
#define KNT_ 1
#define VNT_ 1
#define WNT_ 1
#define ONT_ 0
#define TP_ 1792
#define T0_ 4400

constexpr int WS_EPOCH = 0;
constexpr int WS_ML = 4096;
constexpr int WS_Y = WS_ML + 256 * 16;
constexpr int WS_DP = WS_Y + 256 * 256 * 16;
constexpr int WS_S1 = WS_DP + NFFN * 512 * 16;
constexpr int WS_END = WS_S1 + 32 * 8 * 4 * 64 * 16;
constexpr int OOB = 0x7ffffff0;
constexpr unsigned SPIN_CAP = 400000;

#define SB() __builtin_amdgcn_sched_barrier(0)

__device__ __forceinline__ float dot2(unsigned k, unsigned q, float acc) {
  return __builtin_amdgcn_fdot2_f32_bf16(__builtin_bit_cast(bf16x2, k), __builtin_bit_cast(bf16x2, q), acc, false);
}
template <int CTRL> __device__ __forceinline__ float dpp(float x) {
  return __builtin_bit_cast(float, __builtin_amdgcn_mov_dpp(__builtin_bit_cast(int, x), CTRL, 0xf, 0xf, true));
}
__device__ __forceinline__ float row16_sum(float x) {
  x += dpp<0x128>(x);
  x += dpp<0x124>(x);
  x += dpp<0x122>(x);
  x += dpp<0x121>(x);
  return x;
}
__device__ __forceinline__ float fold32(float a, float b) {
  auto t = __builtin_amdgcn_permlane32_swap(__float_as_uint(a), __float_as_uint(b), false, false);
  return __uint_as_float(t[0]) + __uint_as_float(t[1]);
}
__device__ __forceinline__ float fold16(float a, float b) {
  auto t = __builtin_amdgcn_permlane16_swap(__float_as_uint(a), __float_as_uint(b), false, false);
  return __uint_as_float(t[0]) + __uint_as_float(t[1]);
}
__device__ __forceinline__ float xrow_sum(float x) { x = fold16(x, x); return fold32(x, x); }
__device__ __forceinline__ float xrow_max(float x) {
  auto s = __builtin_amdgcn_permlane16_swap(__float_as_uint(x), __float_as_uint(x), false, false);
  x = fmaxf(__uint_as_float(s[0]), __uint_as_float(s[1]));
  auto t = __builtin_amdgcn_permlane32_swap(__float_as_uint(x), __float_as_uint(x), false, false);
  return fmaxf(__uint_as_float(t[0]), __uint_as_float(t[1]));
}
__device__ __forceinline__ f32x2 fold8(const float (&v)[8]) {
  const float u0 = fold32(v[0], v[4]), u1 = fold32(v[1], v[5]), u2 = fold32(v[2], v[6]), u3 = fold32(v[3], v[7]);
  return f32x2{fold16(u0, u2), fold16(u1, u3)};
}
__device__ __forceinline__ float fold4(float v0, float v1, float v2, float v3) {
  float r = fold16(fold32(v0, v2), fold32(v1, v3));
  r += dpp<0x124>(r); r += dpp<0x128>(r);
  return r;
}
__device__ __forceinline__ float lo(unsigned u) { return __uint_as_float(u << 16); }
__device__ __forceinline__ float hi(unsigned u) { return __uint_as_float(u & 0xffff0000u); }
__device__ __forceinline__ unsigned pk_bf16(float a, float b) {
  bf16x2 r = {(__bf16)a, (__bf16)b};
  return __builtin_bit_cast(unsigned, r);
}
__device__ __forceinline__ unsigned sel4(v4u v, int g) { const unsigned a = (g & 1) ? v[1] : v[0], b = (g & 1) ? v[3] : v[2]; return (g & 2) ? b : a; }
__device__ __forceinline__ int ucol(int u) { return 64 * (u >> 5) + 4 * (u & 15) + 2 * ((u >> 4) & 1); }
template <int NT> __device__ __forceinline__ v4u ldp(const GAS v4u* p) { if (NT) return __builtin_nontemporal_load(p); else return *p; }
__device__ __forceinline__ float gelu_tanh(float x) {
  const float u = 0.7978845608028654f * (x + 0.044715f * x * x * x);
  const float e = __expf(2.f * u);
  const float t = 1.f - 2.f * __builtin_amdgcn_rcpf(e + 1.f);
  return 0.5f * x * (1.f + t);
}
__device__ __forceinline__ void pace_wait(unsigned& due16, unsigned step16) {
  asm volatile("1:\n\ts_memrealtime s[90:91]\n\ts_waitcnt lgkmcnt(0)\n\ts_lshl_b32 s90, s90, 4\n\ts_sub_u32 s91, s90, %0\n\ts_cmp_ge_i32 s91, 0\n\ts_cbranch_scc1 2f\n\ts_sleep 2\n\ts_branch 1b\n"
               "2:\n\ts_add_u32 %0, %0, %1\n\ts_setprio 0\n\ts_cmp_lt_i32 s91, %2\n\ts_cbranch_scc1 3f\n\ts_setprio 2\n3:"
               : "+s"(due16) : "s"(step16), "i"(LATE_PRIO_) : "s90", "s91", "scc");
}
__device__ __forceinline__ unsigned now16() { return (unsigned)__builtin_amdgcn_s_memrealtime() << 4; }
#define MARK(i)
#define TPX TP_
#define T0X T0_
#define FLG 0
#define SPRD 0
#define PFC_ 1
#define GAPX GAP_
#define PACE(pt) pace_wait(due, (unsigned)TPX & ~7u)
constexpr int AUX_SC1 = 16;
__device__ __forceinline__ void st_unit(__amdgpu_buffer_rsrc_t rs, int off, float v0, float v1, unsigned ta, unsigned tb) {
  __builtin_amdgcn_raw_buffer_store_b128(v4u{__float_as_uint(v0), ta, __float_as_uint(v1), tb}, rs, off, 0, AUX_SC1);
}
__device__ __forceinline__ v4u ld_unit(__amdgpu_buffer_rsrc_t rs, int off) { return __builtin_amdgcn_raw_buffer_load_b128(rs, off, 0, AUX_SC1); }
__device__ __forceinline__ void await_unit(v4u& u, __amdgpu_buffer_rsrc_t rs, int off, unsigned ta, unsigned tb) {
  if (!__all(((u[1] ^ ta) | (u[3] ^ tb)) == 0u)) {
#pragma unroll 1
    for (unsigned spin = 0; spin < SPIN_CAP; ++spin) {
      __builtin_amdgcn_s_sleep(1);
      asm volatile("" ::: "memory");
      u = ld_unit(rs, off);
      if (__all(((u[1] ^ ta) | (u[3] ^ tb)) == 0u)) break;
    }
  }
}

__device__ __forceinline__ bool last_wave(unsigned* cnt, int lane) {
  asm volatile("s_waitcnt lgkmcnt(0)" ::: "memory");
  unsigned old = 0;
  if (lane == 0) old = __hip_atomic_fetch_add(cnt, 1u, __ATOMIC_RELAXED, __HIP_MEMORY_SCOPE_WORKGROUP);
  old = __builtin_amdgcn_readfirstlane(old);
  asm volatile("" ::: "memory");
  return old == NWAVE - 1;
}

__device__ __forceinline__ void wait_all(unsigned* cnt, unsigned n = NWAVE) {
  while (__builtin_amdgcn_readfirstlane(__hip_atomic_load(cnt, __ATOMIC_RELAXED, __HIP_MEMORY_SCOPE_WORKGROUP)) < n) __builtin_amdgcn_s_sleep(1);
  asm volatile("" ::: "memory");
}
template <int I> struct IC { static constexpr int value = I; };
template <int N, int I = 0, typename Fn> __device__ __forceinline__ void static_for(Fn& fn) { if constexpr (I < N) { fn(IC<I>{}); static_for<N, I + 1>(fn); } }

struct Args {
  const unsigned short* x; const unsigned short* wq;
  unsigned char* ws; const unsigned short* v; const unsigned short* wo; const unsigned short* wg;
  const unsigned short* wu; const unsigned short* wd; unsigned short* out; const unsigned short* k;
};

__global__ void __launch_bounds__(NTHR, 2) k_all(Args a) {
  __shared__ __attribute__((aligned(16))) float lds[8 * 1024 + 16 * 68 + 16 + 8 * 128 + 8 * 128 + 32 + 16];
  float* ldsY = lds;
  unsigned* ldsA = (unsigned*)(lds + 8 * 1024);
  float* ldsM = lds + 8 * 1024 + 16 * 68;
  float* ldsF = ldsM + 16;
  float* ldsQ = ldsF + 8 * 128;
  float* ldsS = ldsQ + 8 * 128;
  unsigned* ldsC = (unsigned*)(ldsS + 32);
  const unsigned tstart = now16();
  MARK(32);
  const int bid = blockIdx.x;
  const int xcd = bid & 7, j = bid >> 3;
  const int h = xcd * 4 + (j >> 3), b = (j >> 2) & 1, s = j & 3;
  const int tid = threadIdx.x, lane = tid & 63, w = __builtin_amdgcn_readfirstlane(tid >> 6);
  const int g = lane >> 4, c = lane & 15;
  const unsigned long long tptr = ((const CAS unsigned long long*)__builtin_amdgcn_kernarg_segment_ptr())[2 + w];
  asm volatile("" :: "s"(tptr), "s"(a.k), "s"(a.x), "s"(a.wq), "s"(tstart));
  if (tid < 3) *(v4u*)(ldsC + 4 * tid) = v4u{0u, 0u, tid == 2 ? tstart : 0u, 0u};
  const int r8 = j & 7;
  const int fb = xcd * 3 + (j >> 3);
  const bool has_f1 = (j < 24) && (fb < 22) && !(FLG & 1);
  const bool is_fin = j >= 24;
  const int cid = xcd * 8 + (j - 24);
  const bool has_f2 = is_fin && cid < NFFN && !(FLG & 1);
  const __amdgpu_buffer_rsrc_t rs = __builtin_amdgcn_make_buffer_rsrc((void*)a.ws, 0, WS_END, 0x00020000);

  const int row0 = 64 * r8 + 8 * w + 2 * g, lo16 = lane * 16;
  v4u sw0, sw1;
  {
    const GAS v4u* p = (const GAS v4u*)(a.wq + (size_t)row0 * (H * DH) + h * DH + 8 * c);
    sw0 = ldp<WNT_>(p); sw1 = ldp<WNT_>(p + H * DH / 8);
  }
  const unsigned xst = *(const GAS unsigned*)(a.x + (lane >> 5) * D + 64 * w + 2 * (lane & 31));
  const unsigned long long ep = __hip_atomic_load((const GAS unsigned long long*)(tptr + ((unsigned)(b * H + h) * ((0x82u >> w) & 1u) << 21) + ((unsigned)h * ((0x04u >> w) & 1u) << 17)),
                                                  __ATOMIC_RELAXED, __HIP_MEMORY_SCOPE_AGENT);
  const __amdgpu_buffer_rsrc_t rf = __builtin_amdgcn_make_buffer_rsrc((void*)(!has_f1 ? a.wd : fb < 11 ? a.wg : a.wu), 0, (has_f1 || has_f2) ? D * F * 2 : 0, 0x00020000);
  const int fcol = 128 * (fb < 11 ? fb : fb - 11) + 8 * c;
  const int fxo = !has_f1 ? (32 * cid + 4 * w) * D * 2 + lo16 : fcol < F ? (row0 * F + fcol) * 2 : OOB - 4 * F * 2, fxo2 = has_f1 ? OOB - 4096 : fxo;
  const __amdgpu_buffer_rsrc_t rw = __builtin_amdgcn_make_buffer_rsrc((void*)(a.wo + (size_t)h * DH * D), 0, DH * D * 2, 0x00020000);
  asm volatile("s_waitcnt lgkmcnt(0)\n\ts_barrier" ::: "memory");
  v4u xq0, xq1;
  asm volatile("s_load_dwordx4 %0, %2, 0x0\n\ts_load_dwordx4 %1, %2, 0x400" : "=&s"(xq0), "=&s"(xq1) : "s"(a.x + 64 * r8 + 8 * w));
  v4u ffw[4];
  ffw[0] = __builtin_amdgcn_raw_buffer_load_b128(rf, fxo, 0, WNT_ ? 2 : 0);
  ffw[1] = __builtin_amdgcn_raw_buffer_load_b128(rf, fxo, has_f1 ? F * 2 : D * 2, WNT_ ? 2 : 0);
  const v4u pf0 = __builtin_amdgcn_raw_buffer_load_b128(rw, lo16, (16 * r8 + 2 * w) * 1024, 0), pf1 = __builtin_amdgcn_raw_buffer_load_b128(rw, lo16 + 1024, (16 * r8 + 2 * w) * 1024, 0);
  unsigned long long pc0; unsigned csz;
  asm volatile("s_getpc_b64 %0\n.Lk_all_pc0:\n\ts_mov_b32 %1, .Lk_all_end-.Lk_all_pc0" : "=s"(pc0), "=s"(csz));
  const v4u pfc = __builtin_amdgcn_raw_buffer_load_b128(__builtin_amdgcn_make_buffer_rsrc((void*)(pc0 & ~127ull), 0, (w == 3 && PFC_) ? (csz & ~15u) : 0u, 0x00020000), lo16, j * 1024, 0);
  ffw[2] = __builtin_amdgcn_raw_buffer_load_b128(rf, fxo2 + 2048, 0, WNT_ ? 2 : 0);
  ffw[3] = __builtin_amdgcn_raw_buffer_load_b128(rf, fxo2 + 3072, 0, WNT_ ? 2 : 0);
  SB();

  const __amdgpu_buffer_rsrc_t rk = __builtin_amdgcn_make_buffer_rsrc((void*)(a.k + (size_t)(b * H + h) * SK * DH), 0, SK * DH * 2, 0x00020000);
  const __amdgpu_buffer_rsrc_t rv = __builtin_amdgcn_make_buffer_rsrc((void*)(a.v + (size_t)(b * H + h) * SK * DH), 0, SK * DH * 2, 0x00020000);
  const unsigned slot0 = (__builtin_amdgcn_readfirstlane(ldsC[10]) + (unsigned)T0X) / (unsigned)(TPX / 8) + (unsigned)w;
  const int kvo = ((slot0 & 1) << 13) | ((slot0 & 6) << 14) | ((s & 1) << 12) | ((s >> 1) << 14);
  unsigned due = slot0 * (unsigned)(TPX / 8);
#define GOFF(gi_) ((((gi_) & 15) << 17) | (((gi_) & 4) << 12) | (((gi_) & 8) << 9))
  v4u rg[2 * GROUPS][U];

  constexpr float C2 = 0.08838834764831845f * 1.4426950408889634f;
  float sp[4] = {0.f, 0.f, 0.f, 0.f};
  float mw = 0.f, acc[8];
#pragma unroll
  for (int k = 0; k < 8; ++k) acc[k] = 0.f;
  unsigned tagA, tagB, qp[4];
  v2u wor[32]; v4u wb[4][4], fu, qu, mu, e0, e1; unsigned short xo; float gte = 0.f;
  asm volatile("s_waitcnt lgkmcnt(0)" : "+s"(xq0), "+s"(xq1));
  const unsigned xs0 = sel4(xq0, g), xs1 = sel4(xq1, g);
  int fuoff;
  { const int fbs = (g >> 1) * 11 + (cid >> 2), hs = (fbs / 3) * 4 + fbs % 3;
    fuoff = has_f2 ? WS_S1 + ((hs * 8 + w) * 256 + ((2 + (g & 1)) * 64 ^ (w & 1) * 128) + 16 * (cid & 3) + c) * 16 : OOB; }
  const int wvo = g * 8192 + w * 128 + c * 8;
  const int qoff = WS_S1 + ((h * 8 + w) * 256 + (w & 1) * 128 + b * 64 + lane) * 16;
  const int pid = (b * H + h) * NSPLIT + s, muoff = WS_ML + ((pid & ~3) + (lane & 3)) * 16;
  const int bc = (cid >> 5) & 1, cu0 = (cid & 31) * 8;
  const int gp = tid >> 2, qd = tid & 3, ocol = bc * D + ucol(cu0 + (c >> 1)) + (c & 1);
  const int dpoff = WS_DP + ((gp < NFFN ? gp : 0) * 512 + bc * 256 + cu0 + 2 * qd) * 16;

  auto qk = [&](auto gic) __attribute__((always_inline)) {
    constexpr int gi = decltype(gic)::value;
    float d[U];
#pragma unroll
    for (int u = 0; u < U; ++u) d[u] = dot2(rg[gi][u][3], qp[3], dot2(rg[gi][u][2], qp[2], dot2(rg[gi][u][1], qp[1], dot2(rg[gi][u][0], qp[0], 0.f))));
    const bool o1 = c & 1, o2 = c & 2;
    const float x0 = (o1 ? d[1] : d[0]) + dpp<0xB1>(o1 ? d[0] : d[1]), x1 = (o1 ? d[3] : d[2]) + dpp<0xB1>(o1 ? d[2] : d[3]);
    float x = (o2 ? x1 : x0) + dpp<0x4E>(o2 ? x0 : x1);
    x += dpp<0x124>(x); x += dpp<0x128>(x);
    sp[gi / 4] = __builtin_bit_cast(float, __builtin_amdgcn_update_dpp(__builtin_bit_cast(int, sp[gi / 4]), __builtin_bit_cast(int, x), 0xE4, 0xf, 1 << (gi % 4), false));
    asm volatile("" : "+v"(sp[gi / 4]));
    SB();
  };
  auto pv = [&](auto gic) __attribute__((always_inline)) {
    constexpr int gi = decltype(gic)::value;
    const float p0 = dpp<0x150 + 4 * (gi % 4)>(sp[gi / 4]), p1 = dpp<0x151 + 4 * (gi % 4)>(sp[gi / 4]), p2 = dpp<0x152 + 4 * (gi % 4)>(sp[gi / 4]), p3 = dpp<0x153 + 4 * (gi % 4)>(sp[gi / 4]);
    const float p[4] = {p0, p1, p2, p3};
#pragma unroll
    for (int u = 0; u < U; ++u) {
#pragma unroll
      for (int k = 0; k < 4; ++k) { acc[2 * k] = fmaf(p[u], lo(rg[GROUPS + gi][u][k]), acc[2 * k]); acc[2 * k + 1] = fmaf(p[u], hi(rg[GROUPS + gi][u][k]), acc[2 * k + 1]); }
    }
    asm volatile("" : "+v"(acc[0]), "+v"(acc[1]), "+v"(acc[2]), "+v"(acc[3]), "+v"(acc[4]), "+v"(acc[5]), "+v"(acc[6]), "+v"(acc[7]));
    SB();
  };

  {
    float sm = row16_sum(lo(xst) + hi(xst)); sm = fold16(sm, sm) * (1.f / 64);
    const float d0 = lo(xst) - sm, d1 = hi(xst) - sm;
    float m2 = row16_sum(fmaf(d0, d0, d1 * d1)); m2 = fold16(m2, m2);
    if ((lane & 31) == 0) *(f32x2*)(ldsS + (lane >> 5) * 16 + 2 * w) = f32x2{sm, m2};
    float t[24];
#pragma unroll
    for (int k = 0; k < 4; ++k) {
      const float al = lo(sw0[k]), ah = hi(sw0[k]), bl = lo(sw1[k]), bh = hi(sw1[k]);
      t[2 * k] = fmaf(hi(xs0), bl, lo(xs0) * al); t[2 * k + 1] = fmaf(hi(xs0), bh, lo(xs0) * ah);
      t[8 + 2 * k] = fmaf(hi(xs1), bl, lo(xs1) * al); t[8 + 2 * k + 1] = fmaf(hi(xs1), bh, lo(xs1) * ah);
      t[16 + 2 * k] = al + bl; t[16 + 2 * k + 1] = ah + bh;
    }
#pragma unroll
    for (int k = 0; k < 8; ++k) t[k] = fold16(fold32(t[k], t[16 + k]), fold32(t[8 + k], t[16 + k]));
    *(f32x4*)(ldsY + w * 512 + g * 128 + 8 * c) = f32x4{t[0], t[1], t[2], t[3]};
    *(f32x4*)(ldsY + w * 512 + g * 128 + 8 * c + 4) = f32x4{t[4], t[5], t[6], t[7]};
    if (tid == 0) *(unsigned long long*)(ldsC + 8) = ep;
    asm volatile("" :: "v"(ep));
    if (last_wave(ldsC, lane)) {
      f32x4 st[8];
#pragma unroll
      for (int i = 0; i < 8; ++i) st[i] = *(const f32x4*)(ldsS + 4 * i);
      tagA = __builtin_amdgcn_readfirstlane(ldsC[8]) + 1u; tagB = __builtin_amdgcn_readfirstlane(ldsC[9]) + 0x9E3779B9u;
      const float mean0 = ((st[0][0] + st[0][2]) + (st[1][0] + st[1][2]) + (st[2][0] + st[2][2]) + (st[3][0] + st[3][2])) * 0.125f;
      const float mean1 = ((st[4][0] + st[4][2]) + (st[5][0] + st[5][2]) + (st[6][0] + st[6][2]) + (st[7][0] + st[7][2])) * 0.125f;
      float q0 = 0.f, q1 = 0.f, d0 = 0.f, d1 = 0.f;
#pragma unroll
      for (int i = 0; i < 4; ++i) {
        q0 += st[i][1] + st[i][3]; q1 += st[4 + i][1] + st[4 + i][3];
        const float a0 = st[i][0] - mean0, b0 = st[i][2] - mean0, a1 = st[4 + i][0] - mean1, b1 = st[4 + i][2] - mean1;
        d0 += fmaf(a0, a0, b0 * b0); d1 += fmaf(a1, a1, b1 * b1);
      }
      const float rstd0 = __builtin_amdgcn_rsqf(fmaf(64.f, d0, q0) * (1.f / D) + 1e-5f), rstd1 = __builtin_amdgcn_rsqf(fmaf(64.f, d1, q1) * (1.f / D) + 1e-5f);
      if (lane == 0) *(f32x4*)(ldsC + 12) = f32x4{mean0, rstd0, mean1, rstd1};
      last_wave(ldsC, lane);
      f32x2 z0 = {0.f, 0.f}, z1 = {0.f, 0.f}, cs = {0.f, 0.f};
#pragma unroll
      for (int ww = 0; ww < NWAVE; ++ww) {
        z0 += *(const f32x2*)(ldsY + ww * 512 + 2 * lane); z1 += *(const f32x2*)(ldsY + ww * 512 + 128 + 2 * lane); cs += *(const f32x2*)(ldsY + ww * 512 + 256 + 2 * lane); }
      st_unit(rs, WS_S1 + ((h * 8 + r8) * 256 + (r8 & 1) * 128 + lane) * 16, (z0[0] - mean0 * cs[0]) * rstd0, (z0[1] - mean0 * cs[1]) * rstd0, tagA, tagB);
      st_unit(rs, WS_S1 + ((h * 8 + r8) * 256 + (r8 & 1) * 128 + 64 + lane) * 16, (z1[0] - mean1 * cs[0]) * rstd1, (z1[1] - mean1 * cs[1]) * rstd1, tagA, tagB);
    }
  }
  MARK(33);

  auto point = [&](auto pc) __attribute__((always_inline)) {
    constexpr int p = decltype(pc)::value;
    const int so = kvo ^ GOFF((slot0 >> 3) + p);
    PACE(p);
    SB();
#pragma unroll
    for (int u = 0; u < U; ++u) rg[p][u] = __builtin_amdgcn_raw_buffer_load_b128(p < GROUPS ? rk : rv, lo16 + u * 1024, so, 2);
    SB();
    if constexpr (p == 3) qu = ld_unit(rs, qoff);
    if constexpr (p == 11) fu = ld_unit(rs, fuoff);
    if constexpr (p == 29) mu = ld_unit(rs, muoff);
#pragma unroll
    for (int i = won(p); i < won(p + 1); ++i) wor[i] = __builtin_amdgcn_raw_buffer_load_b64(rw, wvo + ABLWO_, (32 * (i >> 3) + (i & 7)) * 1024, WOAUX_);
    if constexpr (p == 25) {
      e0 = ld_unit(rs, is_fin ? dpoff : OOB); e1 = ld_unit(rs, is_fin ? dpoff + 16 : OOB);
      xo = ((const GAS unsigned short*)a.x)[ocol];
    }
    SB();
    if constexpr (p == 1) {
      wait_all(ldsC, NWAVE + 1);
      tagA = __builtin_amdgcn_readfirstlane(ldsC[8]) + 1u; tagB = __builtin_amdgcn_readfirstlane(ldsC[9]) + 0x9E3779B9u;
      asm volatile("" :: "v"(pf0), "v"(pf1), "v"(pfc));
      if (has_f1) {
        const f32x4 ln = *(const f32x4*)(ldsC + 12);
        const float n00 = (lo(xs0) - ln[0]) * ln[1], n01 = (hi(xs0) - ln[0]) * ln[1], n10 = (lo(xs1) - ln[2]) * ln[3], n11 = (hi(xs1) - ln[2]) * ln[3];
        float t[8];
#pragma unroll
        for (int k = 0; k < 4; ++k) {
          const float al = lo(ffw[0][k]), ah = hi(ffw[0][k]), bl = lo(ffw[1][k]), bh = hi(ffw[1][k]);
          t[2 * k] = fold32(fmaf(n01, bl, n00 * al), fmaf(n11, bl, n10 * al)); t[2 * k + 1] = fold32(fmaf(n01, bh, n00 * ah), fmaf(n11, bh, n10 * ah));
        }
        *(f32x4*)(ldsY + 4096 + w * 256 + (g >> 1) * 128 + 8 * c + 4 * (g & 1)) = f32x4{fold16(t[0], t[4]), fold16(t[1], t[5]), fold16(t[2], t[6]), fold16(t[3], t[7])};
        last_wave(ldsC + 2, lane);
      }
    }
    if constexpr (p == 3) {
      if (has_f1 && w < 2) {
        wait_all(ldsC + 2);
        f32x2 z = {0.f, 0.f};
#pragma unroll
        for (int ww = 0; ww < NWAVE; ++ww) z += *(const f32x2*)(ldsY + 4096 + ww * 256 + 2 * tid);
        st_unit(rs, WS_S1 + ((h * 8 + r8) * 256 + (~r8 & 1) * 128 + tid) * 16, z[0], z[1], tagA, tagB);
      }
    }
    if constexpr (p == 5) {
      await_unit(qu, rs, qoff, tagA, tagB);
      *(f32x2*)(ldsQ + w * 128 + 2 * lane) = f32x2{__uint_as_float(qu[0]), __uint_as_float(qu[2])};
      last_wave(ldsC + 1, lane);
    }
    if constexpr (p == 7) {
      wait_all(ldsC + 1);
      f32x4 q0 = {0, 0, 0, 0}, q1 = {0, 0, 0, 0};
#pragma unroll
      for (int ww = 0; ww < NWAVE; ++ww) { q0 += *(const f32x4*)(ldsQ + ww * 128 + 8 * c); q1 += *(const f32x4*)(ldsQ + ww * 128 + 8 * c + 4); }
      qp[0] = pk_bf16(q0[0], q0[1]); qp[1] = pk_bf16(q0[2], q0[3]); qp[2] = pk_bf16(q1[0], q1[1]); qp[3] = pk_bf16(q1[2], q1[3]);
      qk(IC<0>{});
    }
    if constexpr (p == 8) { qk(IC<1>{}); qk(IC<2>{}); qk(IC<3>{}); }
    if constexpr (p == 9) qk(IC<4>{});
    if constexpr (p >= 9 && p < GROUPS + 4) qk(IC<p - 4>{});
    if constexpr (p == 14) {
      if (has_f2) {
        await_unit(fu, rs, fuoff, tagA, tagB);
        *(f32x2*)(ldsF + w * 128 + g * 32 + 2 * c) = f32x2{__uint_as_float(fu[0]), __uint_as_float(fu[2])};
        last_wave(ldsC + 5, lane);
      }
    }
    if constexpr (p == 16) {
      if (has_f2) {
        wait_all(ldsC + 5);
        float gs = 0.f, us = 0.f;
        const int hx = ((lane >> 2) & 1) * 32 + 4 * w + (lane & 3);
#pragma unroll
        for (int ww = 0; ww < NWAVE; ++ww) { gs += ldsF[ww * 128 + hx]; us += ldsF[ww * 128 + 64 + hx]; }
        const float hv = gelu_tanh(gs) * us;
        float dd[16];
#pragma unroll
        for (int k = 0; k < 16; ++k) dd[k] = 0.f;
#pragma unroll
        for (int r = 0; r < 4; ++r) {
          const float h0 = __builtin_bit_cast(float, __builtin_amdgcn_readlane(__builtin_bit_cast(int, hv), r));
          const float h1 = __builtin_bit_cast(float, __builtin_amdgcn_readlane(__builtin_bit_cast(int, hv), 4 + r));
#pragma unroll
          for (int k = 0; k < 4; ++k) {
            const float wl = lo(ffw[r][k]), wh = hi(ffw[r][k]);
            dd[2 * k] = fmaf(h0, wl, dd[2 * k]); dd[2 * k + 1] = fmaf(h0, wh, dd[2 * k + 1]);
            dd[8 + 2 * k] = fmaf(h1, wl, dd[8 + 2 * k]); dd[8 + 2 * k + 1] = fmaf(h1, wh, dd[8 + 2 * k + 1]);
          }
        }
        *(f32x4*)(ldsY + w * 1024 + 8 * lane) = f32x4{dd[0], dd[1], dd[2], dd[3]};
        *(f32x4*)(ldsY + w * 1024 + 8 * lane + 4) = f32x4{dd[4], dd[5], dd[6], dd[7]};
        *(f32x4*)(ldsY + w * 1024 + 512 + 8 * lane) = f32x4{dd[8], dd[9], dd[10], dd[11]};
        *(f32x4*)(ldsY + w * 1024 + 512 + 8 * lane + 4) = f32x4{dd[12], dd[13], dd[14], dd[15]};
        last_wave(ldsC + 3, lane);
      }
    }
    if constexpr (p == 18) {
      if (has_f2) {
        wait_all(ldsC + 3);
        f32x2 dv = {0.f, 0.f};
#pragma unroll
        for (int ww = 0; ww < NWAVE; ++ww) dv += *(const f32x2*)(ldsY + ww * 1024 + (tid & 256) * 2 + ucol(tid & 255));
        st_unit(rs, WS_DP + (cid * 512 + tid) * 16, dv[0], dv[1], tagA, tagB);
      }
    }
    if constexpr (p == 27) {
      if (is_fin && (FLG & 1)) last_wave(ldsC + 2, lane);
      if (is_fin && !(FLG & 1)) {
        await_unit(e0, rs, dpoff, tagA, tagB);
        await_unit(e1, rs, dpoff + 16, tagA, tagB);
        const bool hd = gp < NFFN;
        const float r = fold4(hd ? __uint_as_float(e0[0]) : 0.f, hd ? __uint_as_float(e0[2]) : 0.f, hd ? __uint_as_float(e1[0]) : 0.f, hd ? __uint_as_float(e1[2]) : 0.f);
        if (c < 4) ldsF[w * 16 + 4 * c + g] = r;
        last_wave(ldsC + 2, lane);
      }
    }
    if constexpr (p == GROUPS + 3) {
      float x = fmaxf(fmaxf(sp[0], sp[1]), fmaxf(sp[2], sp[3]));
      x = fmaxf(x, dpp<0x128>(x)); x = fmaxf(x, dpp<0x124>(x)); x = fmaxf(x, dpp<0x122>(x)); x = fmaxf(x, dpp<0x121>(x));
      mw = xrow_max(x) * C2;
#pragma unroll
      for (int i = 0; i < 4; ++i) sp[i] = __builtin_amdgcn_exp2f(fmaf(sp[i], C2, -mw));
      const float lw = xrow_sum(row16_sum((sp[0] + sp[1]) + (sp[2] + sp[3])));
      if (lane == 0) { ldsM[w] = mw; ldsM[8 + w] = lw; }
      last_wave(ldsC + 4, lane);
    }
    if constexpr (p == 21) {
      if (w == 0) {
        wait_all(ldsC + 4);
        const float mi = ldsM[lane & 7], li = ldsM[8 + (lane & 7)];
        float M = fmaxf(mi, dpp<0x124>(mi)); M = fmaxf(M, dpp<0x122>(M)); M = fmaxf(M, dpp<0x121>(M));
        float L = __builtin_amdgcn_exp2f(mi - M) * li; L += dpp<0x124>(L); L += dpp<0x122>(L); L += dpp<0x121>(L);
        if (lane == 0) st_unit(rs, WS_ML + pid * 16, M, L, tagA, tagB);
      }
    }
    if constexpr (p == 30) {
      if (is_fin && w == 0) {
        wait_all(ldsC + 2);
        const float dv = ldsF[c] + ldsF[16 + c] + ldsF[32 + c];
        gte = dv * __builtin_amdgcn_rcpf(1.f + __expf(-dv));
        asm volatile("" : "+v"(gte));
      }
    }
    if constexpr (p == 31) {
      await_unit(mu, rs, muoff, tagA, tagB);
      const float mi = __uint_as_float(mu[0]);
      float M = fmaxf(mi, dpp<0xB1>(mi)); M = fmaxf(M, dpp<0x4E>(M));
      float L = __builtin_amdgcn_exp2f(mi - M) * __uint_as_float(mu[2]); L += dpp<0xB1>(L); L += dpp<0x4E>(L);
      mw = __builtin_amdgcn_exp2f(mw - M) * __builtin_amdgcn_rcpf(L);
      asm volatile("" : "+v"(mw));
    }
    if constexpr (p >= 20 && p < 28) pv(IC<p - 20>{});
    if constexpr (p == 28 || p == 29) { pv(IC<2 * p - 48>{}); pv(IC<2 * p - 47>{}); }
    if constexpr (p == 30) pv(IC<12>{});
    if constexpr (p == 31) { pv(IC<13>{}); pv(IC<14>{}); pv(IC<15>{}); }
    if constexpr (p == 30) {
#pragma unroll
      for (int i = 0; i < 16; ++i) {
        const v2u r0 = wor[2 * i], r1 = wor[2 * i + 1];
        wb[0][i >> 2][i & 3] = __builtin_amdgcn_perm(r1[0], r0[0], 0x05040100u); wb[1][i >> 2][i & 3] = __builtin_amdgcn_perm(r1[0], r0[0], 0x07060302u);
        wb[2][i >> 2][i & 3] = __builtin_amdgcn_perm(r1[1], r0[1], 0x05040100u); wb[3][i >> 2][i & 3] = __builtin_amdgcn_perm(r1[1], r0[1], 0x07060302u);
      }
#pragma unroll
      for (int i = 0; i < 4; ++i) asm volatile("" : "+v"(wb[i][0]), "+v"(wb[i][1]), "+v"(wb[i][2]), "+v"(wb[i][3]));
    }
    SB();
  };
  static_for<2 * GROUPS>(point);
  MARK(34);

  {
    const f32x2 o2 = fold8(acc) * mw;
    const unsigned oh = pk_bf16(o2[0], o2[1]), ol = pk_bf16(o2[0] - lo(oh), o2[1] - hi(oh));
    ldsA[w * 68 + 4 * c + g] = oh; ldsA[(8 + w) * 68 + 4 * c + g] = ol;
  }
  __syncthreads();
  MARK(35);
  {
    v4u af[4];
#pragma unroll
    for (int ks = 0; ks < 4; ++ks) af[ks] = *(const v4u*)(ldsA + c * 68 + 16 * ks + 4 * g);
    f32x4 d[4];
#pragma unroll
    for (int t = 0; t < 4; ++t) d[t] = f32x4{0.f, 0.f, 0.f, 0.f};
#pragma unroll
    for (int ks = 0; ks < 4; ++ks)
#pragma unroll
      for (int t = 0; t < 4; ++t) d[t] = __builtin_amdgcn_mfma_f32_16x16x32_bf16(__builtin_bit_cast(bf16x8, af[ks]), __builtin_bit_cast(bf16x8, wb[t][ks]), d[t], 0, 0, 0);
    float y[4];
#pragma unroll
    for (int t = 0; t < 4; ++t) y[t] = (d[t][0] + d[t][1]) + (d[t][2] + d[t][3]);
    float u0 = fold32(y[0], y[2]), u1 = fold32(y[1], y[3]);
    u0 = fold16(u0, u0); u1 = fold16(u1, u1);
    if ((g & 1) == 0) st_unit(rs, WS_Y + (pid * 256 + 32 * w + 16 * (g >> 1) + c) * 16, u0, u1, tagA, tagB);
  }
  MARK(36);

  if (is_fin) {
    const int yoff = WS_Y + ((bc * 128 + gp) * 256 + cu0 + 2 * qd) * 16;
    __builtin_amdgcn_s_barrier();
    if (PDLY_) __builtin_amdgcn_s_sleep(PDLY_);
    MARK(38);
    v4u y0, y1;
    bool ok = false;
#pragma unroll 1
    for (unsigned spin = 0; spin < SPIN_CAP; ++spin) {
      asm volatile("" ::: "memory");
      if (!ok) {
        y0 = ld_unit(rs, yoff); y1 = ld_unit(rs, yoff + 16);
        ok = (((y0[1] ^ tagA) | (y1[1] ^ tagA)) | ((y0[3] ^ tagB) | (y1[3] ^ tagB))) == 0u;
      }
      if (__all(ok)) break;
      __builtin_amdgcn_s_sleep(1);
    }
    MARK(39);
    const float r = fold4(__uint_as_float(y0[0]), __uint_as_float(y0[2]), __uint_as_float(y1[0]), __uint_as_float(y1[2]));
    if (c < 4) ldsF[128 + w * 16 + 4 * c + g] = r;
    __syncthreads();
    if (tid < 16) {
      float sv = 0.f;
#pragma unroll
      for (int ww = 0; ww < NWAVE; ++ww) sv += ldsF[128 + ww * 16 + tid];
      const float o = __uint_as_float((unsigned)xo << 16) + sv * gte;
      const __bf16 ob = (__bf16)o;
      ((GAS unsigned short*)a.out)[ocol] = __builtin_bit_cast(unsigned short, ob);
      if (cid == 0 && tid == 0)
        __hip_atomic_store((GAS unsigned long long*)(a.ws + WS_EPOCH), ((unsigned long long)tagB << 32) | tagA, __ATOMIC_RELAXED, __HIP_MEMORY_SCOPE_AGENT);
    }
  }
  asm volatile(".Lk_all_end:");
}

extern "C" void kernel_launch(void* const* d_in, const int* in_sizes, int n_in, void* d_out, int out_size, void* d_ws, size_t ws_size,
                              hipStream_t stream) {
  Args a;
  a.x = (const unsigned short*)d_in[0];
  a.k = (const unsigned short*)d_in[1];
  a.v = (const unsigned short*)d_in[2];
  a.wq = (const unsigned short*)d_in[3];
  a.wo = (const unsigned short*)d_in[4];
  a.wg = (const unsigned short*)d_in[6];
  a.wu = (const unsigned short*)d_in[7];
  a.wd = (const unsigned short*)d_in[8];
  a.out = (unsigned short*)d_out;
  a.ws = (unsigned char*)d_ws;
  hipLaunchKernelGGL(k_all, dim3(256), dim3(NTHR), 0, stream, a);
}
```

```cpp
#include <hip/hip_runtime.h>
#include <hip/hip_bf16.h>
#include <stdint.h>

typedef unsigned v4u __attribute__((ext_vector_type(4)));
typedef float f32x4 __attribute__((ext_vector_type(4)));
typedef float f32x2 __attribute__((ext_vector_type(2)));
typedef __bf16 bf16x2 __attribute__((ext_vector_type(2)));
typedef __bf16 bf16x8 __attribute__((ext_vector_type(8)));
typedef unsigned v2u __attribute__((ext_vector_type(2)));
#define GAS __attribute__((address_space(1)))
#define CAS __attribute__((address_space(4)))

constexpr int D = 512, H = 32, SK = 8192, DH = 128, F = 1376;
constexpr int NSPLIT = 4, KEYS_PER_WG = SK / NSPLIT;
constexpr int NWAVE = 8, NTHR = NWAVE * 64;
constexpr int NFFN = F / 32;
constexpr int U = 4;
constexpr int GROUPS = KEYS_PER_WG / NWAVE / (4 * U);
#define PWO_ 1
constexpr int won(int p) { return p <= PWO_ ? 0 : p >= 28 ? 32 : (p - PWO_) * 32 / (28 - PWO_); }
#define ABLWO_ 0
#define WOAUX_ 0
#define GAP_ 0
#define GAPAT(p) ((p) == 4 || (p) == 18)
#define PWAUX_ 0
#define MSA_ 16
#define YSA_ 16
#define EX1_ 160
#define EX2_ 120
#define STEP(pt) (((unsigned)TPX & ~7u) + (unsigned)(EXT(pt)))
#define EXT(pt) ((pt) < 8 ? EX1_ : (pt) >= 13 && (pt) < 17 ? EX2_ : 0)
#define PDLY_ 8
#define LATE_PRIO_ 400
#define KNT_ 1
#define VNT_ 1
#define WNT_ 1
#define ONT_ 0
#define TP_ 1728
#define T0_ 4400

constexpr int WS_EPOCH = 0;
constexpr int WS_ML = 4096;
constexpr int WS_Y = WS_ML + 256 * 16;
constexpr int WS_DP = WS_Y + 256 * 256 * 16;
constexpr int WS_S1 = WS_DP + NFFN * 512 * 16;
constexpr int WS_END = WS_S1 + 32 * 8 * 4 * 64 * 16;
constexpr int OOB = 0x7ffffff0;
constexpr unsigned SPIN_CAP = 400000;

#define SB() __builtin_amdgcn_sched_barrier(0)

__device__ __forceinline__ float dot2(unsigned k, unsigned q, float acc) {
  return __builtin_amdgcn_fdot2_f32_bf16(__builtin_bit_cast(bf16x2, k), __builtin_bit_cast(bf16x2, q), acc, false);
}
template <int CTRL> __device__ __forceinline__ float dpp(float x) {
  return __builtin_bit_cast(float, __builtin_amdgcn_mov_dpp(__builtin_bit_cast(int, x), CTRL, 0xf, 0xf, true));
}
__device__ __forceinline__ float row16_sum(float x) {
  x += dpp<0x128>(x);
  x += dpp<0x124>(x);
  x += dpp<0x122>(x);
  x += dpp<0x121>(x);
  return x;
}
__device__ __forceinline__ float fold32(float a, float b) {
  auto t = __builtin_amdgcn_permlane32_swap(__float_as_uint(a), __float_as_uint(b), false, false);
  return __uint_as_float(t[0]) + __uint_as_float(t[1]);
}
__device__ __forceinline__ float fold16(float a, float b) {
  auto t = __builtin_amdgcn_permlane16_swap(__float_as_uint(a), __float_as_uint(b), false, false);
  return __uint_as_float(t[0]) + __uint_as_float(t[1]);
}
__device__ __forceinline__ float xrow_sum(float x) { x = fold16(x, x); return fold32(x, x); }
__device__ __forceinline__ float xrow_max(float x) {
  auto s = __builtin_amdgcn_permlane16_swap(__float_as_uint(x), __float_as_uint(x), false, false);
  x = fmaxf(__uint_as_float(s[0]), __uint_as_float(s[1]));
  auto t = __builtin_amdgcn_permlane32_swap(__float_as_uint(x), __float_as_uint(x), false, false);
  return fmaxf(__uint_as_float(t[0]), __uint_as_float(t[1]));
}
__device__ __forceinline__ f32x2 fold8(const float (&v)[8]) {
  const float u0 = fold32(v[0], v[4]), u1 = fold32(v[1], v[5]), u2 = fold32(v[2], v[6]), u3 = fold32(v[3], v[7]);
  return f32x2{fold16(u0, u2), fold16(u1, u3)};
}
__device__ __forceinline__ float fold4(float v0, float v1, float v2, float v3) {
  float r = fold16(fold32(v0, v2), fold32(v1, v3));
  r += dpp<0x124>(r); r += dpp<0x128>(r);
  return r;
}
__device__ __forceinline__ float lo(unsigned u) { return __uint_as_float(u << 16); }
__device__ __forceinline__ float hi(unsigned u) { return __uint_as_float(u & 0xffff0000u); }
__device__ __forceinline__ unsigned pk_bf16(float a, float b) {
  bf16x2 r = {(__bf16)a, (__bf16)b};
  return __builtin_bit_cast(unsigned, r);
}
__device__ __forceinline__ unsigned sel4(v4u v, int g) { const unsigned a = (g & 1) ? v[1] : v[0], b = (g & 1) ? v[3] : v[2]; return (g & 2) ? b : a; }
__device__ __forceinline__ int ucol(int u) { return 64 * (u >> 5) + 4 * (u & 15) + 2 * ((u >> 4) & 1); }
__device__ __forceinline__ int s1off(int hg, int r, int k, int rotf) { return rotf ? WS_S1 + (hg * 8 + ((r + hg) & 7)) * 4096 + ((k ^ r ^ (hg >> 3)) & 3) * 1024 : WS_S1 + (hg * 8 + r) * 4096 + (k ^ (r & 1) * 2) * 1024; }
template <int NT> __device__ __forceinline__ v4u ldp(const GAS v4u* p) { if (NT) return __builtin_nontemporal_load(p); else return *p; }
__device__ __forceinline__ float gelu_tanh(float x) {
  const float u = 0.7978845608028654f * (x + 0.044715f * x * x * x);
  const float e = __expf(2.f * u);
  const float t = 1.f - 2.f * __builtin_amdgcn_rcpf(e + 1.f);
  return 0.5f * x * (1.f + t);
}
__device__ __forceinline__ void pace_wait(unsigned& due16, unsigned step16) {
  asm volatile("1:\n\ts_memrealtime s[90:91]\n\ts_waitcnt lgkmcnt(0)\n\ts_lshl_b32 s90, s90, 4\n\ts_sub_u32 s91, s90, %0\n\ts_cmp_ge_i32 s91, 0\n\ts_cbranch_scc1 2f\n\ts_sleep 2\n\ts_branch 1b\n"
               "2:\n\ts_add_u32 %0, %0, %1\n\ts_setprio 0\n\ts_cmp_lt_i32 s91, %2\n\ts_cbranch_scc1 3f\n\ts_setprio 2\n3:"
               : "+s"(due16) : "s"(step16), "i"(LATE_PRIO_) : "s90", "s91", "scc");
}
__device__ __forceinline__ unsigned now16() { return (unsigned)__builtin_amdgcn_s_memrealtime() << 4; }
#define MARK(i)
#define TPX TP_
#define T0X T0_
#define FLG 0
#define DT2X DT2_
#define SPRD 0
#define ROTF 1
#define PFC_ 1
#define GAPX GAP_
#define PACE(pt) pace_wait(due, STEP(pt))
constexpr int AUX_SC1 = 16;
template <int AUX = MSA_> __device__ __forceinline__ void st_unit(__amdgpu_buffer_rsrc_t rs, int off, float v0, float v1, unsigned ta, unsigned tb) {
  __builtin_amdgcn_raw_buffer_store_b128(v4u{__float_as_uint(v0), ta, __float_as_uint(v1), tb}, rs, off, 0, AUX);
}
__device__ __forceinline__ v4u ld_unit(__amdgpu_buffer_rsrc_t rs, int off) { return __builtin_amdgcn_raw_buffer_load_b128(rs, off, 0, AUX_SC1); }
__device__ __forceinline__ void await_unit(v4u& u, __amdgpu_buffer_rsrc_t rs, int off, unsigned ta, unsigned tb) {
  if (!__all(((u[1] ^ ta) | (u[3] ^ tb)) == 0u)) {
#pragma unroll 1
    for (unsigned spin = 0; spin < SPIN_CAP; ++spin) {
      __builtin_amdgcn_s_sleep(1);
      asm volatile("" ::: "memory");
      u = ld_unit(rs, off);
      if (__all(((u[1] ^ ta) | (u[3] ^ tb)) == 0u)) break;
    }
  }
}

__device__ __forceinline__ bool last_wave(unsigned* cnt, int lane) {
  asm volatile("s_waitcnt lgkmcnt(0)" ::: "memory");
  unsigned old = 0;
  if (lane == 0) old = __hip_atomic_fetch_add(cnt, 1u, __ATOMIC_RELAXED, __HIP_MEMORY_SCOPE_WORKGROUP);
  old = __builtin_amdgcn_readfirstlane(old);
  asm volatile("" ::: "memory");
  return old == NWAVE - 1;
}

__device__ __forceinline__ void wait_all(unsigned* cnt, unsigned n = NWAVE) {
  while (__builtin_amdgcn_readfirstlane(__hip_atomic_load(cnt, __ATOMIC_RELAXED, __HIP_MEMORY_SCOPE_WORKGROUP)) < n) __builtin_amdgcn_s_sleep(1);
  asm volatile("" ::: "memory");
}
template <int I> struct IC { static constexpr int value = I; };
template <int N, int I = 0, typename Fn> __device__ __forceinline__ void static_for(Fn& fn) { if constexpr (I < N) { fn(IC<I>{}); static_for<N, I + 1>(fn); } }

struct Args {
  const unsigned short* x; const unsigned short* wq;
  unsigned char* ws; const unsigned short* v; const unsigned short* wo; const unsigned short* wg;
  const unsigned short* wu; const unsigned short* wd; unsigned short* out; const unsigned short* k;
};

__global__ void __launch_bounds__(NTHR, 2) k_all(Args a) {
  __shared__ __attribute__((aligned(16))) float lds[8 * 1024 + 16 * 68 + 16 + 8 * 128 + 8 * 128 + 32 + 16];
  float* ldsY = lds;
  unsigned* ldsA = (unsigned*)(lds + 8 * 1024);
  float* ldsM = lds + 8 * 1024 + 16 * 68;
  float* ldsF = ldsM + 16;
  float* ldsQ = ldsF + 8 * 128;
  float* ldsS = ldsQ + 8 * 128;
  unsigned* ldsC = (unsigned*)(ldsS + 32);
  const unsigned tstart = now16();
  MARK(32);
  const int bid = blockIdx.x;
  const int xcd = bid & 7, j = bid >> 3;
  const int h = xcd * 4 + (j >> 3), b = (j >> 2) & 1, s = j & 3;
  const int tid = threadIdx.x, lane = tid & 63, w = __builtin_amdgcn_readfirstlane(tid >> 6);
  const int g = lane >> 4, c = lane & 15;
  const unsigned long long tptr = ((const CAS unsigned long long*)__builtin_amdgcn_kernarg_segment_ptr())[2 + w];
  asm volatile("" :: "s"(tptr), "s"(a.k), "s"(a.x), "s"(a.wq), "s"(tstart));
  if (tid < 3) *(v4u*)(ldsC + 4 * tid) = v4u{0u, 0u, tid == 2 ? tstart : 0u, 0u};
  const int r8 = j & 7;
  const int fb = xcd * 3 + (j >> 3);
  const bool has_f1 = (j < 24) && (fb < 22) && !(FLG & 1);
  const bool is_fin = j >= 24;
  const int cid = xcd * 8 + (j - 24);
  const bool has_f2 = is_fin && cid < NFFN && !(FLG & 1);
  const __amdgpu_buffer_rsrc_t rs = __builtin_amdgcn_make_buffer_rsrc((void*)a.ws, 0, WS_END, 0x00020000);

  const int row0 = 64 * r8 + 8 * w + 2 * g, lo16 = lane * 16;
  v4u sw0, sw1;
  {
    const GAS v4u* p = (const GAS v4u*)(a.wq + (size_t)row0 * (H * DH) + h * DH + 8 * c);
    sw0 = ldp<WNT_>(p); sw1 = ldp<WNT_>(p + H * DH / 8);
  }
  const unsigned xst = *(const GAS unsigned*)(a.x + (lane >> 5) * D + 64 * w + 2 * (lane & 31));
  const unsigned long long ep = __hip_atomic_load((const GAS unsigned long long*)(tptr + ((unsigned)(b * H + h) * ((0x82u >> w) & 1u) << 21) + ((unsigned)h * ((0x04u >> w) & 1u) << 17)),
                                                  __ATOMIC_RELAXED, __HIP_MEMORY_SCOPE_AGENT);
  const __amdgpu_buffer_rsrc_t rf = __builtin_amdgcn_make_buffer_rsrc((void*)(!has_f1 ? a.wd : fb < 11 ? a.wg : a.wu), 0, (has_f1 || has_f2) ? D * F * 2 : 0, 0x00020000);
  const int fcol = 128 * (fb < 11 ? fb : fb - 11) + 8 * c;
  const int fxo = !has_f1 ? (32 * cid + 4 * w) * D * 2 + lo16 : fcol < F ? (row0 * F + fcol) * 2 : OOB - 4 * F * 2, fxo2 = has_f1 ? OOB - 4096 : fxo;
  const __amdgpu_buffer_rsrc_t rw = __builtin_amdgcn_make_buffer_rsrc((void*)(a.wo + (size_t)h * DH * D), 0, DH * D * 2, 0x00020000);
  asm volatile("s_waitcnt lgkmcnt(0)\n\ts_barrier" ::: "memory");
  v4u xq0, xq1;
  asm volatile("s_load_dwordx4 %0, %2, 0x0\n\ts_load_dwordx4 %1, %2, 0x400" : "=&s"(xq0), "=&s"(xq1) : "s"(a.x + 64 * r8 + 8 * w));
  v4u ffw[4];
  ffw[0] = __builtin_amdgcn_raw_buffer_load_b128(rf, fxo, 0, WNT_ ? 2 : 0);
  ffw[1] = __builtin_amdgcn_raw_buffer_load_b128(rf, fxo, has_f1 ? F * 2 : D * 2, WNT_ ? 2 : 0);
  const v4u pf0 = __builtin_amdgcn_raw_buffer_load_b128(rw, lo16, (16 * r8 + 2 * w) * 1024, 0), pf1 = __builtin_amdgcn_raw_buffer_load_b128(rw, lo16 + 1024, (16 * r8 + 2 * w) * 1024, 0);
  unsigned long long pc0; unsigned csz;
  asm volatile("s_getpc_b64 %0\n.Lk_all_pc0:\n\ts_mov_b32 %1, .Lk_all_end-.Lk_all_pc0" : "=s"(pc0), "=s"(csz));
  const v4u pfc = __builtin_amdgcn_raw_buffer_load_b128(__builtin_amdgcn_make_buffer_rsrc((void*)(pc0 & ~127ull), 0, (w == 3 && PFC_) ? (csz & ~15u) : 0u, 0x00020000), lo16, j * 1024, 0);
  ffw[2] = __builtin_amdgcn_raw_buffer_load_b128(rf, fxo2 + 2048, 0, WNT_ ? 2 : 0);
  ffw[3] = __builtin_amdgcn_raw_buffer_load_b128(rf, fxo2 + 3072, 0, WNT_ ? 2 : 0);
  SB();

  const __amdgpu_buffer_rsrc_t rk = __builtin_amdgcn_make_buffer_rsrc((void*)(a.k + (size_t)(b * H + h) * SK * DH), 0, SK * DH * 2, 0x00020000);
  const __amdgpu_buffer_rsrc_t rv = __builtin_amdgcn_make_buffer_rsrc((void*)(a.v + (size_t)(b * H + h) * SK * DH), 0, SK * DH * 2, 0x00020000);
  const unsigned slot0 = (__builtin_amdgcn_readfirstlane(ldsC[10]) + (unsigned)T0X) / (unsigned)(TPX / 8) + (unsigned)w;
  const int kvo = ((slot0 & 1) << 13) | ((slot0 & 6) << 14) | ((s & 1) << 12) | ((s >> 1) << 14);
  unsigned due = slot0 * (unsigned)(TPX / 8);
#define GOFF(gi_) ((((gi_) & 15) << 17) | (((gi_) & 4) << 12) | (((gi_) & 8) << 9))
  v4u rg[2 * GROUPS][U];

  constexpr float C2 = 0.08838834764831845f * 1.4426950408889634f;
  float sp[4] = {0.f, 0.f, 0.f, 0.f};
  float mw = 0.f;
  f32x2 bs[8];
  unsigned tagA, tagB, qp[4];
  v2u wor[32]; v4u wb[4][4], fu, qu, mu, e0, e1; unsigned short xo; float gte = 0.f;
  asm volatile("s_waitcnt lgkmcnt(0)" : "+s"(xq0), "+s"(xq1));
  const unsigned xs0 = sel4(xq0, g), xs1 = sel4(xq1, g);
  int fuoff;
  { const int fbs = (g >> 1) * 11 + (cid >> 2), hs = (fbs / 3) * 4 + fbs % 3;
    fuoff = has_f2 ? s1off(hs, w, 2 + (g & 1), ROTF) + (16 * (cid & 3) + c) * 16 : OOB; }
  const int wvo = g * 8192 + w * 128 + c * 8 + ((FLG & 32) ? 0x1000000 : 0);
  const int qoff = s1off(h, w, b, ROTF) + lo16;
  const int pid = (b * H + h) * NSPLIT + s, muoff = WS_ML + ((pid & ~3) + (lane & 3)) * 16;
  const int bc = (cid >> 5) & 1, cu0 = (cid & 31) * 8;
  const int gp = tid >> 2, qd = tid & 3, ocol = bc * D + ucol(cu0 + (c >> 1)) + (c & 1);
  const int dpoff = WS_DP + ((gp < NFFN ? gp : 0) * 512 + bc * 256 + cu0 + 2 * qd) * 16;

  auto qk = [&](auto gic) __attribute__((always_inline)) {
    constexpr int gi = decltype(gic)::value;
    float d[U];
#pragma unroll
    for (int u = 0; u < U; ++u) d[u] = dot2(rg[gi][u][3], qp[3], dot2(rg[gi][u][2], qp[2], dot2(rg[gi][u][1], qp[1], dot2(rg[gi][u][0], qp[0], 0.f))));
    const bool o1 = c & 1, o2 = c & 2;
    const float x0 = (o1 ? d[1] : d[0]) + dpp<0xB1>(o1 ? d[0] : d[1]), x1 = (o1 ? d[3] : d[2]) + dpp<0xB1>(o1 ? d[2] : d[3]);
    float x = (o2 ? x1 : x0) + dpp<0x4E>(o2 ? x0 : x1);
    x += dpp<0x128>(x); x += dpp<0x124>(x);
    sp[gi / 4] = __builtin_bit_cast(float, __builtin_amdgcn_update_dpp(__builtin_bit_cast(int, sp[gi / 4]), __builtin_bit_cast(int, x), 0xE4, 0xf, 1 << (gi % 4), false));
    asm volatile("" : "+v"(sp[gi / 4]));
    SB();
  };
  auto pv = [&](auto gic) __attribute__((always_inline)) {
    constexpr int gi = decltype(gic)::value;
    const float p[4] = {dpp<0x150 + 4 * (gi % 4)>(sp[gi / 4]), dpp<0x151 + 4 * (gi % 4)>(sp[gi / 4]), dpp<0x152 + 4 * (gi % 4)>(sp[gi / 4]), dpp<0x153 + 4 * (gi % 4)>(sp[gi / 4])};
    float x[8];
#pragma unroll
    for (int k = 0; k < 4; ++k) { x[2 * k] = p[0] * lo(rg[GROUPS + gi][0][k]); x[2 * k + 1] = p[0] * hi(rg[GROUPS + gi][0][k]); }
#pragma unroll
    for (int u = 1; u < U; ++u) {
#pragma unroll
      for (int k = 0; k < 4; ++k) { x[2 * k] = fmaf(p[u], lo(rg[GROUPS + gi][u][k]), x[2 * k]); x[2 * k + 1] = fmaf(p[u], hi(rg[GROUPS + gi][u][k]), x[2 * k + 1]); }
    }
    const f32x2 y = fold8(x);
    if constexpr (gi < 8) bs[gi] = y; else bs[gi - 8] += y;
    if constexpr (gi >= 12) bs[gi - 12] += bs[gi - 8];
    if constexpr (gi >= 14) bs[gi - 14] += bs[gi - 12];
    asm volatile("" : "+v"(bs[gi >= 14 ? gi - 14 : gi >= 12 ? gi - 12 : gi % 8]));
    SB();
  };

  {
    float sm = row16_sum(lo(xst) + hi(xst)); sm = fold16(sm, sm) * (1.f / 64);
    const float d0 = lo(xst) - sm, d1 = hi(xst) - sm;
    float m2 = row16_sum(fmaf(d0, d0, d1 * d1)); m2 = fold16(m2, m2);
    if ((lane & 31) == 0) *(f32x2*)(ldsS + (lane >> 5) * 16 + 2 * w) = f32x2{sm, m2};
    float t[24];
#pragma unroll
    for (int k = 0; k < 4; ++k) {
      const float al = lo(sw0[k]), ah = hi(sw0[k]), bl = lo(sw1[k]), bh = hi(sw1[k]);
      t[2 * k] = fmaf(hi(xs0), bl, lo(xs0) * al); t[2 * k + 1] = fmaf(hi(xs0), bh, lo(xs0) * ah);
      t[8 + 2 * k] = fmaf(hi(xs1), bl, lo(xs1) * al); t[8 + 2 * k + 1] = fmaf(hi(xs1), bh, lo(xs1) * ah);
      t[16 + 2 * k] = al + bl; t[16 + 2 * k + 1] = ah + bh;
    }
#pragma unroll
    for (int k = 0; k < 8; ++k) t[k] = fold16(fold32(t[k], t[16 + k]), fold32(t[8 + k], t[16 + k]));
    *(f32x4*)(ldsY + w * 512 + g * 128 + 8 * c) = f32x4{t[0], t[1], t[2], t[3]};
    *(f32x4*)(ldsY + w * 512 + g * 128 + 8 * c + 4) = f32x4{t[4], t[5], t[6], t[7]};
    if (tid == 0) *(unsigned long long*)(ldsC + 8) = ep;
    asm volatile("" :: "v"(ep));
    if (last_wave(ldsC, lane)) {
      f32x4 st[8];
#pragma unroll
      for (int i = 0; i < 8; ++i) st[i] = *(const f32x4*)(ldsS + 4 * i);
      tagA = __builtin_amdgcn_readfirstlane(ldsC[8]) + 1u; tagB = __builtin_amdgcn_readfirstlane(ldsC[9]) + 0x9E3779B9u;
      const float mean0 = ((st[0][0] + st[0][2]) + (st[1][0] + st[1][2]) + (st[2][0] + st[2][2]) + (st[3][0] + st[3][2])) * 0.125f;
      const float mean1 = ((st[4][0] + st[4][2]) + (st[5][0] + st[5][2]) + (st[6][0] + st[6][2]) + (st[7][0] + st[7][2])) * 0.125f;
      float q0 = 0.f, q1 = 0.f, d0 = 0.f, d1 = 0.f;
#pragma unroll
      for (int i = 0; i < 4; ++i) {
        q0 += st[i][1] + st[i][3]; q1 += st[4 + i][1] + st[4 + i][3];
        const float a0 = st[i][0] - mean0, b0 = st[i][2] - mean0, a1 = st[4 + i][0] - mean1, b1 = st[4 + i][2] - mean1;
        d0 += fmaf(a0, a0, b0 * b0); d1 += fmaf(a1, a1, b1 * b1);
      }
      const float rstd0 = __builtin_amdgcn_rsqf(fmaf(64.f, d0, q0) * (1.f / D) + 1e-5f), rstd1 = __builtin_amdgcn_rsqf(fmaf(64.f, d1, q1) * (1.f / D) + 1e-5f);
      if (lane == 0) *(f32x4*)(ldsC + 12) = f32x4{mean0, rstd0, mean1, rstd1};
      last_wave(ldsC, lane);
      f32x2 z0 = {0.f, 0.f}, z1 = {0.f, 0.f}, cs = {0.f, 0.f};
#pragma unroll
      for (int ww = 0; ww < NWAVE; ++ww) {
        z0 += *(const f32x2*)(ldsY + ww * 512 + 2 * lane); z1 += *(const f32x2*)(ldsY + ww * 512 + 128 + 2 * lane); cs += *(const f32x2*)(ldsY + ww * 512 + 256 + 2 * lane); }
      if (!(FLG & 16)) st_unit(rs, s1off(h, r8, 0, ROTF) + lo16, (z0[0] - mean0 * cs[0]) * rstd0, (z0[1] - mean0 * cs[1]) * rstd0, tagA, tagB);
      if (!(FLG & 16)) st_unit(rs, s1off(h, r8, 1, ROTF) + lo16, (z1[0] - mean1 * cs[0]) * rstd1, (z1[1] - mean1 * cs[1]) * rstd1, tagA, tagB);
    }
  }
  MARK(33);

  auto point = [&](auto pc) __attribute__((always_inline)) {
    constexpr int p = decltype(pc)::value;
    const int so = kvo ^ GOFF((slot0 >> 3) + p);
    PACE(p);
    SB();
#pragma unroll
    for (int u = 0; u < U; ++u) rg[p][u] = __builtin_amdgcn_raw_buffer_load_b128(p < GROUPS ? rk : rv, lo16 + u * 1024, so, 2);
    SB();
    if constexpr (p == 3) qu = ld_unit(rs, ((FLG & 8) || (SPRD && (h & 1))) ? OOB : qoff);
    if constexpr (p == 4) { if (SPRD && (h & 1)) qu = ld_unit(rs, qoff); }
    if constexpr (p == 6) fu = ld_unit(rs, fuoff);
    if constexpr (p == 29) mu = ld_unit(rs, muoff);
#pragma unroll
    for (int i = won(p); i < won(p + 1); ++i) wor[i] = __builtin_amdgcn_raw_buffer_load_b64(rw, wvo + ABLWO_, (32 * (i >> 3) + (i & 7)) * 1024, WOAUX_);
    if constexpr (p == 16) {
      e0 = ld_unit(rs, is_fin ? dpoff : OOB); e1 = ld_unit(rs, is_fin ? dpoff + 16 : OOB);
      xo = ((const GAS unsigned short*)a.x)[ocol];
    }
    SB();
    if constexpr (p == 1) {
      wait_all(ldsC, NWAVE + 1);
      tagA = __builtin_amdgcn_readfirstlane(ldsC[8]) + 1u; tagB = __builtin_amdgcn_readfirstlane(ldsC[9]) + 0x9E3779B9u;
      asm volatile("" :: "v"(pf0), "v"(pf1), "v"(pfc));
      if (has_f1) {
        const f32x4 ln = *(const f32x4*)(ldsC + 12);
        const float n00 = (lo(xs0) - ln[0]) * ln[1], n01 = (hi(xs0) - ln[0]) * ln[1], n10 = (lo(xs1) - ln[2]) * ln[3], n11 = (hi(xs1) - ln[2]) * ln[3];
        float t[8];
#pragma unroll
        for (int k = 0; k < 4; ++k) {
          const float al = lo(ffw[0][k]), ah = hi(ffw[0][k]), bl = lo(ffw[1][k]), bh = hi(ffw[1][k]);
          t[2 * k] = fold32(fmaf(n01, bl, n00 * al), fmaf(n11, bl, n10 * al)); t[2 * k + 1] = fold32(fmaf(n01, bh, n00 * ah), fmaf(n11, bh, n10 * ah));
        }
        *(f32x4*)(ldsY + 4096 + w * 256 + (g >> 1) * 128 + 8 * c + 4 * (g & 1)) = f32x4{fold16(t[0], t[4]), fold16(t[1], t[5]), fold16(t[2], t[6]), fold16(t[3], t[7])};
        last_wave(ldsC + 2, lane);
      }
    }
    if constexpr (p == 3) {
      if (has_f1 && w < 2) {
        wait_all(ldsC + 2);
        f32x2 z = {0.f, 0.f};
#pragma unroll
        for (int ww = 0; ww < NWAVE; ++ww) z += *(const f32x2*)(ldsY + 4096 + ww * 256 + 2 * tid);
        st_unit(rs, s1off(h, r8, 2 + w, ROTF) + lo16, z[0], z[1], tagA, tagB);
      }
    }
    if constexpr (p == 6) {
      if (!(FLG & 8)) await_unit(qu, rs, qoff, tagA, tagB);
      *(f32x2*)(ldsQ + w * 128 + 2 * lane) = f32x2{__uint_as_float(qu[0]), __uint_as_float(qu[2])};
      last_wave(ldsC + 1, lane);
    }
    if constexpr (p == 8) {
      wait_all(ldsC + 1);
      f32x4 q0 = {0, 0, 0, 0}, q1 = {0, 0, 0, 0};
#pragma unroll
      for (int ww = 0; ww < NWAVE; ++ww) { q0 += *(const f32x4*)(ldsQ + ww * 128 + 8 * c); q1 += *(const f32x4*)(ldsQ + ww * 128 + 8 * c + 4); }
      qp[0] = pk_bf16(q0[0], q0[1]); qp[1] = pk_bf16(q0[2], q0[3]); qp[2] = pk_bf16(q1[0], q1[1]); qp[3] = pk_bf16(q1[2], q1[3]);
      qk(IC<0>{});
    }
    if constexpr (p == 9) { qk(IC<1>{}); qk(IC<2>{}); qk(IC<3>{}); }
    if constexpr (p == 10) { qk(IC<4>{}); qk(IC<5>{}); }
    if constexpr (p >= 10 && p < GROUPS + 4) qk(IC<p - 4>{});
    if constexpr (p == 9) {
      if (has_f2) {
        await_unit(fu, rs, fuoff, tagA, tagB);
        *(f32x2*)(ldsF + w * 128 + g * 32 + 2 * c) = f32x2{__uint_as_float(fu[0]), __uint_as_float(fu[2])};
        last_wave(ldsC + 5, lane);
      }
    }
    if constexpr (p == 11) {
      if (has_f2) {
        wait_all(ldsC + 5);
        float gs = 0.f, us = 0.f;
        const int hx = ((lane >> 2) & 1) * 32 + 4 * w + (lane & 3);
#pragma unroll
        for (int ww = 0; ww < NWAVE; ++ww) { gs += ldsF[ww * 128 + hx]; us += ldsF[ww * 128 + 64 + hx]; }
        const float hv = gelu_tanh(gs) * us;
        float dd[16];
#pragma unroll
        for (int k = 0; k < 16; ++k) dd[k] = 0.f;
#pragma unroll
        for (int r = 0; r < 4; ++r) {
          const float h0 = __builtin_bit_cast(float, __builtin_amdgcn_readlane(__builtin_bit_cast(int, hv), r));
          const float h1 = __builtin_bit_cast(float, __builtin_amdgcn_readlane(__builtin_bit_cast(int, hv), 4 + r));
#pragma unroll
          for (int k = 0; k < 4; ++k) {
            const float wl = lo(ffw[r][k]), wh = hi(ffw[r][k]);
            dd[2 * k] = fmaf(h0, wl, dd[2 * k]); dd[2 * k + 1] = fmaf(h0, wh, dd[2 * k + 1]);
            dd[8 + 2 * k] = fmaf(h1, wl, dd[8 + 2 * k]); dd[8 + 2 * k + 1] = fmaf(h1, wh, dd[8 + 2 * k + 1]);
          }
        }
        *(f32x4*)(ldsY + w * 1024 + 8 * lane) = f32x4{dd[0], dd[1], dd[2], dd[3]};
        *(f32x4*)(ldsY + w * 1024 + 8 * lane + 4) = f32x4{dd[4], dd[5], dd[6], dd[7]};
        *(f32x4*)(ldsY + w * 1024 + 512 + 8 * lane) = f32x4{dd[8], dd[9], dd[10], dd[11]};
        *(f32x4*)(ldsY + w * 1024 + 512 + 8 * lane + 4) = f32x4{dd[12], dd[13], dd[14], dd[15]};
        last_wave(ldsC + 3, lane);
      }
    }
    if constexpr (p == 13) {
      if (has_f2) {
        wait_all(ldsC + 3);
        f32x2 dv = {0.f, 0.f};
#pragma unroll
        for (int ww = 0; ww < NWAVE; ++ww) dv += *(const f32x2*)(ldsY + ww * 1024 + (tid & 256) * 2 + ucol(tid & 255));
        st_unit(rs, WS_DP + (cid * 512 + tid) * 16, dv[0], dv[1], tagA, tagB);
      }
    }
    if constexpr (p == 23) {
      if (is_fin && (FLG & 1)) last_wave(ldsC + 2, lane);
      if (is_fin && !(FLG & 1)) {
        await_unit(e0, rs, dpoff, tagA, tagB);
        await_unit(e1, rs, dpoff + 16, tagA, tagB);
        const bool hd = gp < NFFN;
        const float r = fold4(hd ? __uint_as_float(e0[0]) : 0.f, hd ? __uint_as_float(e0[2]) : 0.f, hd ? __uint_as_float(e1[0]) : 0.f, hd ? __uint_as_float(e1[2]) : 0.f);
        if (c < 4) ldsF[w * 16 + 4 * c + g] = r;
        last_wave(ldsC + 2, lane);
      }
    }
    if constexpr (p == GROUPS + 3) {
      float x = fmaxf(fmaxf(sp[0], sp[1]), fmaxf(sp[2], sp[3]));
      x = fmaxf(x, dpp<0x128>(x)); x = fmaxf(x, dpp<0x124>(x)); x = fmaxf(x, dpp<0x122>(x)); x = fmaxf(x, dpp<0x121>(x));
      mw = xrow_max(x) * C2;
#pragma unroll
      for (int i = 0; i < 4; ++i) sp[i] = __builtin_amdgcn_exp2f(fmaf(sp[i], C2, -mw));
      const float lw = xrow_sum(row16_sum((sp[0] + sp[2]) + (sp[1] + sp[3])));
      if (lane == 0) { ldsM[slot0 & 7] = mw; ldsM[8 + (slot0 & 7)] = lw; }
      last_wave(ldsC + 4, lane);
    }
    if constexpr (p == 21) {
      if (w == 0) {
        wait_all(ldsC + 4);
        const float mi = ldsM[lane & 7], li = ldsM[8 + (lane & 7)];
        float M = fmaxf(mi, dpp<0x124>(mi)); M = fmaxf(M, dpp<0x122>(M)); M = fmaxf(M, dpp<0x121>(M));
        float L = __builtin_amdgcn_exp2f(mi - M) * li; L += dpp<0x124>(L); L += dpp<0x122>(L); L += dpp<0x121>(L);
        if (lane == 0) st_unit(rs, WS_ML + pid * 16, M, L, tagA, tagB);
      }
    }
    if constexpr (p == 26) {
      if (is_fin && w == 0) {
        wait_all(ldsC + 2);
        const float dv = ldsF[c] + ldsF[16 + c] + ldsF[32 + c];
        gte = dv * __builtin_amdgcn_rcpf(1.f + __expf(-dv));
        asm volatile("" : "+v"(gte));
      }
    }
    if constexpr (p == 31) {
      await_unit(mu, rs, muoff, tagA, tagB);
      const float mi = __uint_as_float(mu[0]);
      float M = fmaxf(mi, dpp<0xB1>(mi)); M = fmaxf(M, dpp<0x4E>(M));
      float L = __builtin_amdgcn_exp2f(mi - M) * __uint_as_float(mu[2]); L += dpp<0xB1>(L); L += dpp<0x4E>(L);
      mw = __builtin_amdgcn_exp2f(mw - M) * __builtin_amdgcn_rcpf(L);
      asm volatile("" : "+v"(mw));
    }
    if constexpr (p >= 20 && p < 28) pv(IC<p - 20>{});
    if constexpr (p == 28 || p == 29) { pv(IC<2 * p - 48>{}); pv(IC<2 * p - 47>{}); }
    if constexpr (p == 30) pv(IC<12>{});
    if constexpr (p == 31) { pv(IC<13>{}); pv(IC<14>{}); pv(IC<15>{}); }
    if constexpr (p == 30) {
#pragma unroll
      for (int i = 0; i < 16; ++i) {
        const v2u r0 = wor[2 * i], r1 = wor[2 * i + 1];
        wb[0][i >> 2][i & 3] = __builtin_amdgcn_perm(r1[0], r0[0], 0x05040100u); wb[1][i >> 2][i & 3] = __builtin_amdgcn_perm(r1[0], r0[0], 0x07060302u);
        wb[2][i >> 2][i & 3] = __builtin_amdgcn_perm(r1[1], r0[1], 0x05040100u); wb[3][i >> 2][i & 3] = __builtin_amdgcn_perm(r1[1], r0[1], 0x07060302u);
      }
#pragma unroll
      for (int i = 0; i < 4; ++i) asm volatile("" : "+v"(wb[i][0]), "+v"(wb[i][1]), "+v"(wb[i][2]), "+v"(wb[i][3]));
    }
    SB();
  };
  static_for<2 * GROUPS>(point);
  MARK(34);

  {
    const f32x2 o2 = (bs[0] + bs[1]) * mw;
    const unsigned oh = pk_bf16(o2[0], o2[1]), ol = pk_bf16(o2[0] - lo(oh), o2[1] - hi(oh));
    const int ar = (slot0 & 7) * 68 + 4 * c + g;
    ldsA[ar] = oh; ldsA[8 * 68 + ar] = ol;
  }
  __syncthreads();
  MARK(35);
  {
    v4u af[4];
#pragma unroll
    for (int ks = 0; ks < 4; ++ks) af[ks] = *(const v4u*)(ldsA + c * 68 + 16 * ks + 4 * g);
    f32x4 d[4];
#pragma unroll
    for (int t = 0; t < 4; ++t) d[t] = f32x4{0.f, 0.f, 0.f, 0.f};
#pragma unroll
    for (int ks = 0; ks < 4; ++ks)
#pragma unroll
      for (int t = 0; t < 4; ++t) d[t] = __builtin_amdgcn_mfma_f32_16x16x32_bf16(__builtin_bit_cast(bf16x8, af[ks]), __builtin_bit_cast(bf16x8, wb[t][ks]), d[t], 0, 0, 0);
    float y[4];
#pragma unroll
    for (int t = 0; t < 4; ++t) y[t] = (d[t][0] + d[t][1]) + (d[t][2] + d[t][3]);
    float u0 = fold32(y[0], y[2]), u1 = fold32(y[1], y[3]);
    u0 = fold16(u0, u0); u1 = fold16(u1, u1);
    if ((g & 1) == 0) st_unit<YSA_>(rs, WS_Y + (pid * 256 + 32 * w + 16 * (g >> 1) + c) * 16, u0, u1, tagA, tagB);
  }
  MARK(36);

  if (is_fin) {
    const int yoff = WS_Y + ((bc * 128 + gp) * 256 + cu0 + 2 * qd) * 16;
    __builtin_amdgcn_s_barrier();
    if (PDLY_) __builtin_amdgcn_s_sleep(PDLY_);
    MARK(38);
    v4u y0, y1;
    bool ok = false;
#pragma unroll 1
    for (unsigned spin = 0; spin < SPIN_CAP; ++spin) {
      asm volatile("" ::: "memory");
      if (!ok) {
        y0 = ld_unit(rs, yoff); y1 = ld_unit(rs, yoff + 16);
        ok = (((y0[1] ^ tagA) | (y1[1] ^ tagA)) | ((y0[3] ^ tagB) | (y1[3] ^ tagB))) == 0u;
      }
      if (__all(ok)) break;
      __builtin_amdgcn_s_sleep(1);
    }
    MARK(39);
    const float r = fold4(__uint_as_float(y0[0]), __uint_as_float(y0[2]), __uint_as_float(y1[0]), __uint_as_float(y1[2]));
    if (c < 4) ldsF[128 + w * 16 + 4 * c + g] = r;
    __syncthreads();
    if (tid < 16) {
      float sv = 0.f;
#pragma unroll
      for (int ww = 0; ww < NWAVE; ++ww) sv += ldsF[128 + ww * 16 + tid];
      const float o = __uint_as_float((unsigned)xo << 16) + sv * gte;
      const __bf16 ob = (__bf16)o;
      ((GAS unsigned short*)a.out)[ocol] = __builtin_bit_cast(unsigned short, ob);
      if (cid == 0 && tid == 0)
        __hip_atomic_store((GAS unsigned long long*)(a.ws + WS_EPOCH), ((unsigned long long)tagB << 32) | tagA, __ATOMIC_RELAXED, __HIP_MEMORY_SCOPE_AGENT);
    }
  }
  asm volatile(".Lk_all_end:");
}

extern "C" void kernel_launch(void* const* d_in, const int* in_sizes, int n_in, void* d_out, int out_size, void* d_ws, size_t ws_size,
                              hipStream_t stream) {
  Args a;
  a.x = (const unsigned short*)d_in[0];
  a.k = (const unsigned short*)d_in[1];
  a.v = (const unsigned short*)d_in[2];
  a.wq = (const unsigned short*)d_in[3];
  a.wo = (const unsigned short*)d_in[4];
  a.wg = (const unsigned short*)d_in[6];
  a.wu = (const unsigned short*)d_in[7];
  a.wd = (const unsigned short*)d_in[8];
  a.out = (unsigned short*)d_out;
  a.ws = (unsigned char*)d_ws;
  hipLaunchKernelGGL(k_all, dim3(256), dim3(NTHR), 0, stream, a);
}
```

```cpp
#include <hip/hip_runtime.h>
#include <hip/hip_bf16.h>
#include <stdint.h>

typedef unsigned v4u __attribute__((ext_vector_type(4)));
typedef float f32x4 __attribute__((ext_vector_type(4)));
typedef float f32x2 __attribute__((ext_vector_type(2)));
typedef __bf16 bf16x2 __attribute__((ext_vector_type(2)));
typedef __bf16 bf16x8 __attribute__((ext_vector_type(8)));
typedef unsigned v2u __attribute__((ext_vector_type(2)));
#define GAS __attribute__((address_space(1)))
#define CAS __attribute__((address_space(4)))

constexpr int D = 512, H = 32, SK = 8192, DH = 128, F = 1376;
constexpr int NSPLIT = 4, KEYS_PER_WG = SK / NSPLIT;
constexpr int NWAVE = 8, NTHR = NWAVE * 64;
constexpr int NFFN = F / 32;
constexpr int U = 4;
constexpr int GROUPS = KEYS_PER_WG / NWAVE / (4 * U);
#define PWO_ 1
constexpr int won(int p) { return p <= PWO_ ? 0 : p >= 28 ? 32 : (p - PWO_) * 32 / (28 - PWO_); }
#define MSA_ 16
#define YSA_ 16
#define EX1_ 140
#define EX2_ 100
#define STEP(pt) (((unsigned)TPX & ~7u) + (unsigned)(EXT(pt)))
#define EXT(pt) ((pt) < 8 ? EX1_ : (pt) >= 13 && (pt) < 17 ? EX2_ : 0)
#define PDLY_ 8
#define LATE_PRIO_ 400
#define TP_ 1728
#define T0_ 1800

constexpr int WS_EPOCH = 0;
constexpr int WS_ML = 4096;
constexpr int WS_Y = WS_ML + 256 * 16;
constexpr int WS_DP = WS_Y + 256 * 256 * 16;
constexpr int WS_S1 = WS_DP + NFFN * 512 * 16;
constexpr int WS_END = WS_S1 + 32 * 8 * 4 * 64 * 16;
constexpr int OOB = 0x7ffffff0;
constexpr unsigned SPIN_CAP = 400000;

#define SB() __builtin_amdgcn_sched_barrier(0)

__device__ __forceinline__ float dot2(unsigned k, unsigned q, float acc) {
  return __builtin_amdgcn_fdot2_f32_bf16(__builtin_bit_cast(bf16x2, k), __builtin_bit_cast(bf16x2, q), acc, false);
}
template <int CTRL> __device__ __forceinline__ float dpp(float x) {
  return __builtin_bit_cast(float, __builtin_amdgcn_mov_dpp(__builtin_bit_cast(int, x), CTRL, 0xf, 0xf, true));
}
__device__ __forceinline__ float row16_sum(float x) {
  x += dpp<0x128>(x);
  x += dpp<0x124>(x);
  x += dpp<0x122>(x);
  x += dpp<0x121>(x);
  return x;
}
__device__ __forceinline__ float fold32(float a, float b) {
  auto t = __builtin_amdgcn_permlane32_swap(__float_as_uint(a), __float_as_uint(b), false, false);
  return __uint_as_float(t[0]) + __uint_as_float(t[1]);
}
__device__ __forceinline__ float fold16(float a, float b) {
  auto t = __builtin_amdgcn_permlane16_swap(__float_as_uint(a), __float_as_uint(b), false, false);
  return __uint_as_float(t[0]) + __uint_as_float(t[1]);
}
__device__ __forceinline__ float xrow_sum(float x) { x = fold16(x, x); return fold32(x, x); }
__device__ __forceinline__ float xrow_max(float x) {
  auto s = __builtin_amdgcn_permlane16_swap(__float_as_uint(x), __float_as_uint(x), false, false);
  x = fmaxf(__uint_as_float(s[0]), __uint_as_float(s[1]));
  auto t = __builtin_amdgcn_permlane32_swap(__float_as_uint(x), __float_as_uint(x), false, false);
  return fmaxf(__uint_as_float(t[0]), __uint_as_float(t[1]));
}
__device__ __forceinline__ f32x2 fold8(const float (&v)[8]) {
  const float u0 = fold32(v[0], v[4]), u1 = fold32(v[1], v[5]), u2 = fold32(v[2], v[6]), u3 = fold32(v[3], v[7]);
  return f32x2{fold16(u0, u2), fold16(u1, u3)};
}
__device__ __forceinline__ float fold4(float v0, float v1, float v2, float v3) {
  float r = fold16(fold32(v0, v2), fold32(v1, v3));
  r += dpp<0x124>(r); r += dpp<0x128>(r);
  return r;
}
__device__ __forceinline__ float lo(unsigned u) { return __uint_as_float(u << 16); }
__device__ __forceinline__ float hi(unsigned u) { return __uint_as_float(u & 0xffff0000u); }
__device__ __forceinline__ unsigned pk_bf16(float a, float b) {
  bf16x2 r = {(__bf16)a, (__bf16)b};
  return __builtin_bit_cast(unsigned, r);
}
__device__ __forceinline__ unsigned sel4(v4u v, int g) { const unsigned a = (g & 1) ? v[1] : v[0], b = (g & 1) ? v[3] : v[2]; return (g & 2) ? b : a; }
__device__ __forceinline__ int ucol(int u) { return 64 * (u >> 5) + 4 * (u & 15) + 2 * ((u >> 4) & 1); }
__device__ __forceinline__ int s1off(int hg, int r, int k) { return WS_S1 + (hg * 8 + ((r + hg) & 7)) * 4096 + ((k ^ r ^ (hg >> 3)) & 3) * 1024; }
__device__ __forceinline__ float gelu_tanh(float x) {
  const float u = 0.7978845608028654f * (x + 0.044715f * x * x * x);
  const float e = __expf(2.f * u);
  const float t = 1.f - 2.f * __builtin_amdgcn_rcpf(e + 1.f);
  return 0.5f * x * (1.f + t);
}
__device__ __forceinline__ void pace_wait(unsigned& due16, unsigned step16) {
  asm volatile("1:\n\ts_memrealtime s[90:91]\n\ts_waitcnt lgkmcnt(0)\n\ts_lshl_b32 s90, s90, 4\n\ts_sub_u32 s91, s90, %0\n\ts_cmp_ge_i32 s91, 0\n\ts_cbranch_scc1 2f\n\ts_sleep 2\n\ts_branch 1b\n"
               "2:\n\ts_add_u32 %0, %0, %1\n\ts_setprio 0\n\ts_cmp_lt_i32 s91, %2\n\ts_cbranch_scc1 3f\n\ts_setprio 2\n3:"
               : "+s"(due16) : "s"(step16), "i"(LATE_PRIO_) : "s90", "s91", "scc");
}
__device__ __forceinline__ unsigned now16() { return (unsigned)__builtin_amdgcn_s_memrealtime() << 4; }
#define MARK(i)
#define TPX TP_
#define T0X T0_
#define PACE(pt) pace_wait(due, STEP(pt))
constexpr int AUX_SC1 = 16;
template <int AUX = MSA_> __device__ __forceinline__ void st_unit(__amdgpu_buffer_rsrc_t rs, int off, float v0, float v1, unsigned ta, unsigned tb) {
  __builtin_amdgcn_raw_buffer_store_b128(v4u{__float_as_uint(v0), ta, __float_as_uint(v1), tb}, rs, off, 0, AUX);
}
__device__ __forceinline__ v4u ld_unit(__amdgpu_buffer_rsrc_t rs, int off) { return __builtin_amdgcn_raw_buffer_load_b128(rs, off, 0, AUX_SC1); }
__device__ __forceinline__ void await_unit(v4u& u, __amdgpu_buffer_rsrc_t rs, int off, unsigned ta, unsigned tb) {
  if (!__all(((u[1] ^ ta) | (u[3] ^ tb)) == 0u)) {
#pragma unroll 1
    for (unsigned spin = 0; spin < SPIN_CAP; ++spin) {
      __builtin_amdgcn_s_sleep(1);
      asm volatile("" ::: "memory");
      u = ld_unit(rs, off);
      if (__all(((u[1] ^ ta) | (u[3] ^ tb)) == 0u)) break;
    }
  }
}

__device__ __forceinline__ bool last_wave(unsigned* cnt, int lane) {
  asm volatile("s_waitcnt lgkmcnt(0)" ::: "memory");
  unsigned old = 0;
  if (lane == 0) old = __hip_atomic_fetch_add(cnt, 1u, __ATOMIC_RELAXED, __HIP_MEMORY_SCOPE_WORKGROUP);
  old = __builtin_amdgcn_readfirstlane(old);
  asm volatile("" ::: "memory");
  return old == NWAVE - 1;
}

__device__ __forceinline__ void wait_all(unsigned* cnt, unsigned n = NWAVE) {
  while (__builtin_amdgcn_readfirstlane(__hip_atomic_load(cnt, __ATOMIC_RELAXED, __HIP_MEMORY_SCOPE_WORKGROUP)) < n) __builtin_amdgcn_s_sleep(1);
  asm volatile("" ::: "memory");
}
template <int I> struct IC { static constexpr int value = I; };
template <int N, int I = 0, typename Fn> __device__ __forceinline__ void static_for(Fn& fn) { if constexpr (I < N) { fn(IC<I>{}); static_for<N, I + 1>(fn); } }

struct Args {
  const unsigned short* x; const unsigned short* wq;
  unsigned char* ws; const unsigned short* v; const unsigned short* wo; const unsigned short* wg;
  const unsigned short* wu; const unsigned short* wd; unsigned short* out; const unsigned short* k;
};

__global__ void __launch_bounds__(NTHR, 2) k_all(Args a) {
  __shared__ __attribute__((aligned(16))) float lds[8 * 1024 + 16 * 68 + 16 + 8 * 128 + 8 * 128 + 32 + 16];
  float* ldsY = lds;
  unsigned* ldsA = (unsigned*)(lds + 8 * 1024);
  float* ldsM = lds + 8 * 1024 + 16 * 68;
  float* ldsF = ldsM + 16;
  float* ldsQ = ldsF + 8 * 128;
  float* ldsS = ldsQ + 8 * 128;
  unsigned* ldsC = (unsigned*)(ldsS + 32);
  MARK(32);
  const int bid = blockIdx.x;
  const int xcd = bid & 7, j = bid >> 3;
  const int tid = threadIdx.x, lane = tid & 63, w = __builtin_amdgcn_readfirstlane(tid >> 6), lo16 = lane * 16;
  unsigned long long pc0; unsigned csz;
  asm volatile("s_getpc_b64 %0\n.Lk_all_pc0:\n\ts_mov_b32 %1, .Lk_all_end-.Lk_all_pc0" : "=s"(pc0), "=s"(csz));
  const v4u pfc = __builtin_amdgcn_raw_buffer_load_b128(__builtin_amdgcn_make_buffer_rsrc((void*)(pc0 & ~127ull), 0, w == 0 ? (csz & ~15u) : 0u, 0x00020000), lo16, j * 1024, 0);
  const unsigned long long tptr = ((const CAS unsigned long long*)__builtin_amdgcn_kernarg_segment_ptr())[2 + w];
  const CAS v4u* kap = (const CAS v4u*)__builtin_amdgcn_kernarg_segment_ptr();
  v4u ka[5];
#pragma unroll
  for (int i = 0; i < 5; ++i) ka[i] = kap[i];
  SB();
  if (tid < 3) *(v4u*)(ldsC + 4 * tid) = v4u{0u, 0u, 0u, 0u};
  const int h = xcd * 4 + (j >> 3), b = (j >> 2) & 1, s = j & 3;
  const int g = lane >> 4, c = lane & 15;
  const int r8 = j & 7;
  const int fb = xcd * 3 + (j >> 3);
  const bool has_f1 = (j < 24) && (fb < 22);
  const bool is_fin = j >= 24;
  const int cid = xcd * 8 + (j - 24);
  const bool has_f2 = is_fin && cid < NFFN;
  const int row0 = 64 * r8 + 8 * w + 2 * g;
  const int fcol = 128 * (fb < 11 ? fb : fb - 11) + 8 * c;
  int qwo = (2 * g * H * DH + 8 * c) * 2, xso = (lane >> 5) * D * 2 + (lane & 31) * 4;
  int fx1 = fcol < F ? (row0 * F + fcol) * 2 : OOB - 4 * F * 2, fx2 = (32 * cid + 4 * w) * D * 2 + lo16;
  asm volatile("" : "+v"(fx1), "+v"(fx2));
  int fxo = has_f1 ? fx1 : fx2, fxo2 = has_f1 ? OOB - 4096 : fx2;
  unsigned tof = ((unsigned)(b * H + h) * ((0x82u >> w) & 1u) << 21) + ((unsigned)h * ((0x04u >> w) & 1u) << 17);
  int qso = ((64 * r8 + 8 * w) * H * DH + h * DH) * 2, wso = h * DH * D * 2 + (16 * r8 + 2 * w) * 1024, fsz = (has_f1 || has_f2) ? D * F * 2 : 0, fst = has_f1 ? F * 2 : D * 2;
  asm volatile("" : "+v"(qwo), "+v"(xso), "+v"(fxo), "+v"(fxo2), "+s"(tof), "+s"(qso), "+s"(wso), "+s"(fsz), "+s"(fst));
  SB();
  asm volatile("" : "+s"(ka[0]), "+s"(ka[1]), "+s"(ka[2]), "+s"(ka[3]), "+s"(ka[4]));
#define KAP(i) ((unsigned long long)ka[(i) / 2][2 * ((i) & 1)] | ((unsigned long long)ka[(i) / 2][2 * ((i) & 1) + 1] << 32))
#define RSRC(p_, n_) __builtin_amdgcn_make_buffer_rsrc((void*)(p_), 0, (n_), 0x00020000)
  const unsigned long long px = KAP(0), pws = KAP(2);

  const __amdgpu_buffer_rsrc_t rq = RSRC(KAP(1), D * H * DH * 2);
  const v4u sw0 = __builtin_amdgcn_raw_buffer_load_b128(rq, qwo, qso, 2), sw1 = __builtin_amdgcn_raw_buffer_load_b128(rq, qwo, qso + H * DH * 2, 2);
  const unsigned xst = __builtin_amdgcn_raw_buffer_load_b32(RSRC(px, 2 * D * 2), xso, 128 * w, 0);
  const unsigned long long ep = __hip_atomic_load((const GAS unsigned long long*)(tptr + tof), __ATOMIC_RELAXED, __HIP_MEMORY_SCOPE_AGENT);
  SB();
  const __amdgpu_buffer_rsrc_t rf = RSRC(!has_f1 ? KAP(7) : fb < 11 ? KAP(5) : KAP(6), fsz);
  const __amdgpu_buffer_rsrc_t rwa = RSRC(KAP(4), H * DH * D * 2);
  MARK(41);
  asm volatile("s_waitcnt lgkmcnt(0)\n\ts_barrier" ::: "memory");
  v4u ffw[4];
  ffw[0] = __builtin_amdgcn_raw_buffer_load_b128(rf, fxo, 0, 2);
  ffw[1] = __builtin_amdgcn_raw_buffer_load_b128(rf, fxo, fst, 2);
  const v4u pf0 = __builtin_amdgcn_raw_buffer_load_b128(rwa, lo16, wso, 0), pf1 = __builtin_amdgcn_raw_buffer_load_b128(rwa, lo16 + 1024, wso, 0);
  ffw[2] = __builtin_amdgcn_raw_buffer_load_b128(rf, fxo2 + 2048, 0, 2);
  ffw[3] = __builtin_amdgcn_raw_buffer_load_b128(rf, fxo2 + 3072, 0, 2);
  v4u xq0, xq1;
  asm volatile("s_load_dwordx4 %0, %2, 0x0\n\ts_load_dwordx4 %1, %2, 0x400" : "=&s"(xq0), "=&s"(xq1) : "s"(px + (64 * r8 + 8 * w) * 2));
  SB();
  MARK(42);
  const __amdgpu_buffer_rsrc_t rs = RSRC(pws, WS_END), rw = RSRC(KAP(4) + (size_t)h * DH * D * 2, DH * D * 2);

  const __amdgpu_buffer_rsrc_t rk = RSRC(KAP(9) + (size_t)(b * H + h) * SK * DH * 2, SK * DH * 2);
  const __amdgpu_buffer_rsrc_t rv = RSRC(KAP(3) + (size_t)(b * H + h) * SK * DH * 2, SK * DH * 2);
#define GOFF(gi_) ((((gi_) & 15) << 17) | (((gi_) & 4) << 12) | (((gi_) & 8) << 9))
  v4u rg[2 * GROUPS][U];

  constexpr float C2 = 0.08838834764831845f * 1.4426950408889634f;
  float sp[4] = {0.f, 0.f, 0.f, 0.f};
  float mw = 0.f;
  f32x2 bs[8];
  unsigned tagA, tagB, qp[4], slot0;
  v2u wor[32]; v4u wb[4][4], fu, qu, mu, e0, e1; unsigned short xo; float gte = 0.f;
  asm volatile("s_waitcnt lgkmcnt(0)" : "+s"(xq0), "+s"(xq1));
  const unsigned xs0 = sel4(xq0, g), xs1 = sel4(xq1, g);
  int fuoff;
  const int wvo = g * 8192 + w * 128 + c * 8;
  const int qoff = s1off(h, w, b) + lo16;
  const int pid = (b * H + h) * NSPLIT + s, muoff = WS_ML + ((pid & ~3) + (lane & 3)) * 16;
  const int bc = (cid >> 5) & 1, cu0 = (cid & 31) * 8;
  const int gp = tid >> 2, qd = tid & 3, ocol = bc * D + ucol(cu0 + (c >> 1)) + (c & 1);
  const int dpoff = WS_DP + ((gp < NFFN ? gp : 0) * 512 + bc * 256 + cu0 + 2 * qd) * 16;

  auto qk = [&](auto gic) __attribute__((always_inline)) {
    constexpr int gi = decltype(gic)::value;
    float d[U];
#pragma unroll
    for (int u = 0; u < U; ++u) d[u] = dot2(rg[gi][u][3], qp[3], dot2(rg[gi][u][2], qp[2], dot2(rg[gi][u][1], qp[1], dot2(rg[gi][u][0], qp[0], 0.f))));
    const bool o1 = c & 1, o2 = c & 2;
    const float x0 = (o1 ? d[1] : d[0]) + dpp<0xB1>(o1 ? d[0] : d[1]), x1 = (o1 ? d[3] : d[2]) + dpp<0xB1>(o1 ? d[2] : d[3]);
    float x = (o2 ? x1 : x0) + dpp<0x4E>(o2 ? x0 : x1);
    x += dpp<0x128>(x); x += dpp<0x124>(x);
    sp[gi / 4] = __builtin_bit_cast(float, __builtin_amdgcn_update_dpp(__builtin_bit_cast(int, sp[gi / 4]), __builtin_bit_cast(int, x), 0xE4, 0xf, 1 << (gi % 4), false));
    asm volatile("" : "+v"(sp[gi / 4]));
    SB();
  };
  auto pv = [&](auto gic) __attribute__((always_inline)) {
    constexpr int gi = decltype(gic)::value;
    const float p[4] = {dpp<0x150 + 4 * (gi % 4)>(sp[gi / 4]), dpp<0x151 + 4 * (gi % 4)>(sp[gi / 4]), dpp<0x152 + 4 * (gi % 4)>(sp[gi / 4]), dpp<0x153 + 4 * (gi % 4)>(sp[gi / 4])};
    float x[8];
#pragma unroll
    for (int k = 0; k < 4; ++k) { x[2 * k] = p[0] * lo(rg[GROUPS + gi][0][k]); x[2 * k + 1] = p[0] * hi(rg[GROUPS + gi][0][k]); }
#pragma unroll
    for (int u = 1; u < U; ++u) {
#pragma unroll
      for (int k = 0; k < 4; ++k) { x[2 * k] = fmaf(p[u], lo(rg[GROUPS + gi][u][k]), x[2 * k]); x[2 * k + 1] = fmaf(p[u], hi(rg[GROUPS + gi][u][k]), x[2 * k + 1]); }
    }
    const f32x2 y = fold8(x);
    if constexpr (gi < 8) bs[gi] = y; else bs[gi - 8] += y;
    if constexpr (gi >= 12) bs[gi - 12] += bs[gi - 8];
    if constexpr (gi >= 14) bs[gi - 14] += bs[gi - 12];
    asm volatile("" : "+v"(bs[gi >= 14 ? gi - 14 : gi >= 12 ? gi - 12 : gi % 8]));
    SB();
  };

  {
    asm volatile("" :: "v"(xst), "v"(sw1));
    unsigned tk = now16() | 1u, zero = 0u;
    MARK(40);
    if (lane == 0) { __hip_atomic_compare_exchange_strong(ldsC + 10, &zero, tk, __ATOMIC_RELAXED, __ATOMIC_RELAXED, __HIP_MEMORY_SCOPE_WORKGROUP); if (zero) tk = zero; }
    slot0 = __builtin_amdgcn_readfirstlane((__builtin_amdgcn_readfirstlane(tk) + (unsigned)T0X) / (unsigned)(TPX / 8) + (unsigned)w);
    float sm = row16_sum(lo(xst) + hi(xst)); sm = fold16(sm, sm) * (1.f / 64);
    const float d0 = lo(xst) - sm, d1 = hi(xst) - sm;
    float m2 = row16_sum(fmaf(d0, d0, d1 * d1)); m2 = fold16(m2, m2);
    if ((lane & 31) == 0) *(f32x2*)(ldsS + (lane >> 5) * 16 + 2 * w) = f32x2{sm, m2};
    float t[24];
#pragma unroll
    for (int k = 0; k < 4; ++k) {
      const float al = lo(sw0[k]), ah = hi(sw0[k]), bl = lo(sw1[k]), bh = hi(sw1[k]);
      t[2 * k] = fmaf(hi(xs0), bl, lo(xs0) * al); t[2 * k + 1] = fmaf(hi(xs0), bh, lo(xs0) * ah);
      t[8 + 2 * k] = fmaf(hi(xs1), bl, lo(xs1) * al); t[8 + 2 * k + 1] = fmaf(hi(xs1), bh, lo(xs1) * ah);
      t[16 + 2 * k] = al + bl; t[16 + 2 * k + 1] = ah + bh;
    }
#pragma unroll
    for (int k = 0; k < 8; ++k) t[k] = fold16(fold32(t[k], t[16 + k]), fold32(t[8 + k], t[16 + k]));
    *(f32x4*)(ldsY + w * 512 + g * 128 + 8 * c) = f32x4{t[0], t[1], t[2], t[3]};
    *(f32x4*)(ldsY + w * 512 + g * 128 + 8 * c + 4) = f32x4{t[4], t[5], t[6], t[7]};
    if (tid == 0) *(unsigned long long*)(ldsC + 8) = ep;
    asm volatile("" :: "v"(ep));
    if (last_wave(ldsC, lane)) {
      f32x4 st[8];
#pragma unroll
      for (int i = 0; i < 8; ++i) st[i] = *(const f32x4*)(ldsS + 4 * i);
      tagA = __builtin_amdgcn_readfirstlane(ldsC[8]) + 1u; tagB = __builtin_amdgcn_readfirstlane(ldsC[9]) + 0x9E3779B9u;
      const float mean0 = ((st[0][0] + st[0][2]) + (st[1][0] + st[1][2]) + (st[2][0] + st[2][2]) + (st[3][0] + st[3][2])) * 0.125f;
      const float mean1 = ((st[4][0] + st[4][2]) + (st[5][0] + st[5][2]) + (st[6][0] + st[6][2]) + (st[7][0] + st[7][2])) * 0.125f;
      float q0 = 0.f, q1 = 0.f, d0 = 0.f, d1 = 0.f;
#pragma unroll
      for (int i = 0; i < 4; ++i) {
        q0 += st[i][1] + st[i][3]; q1 += st[4 + i][1] + st[4 + i][3];
        const float a0 = st[i][0] - mean0, b0 = st[i][2] - mean0, a1 = st[4 + i][0] - mean1, b1 = st[4 + i][2] - mean1;
        d0 += fmaf(a0, a0, b0 * b0); d1 += fmaf(a1, a1, b1 * b1);
      }
      const float rstd0 = __builtin_amdgcn_rsqf(fmaf(64.f, d0, q0) * (1.f / D) + 1e-5f), rstd1 = __builtin_amdgcn_rsqf(fmaf(64.f, d1, q1) * (1.f / D) + 1e-5f);
      if (lane == 0) *(f32x4*)(ldsC + 12) = f32x4{mean0, rstd0, mean1, rstd1};
      last_wave(ldsC, lane);
      f32x2 z0 = {0.f, 0.f}, z1 = {0.f, 0.f}, cs = {0.f, 0.f};
#pragma unroll
      for (int ww = 0; ww < NWAVE; ++ww) {
        z0 += *(const f32x2*)(ldsY + ww * 512 + 2 * lane); z1 += *(const f32x2*)(ldsY + ww * 512 + 128 + 2 * lane); cs += *(const f32x2*)(ldsY + ww * 512 + 256 + 2 * lane); }
      st_unit(rs, s1off(h, r8, 0) + lo16, (z0[0] - mean0 * cs[0]) * rstd0, (z0[1] - mean0 * cs[1]) * rstd0, tagA, tagB);
      st_unit(rs, s1off(h, r8, 1) + lo16, (z1[0] - mean1 * cs[0]) * rstd1, (z1[1] - mean1 * cs[1]) * rstd1, tagA, tagB);
    }
  }
  MARK(33);
  const int kvo = ((slot0 & 1) << 13) | ((slot0 & 6) << 14) | ((s & 1) << 12) | ((s >> 1) << 14);
  unsigned due = __builtin_amdgcn_readfirstlane(slot0 * (unsigned)(TPX / 8));

  auto point = [&](auto pc) __attribute__((always_inline)) {
    constexpr int p = decltype(pc)::value;
    const int so = kvo ^ GOFF((slot0 >> 3) + p);
    PACE(p);
    SB();
#pragma unroll
    for (int u = 0; u < U; ++u) rg[p][u] = __builtin_amdgcn_raw_buffer_load_b128(p < GROUPS ? rk : rv, lo16 + u * 1024, so, 2);
    SB();
    if constexpr (p == 3) qu = ld_unit(rs, qoff);
    if constexpr (p == 6) fu = ld_unit(rs, fuoff);
    if constexpr (p == 29) mu = ld_unit(rs, muoff);
#pragma unroll
    for (int i = won(p); i < won(p + 1); ++i) wor[i] = __builtin_amdgcn_raw_buffer_load_b64(rw, wvo, (32 * (i >> 3) + (i & 7)) * 1024, 0);
    if constexpr (p == 16) {
      e0 = ld_unit(rs, is_fin ? dpoff : OOB); e1 = ld_unit(rs, is_fin ? dpoff + 16 : OOB);
      xo = ((const GAS unsigned short*)px)[ocol];
    }
    SB();
    if constexpr (p == 1) {
      wait_all(ldsC, NWAVE + 1);
      tagA = __builtin_amdgcn_readfirstlane(ldsC[8]) + 1u; tagB = __builtin_amdgcn_readfirstlane(ldsC[9]) + 0x9E3779B9u;
      asm volatile("" :: "v"(pf0), "v"(pf1), "v"(pfc));
      if (has_f1) {
        const f32x4 ln = *(const f32x4*)(ldsC + 12);
        const float n00 = (lo(xs0) - ln[0]) * ln[1], n01 = (hi(xs0) - ln[0]) * ln[1], n10 = (lo(xs1) - ln[2]) * ln[3], n11 = (hi(xs1) - ln[2]) * ln[3];
        float t[8];
#pragma unroll
        for (int k = 0; k < 4; ++k) {
          const float al = lo(ffw[0][k]), ah = hi(ffw[0][k]), bl = lo(ffw[1][k]), bh = hi(ffw[1][k]);
          t[2 * k] = fold32(fmaf(n01, bl, n00 * al), fmaf(n11, bl, n10 * al)); t[2 * k + 1] = fold32(fmaf(n01, bh, n00 * ah), fmaf(n11, bh, n10 * ah));
        }
        *(f32x4*)(ldsY + 4096 + w * 256 + (g >> 1) * 128 + 8 * c + 4 * (g & 1)) = f32x4{fold16(t[0], t[4]), fold16(t[1], t[5]), fold16(t[2], t[6]), fold16(t[3], t[7])};
        last_wave(ldsC + 2, lane);
      }
    }
    if constexpr (p == 4) {
      int gq = g; asm volatile("" : "+v"(gq));
      const int fbs = (gq >> 1) * 11 + (cid >> 2), hs = (fbs / 3) * 4 + fbs % 3;
      fuoff = has_f2 ? s1off(hs, w, 2 + (g & 1)) + (16 * (cid & 3) + c) * 16 : OOB;
      asm volatile("" : "+v"(fuoff));
    }
    if constexpr (p == 3) {
      if (has_f1 && w < 2) {
        wait_all(ldsC + 2);
        f32x2 z = {0.f, 0.f};
#pragma unroll
        for (int ww = 0; ww < NWAVE; ++ww) z += *(const f32x2*)(ldsY + 4096 + ww * 256 + 2 * tid);
        st_unit(rs, s1off(h, r8, 2 + w) + lo16, z[0], z[1], tagA, tagB);
      }
    }
    if constexpr (p == 6) {
      await_unit(qu, rs, qoff, tagA, tagB);
      *(f32x2*)(ldsQ + w * 128 + 2 * lane) = f32x2{__uint_as_float(qu[0]), __uint_as_float(qu[2])};
      last_wave(ldsC + 1, lane);
    }
    if constexpr (p == 8) {
      wait_all(ldsC + 1);
      f32x4 q0 = {0, 0, 0, 0}, q1 = {0, 0, 0, 0};
#pragma unroll
      for (int ww = 0; ww < NWAVE; ++ww) { q0 += *(const f32x4*)(ldsQ + ww * 128 + 8 * c); q1 += *(const f32x4*)(ldsQ + ww * 128 + 8 * c + 4); }
      qp[0] = pk_bf16(q0[0], q0[1]); qp[1] = pk_bf16(q0[2], q0[3]); qp[2] = pk_bf16(q1[0], q1[1]); qp[3] = pk_bf16(q1[2], q1[3]);
      qk(IC<0>{});
    }
    if constexpr (p == 9) { qk(IC<1>{}); qk(IC<2>{}); qk(IC<3>{}); }
    if constexpr (p == 10) { qk(IC<4>{}); qk(IC<5>{}); }
    if constexpr (p >= 10 && p < GROUPS + 4) qk(IC<p - 4>{});
    if constexpr (p == 9) {
      if (has_f2) {
        await_unit(fu, rs, fuoff, tagA, tagB);
        *(f32x2*)(ldsF + w * 128 + g * 32 + 2 * c) = f32x2{__uint_as_float(fu[0]), __uint_as_float(fu[2])};
        last_wave(ldsC + 5, lane);
      }
    }
    if constexpr (p == 11) {
      if (has_f2) {
        wait_all(ldsC + 5);
        float gs = 0.f, us = 0.f;
        const int hx = ((lane >> 2) & 1) * 32 + 4 * w + (lane & 3);
#pragma unroll
        for (int ww = 0; ww < NWAVE; ++ww) { gs += ldsF[ww * 128 + hx]; us += ldsF[ww * 128 + 64 + hx]; }
        const float hv = gelu_tanh(gs) * us;
        float dd[16];
#pragma unroll
        for (int k = 0; k < 16; ++k) dd[k] = 0.f;
#pragma unroll
        for (int r = 0; r < 4; ++r) {
          const float h0 = __builtin_bit_cast(float, __builtin_amdgcn_readlane(__builtin_bit_cast(int, hv), r));
          const float h1 = __builtin_bit_cast(float, __builtin_amdgcn_readlane(__builtin_bit_cast(int, hv), 4 + r));
#pragma unroll
          for (int k = 0; k < 4; ++k) {
            const float wl = lo(ffw[r][k]), wh = hi(ffw[r][k]);
            dd[2 * k] = fmaf(h0, wl, dd[2 * k]); dd[2 * k + 1] = fmaf(h0, wh, dd[2 * k + 1]);
            dd[8 + 2 * k] = fmaf(h1, wl, dd[8 + 2 * k]); dd[8 + 2 * k + 1] = fmaf(h1, wh, dd[8 + 2 * k + 1]);
          }
        }
        *(f32x4*)(ldsY + w * 1024 + 8 * lane) = f32x4{dd[0], dd[1], dd[2], dd[3]};
        *(f32x4*)(ldsY + w * 1024 + 8 * lane + 4) = f32x4{dd[4], dd[5], dd[6], dd[7]};
        *(f32x4*)(ldsY + w * 1024 + 512 + 8 * lane) = f32x4{dd[8], dd[9], dd[10], dd[11]};
        *(f32x4*)(ldsY + w * 1024 + 512 + 8 * lane + 4) = f32x4{dd[12], dd[13], dd[14], dd[15]};
        last_wave(ldsC + 3, lane);
      }
    }
    if constexpr (p == 13) {
      if (has_f2) {
        wait_all(ldsC + 3);
        f32x2 dv = {0.f, 0.f};
#pragma unroll
        for (int ww = 0; ww < NWAVE; ++ww) dv += *(const f32x2*)(ldsY + ww * 1024 + (tid & 256) * 2 + ucol(tid & 255));
        st_unit(rs, WS_DP + (cid * 512 + tid) * 16, dv[0], dv[1], tagA, tagB);
      }
    }
    if constexpr (p == 23) {
      if (is_fin) {
        await_unit(e0, rs, dpoff, tagA, tagB);
        await_unit(e1, rs, dpoff + 16, tagA, tagB);
        const bool hd = gp < NFFN;
        const float r = fold4(hd ? __uint_as_float(e0[0]) : 0.f, hd ? __uint_as_float(e0[2]) : 0.f, hd ? __uint_as_float(e1[0]) : 0.f, hd ? __uint_as_float(e1[2]) : 0.f);
        if (c < 4) ldsF[w * 16 + 4 * c + g] = r;
        last_wave(ldsC + 2, lane);
      }
    }
    if constexpr (p == GROUPS + 3) {
      float x = fmaxf(fmaxf(sp[0], sp[1]), fmaxf(sp[2], sp[3]));
      x = fmaxf(x, dpp<0x128>(x)); x = fmaxf(x, dpp<0x124>(x)); x = fmaxf(x, dpp<0x122>(x)); x = fmaxf(x, dpp<0x121>(x));
      mw = xrow_max(x) * C2;
#pragma unroll
      for (int i = 0; i < 4; ++i) sp[i] = __builtin_amdgcn_exp2f(fmaf(sp[i], C2, -mw));
      const float lw = xrow_sum(row16_sum((sp[0] + sp[2]) + (sp[1] + sp[3])));
      if (lane == 0) { ldsM[slot0 & 7] = mw; ldsM[8 + (slot0 & 7)] = lw; }
      last_wave(ldsC + 4, lane);
    }
    if constexpr (p == 21) {
      if (w == 0) {
        wait_all(ldsC + 4);
        const float mi = ldsM[lane & 7], li = ldsM[8 + (lane & 7)];
        float M = fmaxf(mi, dpp<0x124>(mi)); M = fmaxf(M, dpp<0x122>(M)); M = fmaxf(M, dpp<0x121>(M));
        float L = __builtin_amdgcn_exp2f(mi - M) * li; L += dpp<0x124>(L); L += dpp<0x122>(L); L += dpp<0x121>(L);
        if (lane == 0) st_unit(rs, WS_ML + pid * 16, M, L, tagA, tagB);
      }
    }
    if constexpr (p == 26) {
      if (is_fin && w == 0) {
        wait_all(ldsC + 2);
        const float dv = ldsF[c] + ldsF[16 + c] + ldsF[32 + c];
        gte = dv * __builtin_amdgcn_rcpf(1.f + __expf(-dv));
        asm volatile("" : "+v"(gte));
      }
    }
    if constexpr (p == 31) {
      await_unit(mu, rs, muoff, tagA, tagB);
      const float mi = __uint_as_float(mu[0]);
      float M = fmaxf(mi, dpp<0xB1>(mi)); M = fmaxf(M, dpp<0x4E>(M));
      float L = __builtin_amdgcn_exp2f(mi - M) * __uint_as_float(mu[2]); L += dpp<0xB1>(L); L += dpp<0x4E>(L);
      mw = __builtin_amdgcn_exp2f(mw - M) * __builtin_amdgcn_rcpf(L);
      asm volatile("" : "+v"(mw));
    }
    if constexpr (p >= 20 && p < 28) pv(IC<p - 20>{});
    if constexpr (p == 28 || p == 29) { pv(IC<2 * p - 48>{}); pv(IC<2 * p - 47>{}); }
    if constexpr (p == 30) pv(IC<12>{});
    if constexpr (p == 31) { pv(IC<13>{}); pv(IC<14>{}); pv(IC<15>{}); }
    if constexpr (p == 30) {
#pragma unroll
      for (int i = 0; i < 16; ++i) {
        const v2u r0 = wor[2 * i], r1 = wor[2 * i + 1];
        wb[0][i >> 2][i & 3] = __builtin_amdgcn_perm(r1[0], r0[0], 0x05040100u); wb[1][i >> 2][i & 3] = __builtin_amdgcn_perm(r1[0], r0[0], 0x07060302u);
        wb[2][i >> 2][i & 3] = __builtin_amdgcn_perm(r1[1], r0[1], 0x05040100u); wb[3][i >> 2][i & 3] = __builtin_amdgcn_perm(r1[1], r0[1], 0x07060302u);
      }
#pragma unroll
      for (int i = 0; i < 4; ++i) asm volatile("" : "+v"(wb[i][0]), "+v"(wb[i][1]), "+v"(wb[i][2]), "+v"(wb[i][3]));
    }
    SB();
  };
  static_for<2 * GROUPS>(point);
  MARK(34);

  {
    const f32x2 o2 = (bs[0] + bs[1]) * mw;
    const unsigned oh = pk_bf16(o2[0], o2[1]), ol = pk_bf16(o2[0] - lo(oh), o2[1] - hi(oh));
    const int ar = (slot0 & 7) * 68 + 4 * c + g;
    ldsA[ar] = oh; ldsA[8 * 68 + ar] = ol;
  }
  __syncthreads();
  MARK(35);
  {
    v4u af[4];
#pragma unroll
    for (int ks = 0; ks < 4; ++ks) af[ks] = *(const v4u*)(ldsA + c * 68 + 16 * ks + 4 * g);
    f32x4 d[4];
#pragma unroll
    for (int t = 0; t < 4; ++t) d[t] = f32x4{0.f, 0.f, 0.f, 0.f};
#pragma unroll
    for (int ks = 0; ks < 4; ++ks)
#pragma unroll
      for (int t = 0; t < 4; ++t) d[t] = __builtin_amdgcn_mfma_f32_16x16x32_bf16(__builtin_bit_cast(bf16x8, af[ks]), __builtin_bit_cast(bf16x8, wb[t][ks]), d[t], 0, 0, 0);
    float y[4];
#pragma unroll
    for (int t = 0; t < 4; ++t) y[t] = (d[t][0] + d[t][1]) + (d[t][2] + d[t][3]);
    float u0 = fold32(y[0], y[2]), u1 = fold32(y[1], y[3]);
    u0 = fold16(u0, u0); u1 = fold16(u1, u1);
    if ((g & 1) == 0) st_unit<YSA_>(rs, WS_Y + (pid * 256 + 32 * w + 16 * (g >> 1) + c) * 16, u0, u1, tagA, tagB);
  }
  MARK(36);

  if (is_fin) {
    const int yoff = WS_Y + ((bc * 128 + gp) * 256 + cu0 + 2 * qd) * 16;
    __builtin_amdgcn_s_barrier();
    if (PDLY_) __builtin_amdgcn_s_sleep(PDLY_);
    MARK(38);
    v4u y0, y1;
    bool ok = false;
#pragma unroll 1
    for (unsigned spin = 0; spin < SPIN_CAP; ++spin) {
      asm volatile("" ::: "memory");
      if (!ok) {
        y0 = ld_unit(rs, yoff); y1 = ld_unit(rs, yoff + 16);
        ok = (((y0[1] ^ tagA) | (y1[1] ^ tagA)) | ((y0[3] ^ tagB) | (y1[3] ^ tagB))) == 0u;
      }
      if (__all(ok)) break;
      __builtin_amdgcn_s_sleep(1);
    }
    MARK(39);
    const float r = fold4(__uint_as_float(y0[0]), __uint_as_float(y0[2]), __uint_as_float(y1[0]), __uint_as_float(y1[2]));
    if (c < 4) ldsF[128 + w * 16 + 4 * c + g] = r;
    __syncthreads();
    if (tid < 16) {
      float sv = 0.f;
#pragma unroll
      for (int ww = 0; ww < NWAVE; ++ww) sv += ldsF[128 + ww * 16 + tid];
      const float o = __uint_as_float((unsigned)xo << 16) + sv * gte;
      const __bf16 ob = (__bf16)o;
      ((GAS unsigned short*)KAP(8))[ocol] = __builtin_bit_cast(unsigned short, ob);
      if (cid == 0 && tid == 0)
        __hip_atomic_store((GAS unsigned long long*)(pws + WS_EPOCH), ((unsigned long long)tagB << 32) | tagA, __ATOMIC_RELAXED, __HIP_MEMORY_SCOPE_AGENT);
    }
  }
  asm volatile(".Lk_all_end:");
}

extern "C" void kernel_launch(void* const* d_in, const int* in_sizes, int n_in, void* d_out, int out_size, void* d_ws, size_t ws_size,
                              hipStream_t stream) {
  Args a;
  a.x = (const unsigned short*)d_in[0];
  a.k = (const unsigned short*)d_in[1];
  a.v = (const unsigned short*)d_in[2];
  a.wq = (const unsigned short*)d_in[3];
  a.wo = (const unsigned short*)d_in[4];
  a.wg = (const unsigned short*)d_in[6];
  a.wu = (const unsigned short*)d_in[7];
  a.wd = (const unsigned short*)d_in[8];
  a.out = (unsigned short*)d_out;
  a.ws = (unsigned char*)d_ws;
  hipLaunchKernelGGL(k_all, dim3(256), dim3(NTHR), 0, stream, a);
}
```

```cpp
#include <hip/hip_runtime.h>
#include <hip/hip_bf16.h>
#include <stdint.h>

typedef unsigned v4u __attribute__((ext_vector_type(4)));
typedef float f32x4 __attribute__((ext_vector_type(4)));
typedef float f32x2 __attribute__((ext_vector_type(2)));
typedef __bf16 bf16x2 __attribute__((ext_vector_type(2)));
typedef __bf16 bf16x8 __attribute__((ext_vector_type(8)));
typedef unsigned v16u __attribute__((ext_vector_type(16)));
typedef unsigned v2u __attribute__((ext_vector_type(2)));
#define GAS __attribute__((address_space(1)))
#define CAS __attribute__((address_space(4)))

constexpr int D = 512, H = 32, SK = 8192, DH = 128, F = 1376;
constexpr int NSPLIT = 4, KEYS_PER_WG = SK / NSPLIT;
constexpr int NWAVE = 8, NTHR = NWAVE * 64;
constexpr int NFFN = F / 32;
constexpr int U = 4;
constexpr int GROUPS = KEYS_PER_WG / NWAVE / (4 * U);
#define PWO_ 1
constexpr int won(int p) { return p <= PWO_ ? 0 : p >= 28 ? 32 : (p - PWO_) * 32 / (28 - PWO_); }
#define MSA_ 16
#define YSA_ 16
#define EX1_ 125
#define EX2_ 100
#define STEP(pt) (((unsigned)TPX & ~7u) + (unsigned)(EXT(pt)))
#define EXT(pt) ((pt) < 9 ? EX1_ : (pt) >= 14 && (pt) < 18 ? EX2_ : 0)
#define PI0_ 0
#define RSL_ 1
#define PDLY_ 13
#define LATE_PRIO_ 400
#define TP_ 1728
#define T0_ 2400

constexpr int WS_EPOCH = 0;
constexpr int WS_ML = 4096;
constexpr int WS_Y = WS_ML + 256 * 16;
constexpr int WS_DP = WS_Y + 256 * 256 * 16;
constexpr int WS_S1 = WS_DP + NFFN * 512 * 16;
constexpr int WS_END = WS_S1 + 32 * 8 * 4 * 64 * 16;
constexpr int OOB = 0x7ffffff0;
constexpr unsigned SPIN_CAP = 400000;

#define SB() __builtin_amdgcn_sched_barrier(0)

__device__ __forceinline__ float dot2(unsigned k, unsigned q, float acc) {
  return __builtin_amdgcn_fdot2_f32_bf16(__builtin_bit_cast(bf16x2, k), __builtin_bit_cast(bf16x2, q), acc, false);
}
template <int CTRL> __device__ __forceinline__ float dpp(float x) {
  return __builtin_bit_cast(float, __builtin_amdgcn_mov_dpp(__builtin_bit_cast(int, x), CTRL, 0xf, 0xf, true));
}
__device__ __forceinline__ float row16_sum(float x) {
  x += dpp<0x128>(x);
  x += dpp<0x124>(x);
  x += dpp<0x122>(x);
  x += dpp<0x121>(x);
  return x;
}
__device__ __forceinline__ float fold32(float a, float b) {
  auto t = __builtin_amdgcn_permlane32_swap(__float_as_uint(a), __float_as_uint(b), false, false);
  return __uint_as_float(t[0]) + __uint_as_float(t[1]);
}
__device__ __forceinline__ float fold16(float a, float b) {
  auto t = __builtin_amdgcn_permlane16_swap(__float_as_uint(a), __float_as_uint(b), false, false);
  return __uint_as_float(t[0]) + __uint_as_float(t[1]);
}
__device__ __forceinline__ float xrow_sum(float x) { x = fold16(x, x); return fold32(x, x); }
__device__ __forceinline__ float xrow_max(float x) {
  auto s = __builtin_amdgcn_permlane16_swap(__float_as_uint(x), __float_as_uint(x), false, false);
  x = fmaxf(__uint_as_float(s[0]), __uint_as_float(s[1]));
  auto t = __builtin_amdgcn_permlane32_swap(__float_as_uint(x), __float_as_uint(x), false, false);
  return fmaxf(__uint_as_float(t[0]), __uint_as_float(t[1]));
}
__device__ __forceinline__ f32x2 fold8(const float (&v)[8]) {
  const float u0 = fold32(v[0], v[4]), u1 = fold32(v[1], v[5]), u2 = fold32(v[2], v[6]), u3 = fold32(v[3], v[7]);
  return f32x2{fold16(u0, u2), fold16(u1, u3)};
}
__device__ __forceinline__ float fold4(float v0, float v1, float v2, float v3) {
  float r = fold16(fold32(v0, v2), fold32(v1, v3));
  r += dpp<0x124>(r); r += dpp<0x128>(r);
  return r;
}
__device__ __forceinline__ float lo(unsigned u) { return __uint_as_float(u << 16); }
__device__ __forceinline__ float hi(unsigned u) { return __uint_as_float(u & 0xffff0000u); }
__device__ __forceinline__ unsigned pk_bf16(float a, float b) {
  bf16x2 r = {(__bf16)a, (__bf16)b};
  return __builtin_bit_cast(unsigned, r);
}
__device__ __forceinline__ unsigned sel4(v4u v, int g) { const unsigned a = (g & 1) ? v[1] : v[0], b = (g & 1) ? v[3] : v[2]; return (g & 2) ? b : a; }
__device__ __forceinline__ int ucol(int u) { return 64 * (u >> 5) + 4 * (u & 15) + 2 * ((u >> 4) & 1); }
__device__ __forceinline__ int s1off(int hg, int r, int k) { return WS_S1 + (hg * 8 + ((r + hg) & 7)) * 4096 + ((k ^ r ^ (hg >> 3)) & 3) * 1024; }
__device__ __forceinline__ float gelu_tanh(float x) {
  const float u = 0.7978845608028654f * (x + 0.044715f * x * x * x);
  const float e = __expf(2.f * u);
  const float t = 1.f - 2.f * __builtin_amdgcn_rcpf(e + 1.f);
  return 0.5f * x * (1.f + t);
}
__device__ __forceinline__ void pace_wait(unsigned& due16, unsigned step16) {
  asm volatile("1:\n\ts_memrealtime s[90:91]\n\ts_waitcnt lgkmcnt(0)\n\ts_lshl_b32 s90, s90, 4\n\ts_sub_u32 s91, s90, %0\n\ts_cmp_ge_i32 s91, 0\n\ts_cbranch_scc1 2f\n\ts_sleep 2\n\ts_branch 1b\n"
               "2:\n\ts_add_u32 %0, %0, %1\n\ts_setprio 0\n\ts_cmp_lt_i32 s91, %2\n\ts_cbranch_scc1 3f\n\ts_setprio 2\n3:"
               : "+s"(due16) : "s"(step16), "i"(LATE_PRIO_) : "s90", "s91", "scc");
}
__device__ __forceinline__ unsigned now16() { return (unsigned)__builtin_amdgcn_s_memrealtime() << 4; }
#define MARK(i)
#define TPX TP_
#define T0X T0_
#define PACE(pt) pace_wait(due, STEP(pt))
constexpr int AUX_SC1 = 16;
template <int AUX = MSA_> __device__ __forceinline__ void st_unit(__amdgpu_buffer_rsrc_t rs, int off, float v0, float v1, unsigned ta, unsigned tb) {
  __builtin_amdgcn_raw_buffer_store_b128(v4u{__float_as_uint(v0), ta, __float_as_uint(v1), tb}, rs, off, 0, AUX);
}
__device__ __forceinline__ v4u ld_unit(__amdgpu_buffer_rsrc_t rs, int off) { return __builtin_amdgcn_raw_buffer_load_b128(rs, off, 0, AUX_SC1); }
__device__ __forceinline__ void await_unit(v4u& u, __amdgpu_buffer_rsrc_t rs, int off, unsigned ta, unsigned tb) {
  if (!__all(((u[1] ^ ta) | (u[3] ^ tb)) == 0u)) {
#pragma unroll 1
    for (unsigned spin = 0; spin < SPIN_CAP; ++spin) {
      __builtin_amdgcn_s_sleep(1);
      asm volatile("" ::: "memory");
      u = ld_unit(rs, off);
      if (__all(((u[1] ^ ta) | (u[3] ^ tb)) == 0u)) break;
    }
  }
}

__device__ __forceinline__ bool last_wave(unsigned* cnt, int lane) {
  asm volatile("s_waitcnt lgkmcnt(0)" ::: "memory");
  unsigned old = 0;
  if (lane == 0) old = __hip_atomic_fetch_add(cnt, 1u, __ATOMIC_RELAXED, __HIP_MEMORY_SCOPE_WORKGROUP);
  old = __builtin_amdgcn_readfirstlane(old);
  asm volatile("" ::: "memory");
  return old == NWAVE - 1;
}

__device__ __forceinline__ void wait_all(unsigned* cnt, unsigned n = NWAVE) {
  while (__builtin_amdgcn_readfirstlane(__hip_atomic_load(cnt, __ATOMIC_RELAXED, __HIP_MEMORY_SCOPE_WORKGROUP)) < n) __builtin_amdgcn_s_sleep(1);
  asm volatile("" ::: "memory");
}
template <int I> struct IC { static constexpr int value = I; };
template <int N, int I = 0, typename Fn> __device__ __forceinline__ void static_for(Fn& fn) { if constexpr (I < N) { fn(IC<I>{}); static_for<N, I + 1>(fn); } }

struct Args {
  const unsigned short* x; const unsigned short* wq;
  const unsigned short* k; const unsigned short* v; const unsigned short* wo; const unsigned short* wg;
  const unsigned short* wu; const unsigned short* wd; unsigned short* out; unsigned char* ws;
};

__global__ void __launch_bounds__(NTHR, 2) k_all(Args a) {
  __shared__ __attribute__((aligned(16))) float lds[8 * 1024 + 16 * 68 + 16 + 8 * 128 + 8 * 128 + 32 + 16];
  float* ldsY = lds;
  unsigned* ldsA = (unsigned*)(lds + 8 * 1024);
  float* ldsM = lds + 8 * 1024 + 16 * 68;
  float* ldsF = ldsM + 16;
  float* ldsQ = ldsF + 8 * 128;
  float* ldsS = ldsQ + 8 * 128;
  unsigned* ldsC = (unsigned*)(ldsS + 32);
  MARK(32);
  const int bid = blockIdx.x;
  const int xcd = bid & 7, j = bid >> 3;
  const int tid = threadIdx.x, lane = tid & 63, w = __builtin_amdgcn_readfirstlane(tid >> 6), lo16 = lane * 16;
  v16u ka; v4u kb; unsigned long long tptr, pc0; unsigned csz;
  asm volatile("s_load_dwordx16 %0, %5, 0x0\n\ts_load_dwordx4 %1, %5, 0x40\n\ts_load_dwordx2 %2, %5, %6 offset:0x10\n\t"
               "s_getpc_b64 %3\n.Lk_all_pc0:\n\ts_mov_b32 %4, (.Lk_all_end-.Lk_all_pc0)&0xfffffff0"
               : "=&s"(ka), "=&s"(kb), "=&s"(tptr), "=&s"(pc0), "=&s"(csz) : "s"(__builtin_amdgcn_kernarg_segment_ptr()), "s"(8 * w));
#define RSRC(p_, n_) __builtin_amdgcn_make_buffer_rsrc((void*)(p_), 0, (n_), 0x00020000)
  const v4u pfc = __builtin_amdgcn_raw_buffer_load_b128(RSRC(pc0 & ~127ull, w == 4 ? csz : 0u), lo16, j * 1024, 0);
  ldsC[tid & 15] = 0u;
  SB();
  const int h = xcd * 4 + (j >> 3), b = (j >> 2) & 1, s = j & 3;
  const int g = lane >> 4, c = lane & 15;
  const int r8 = j & 7;
  int qwo = (2 * g * H * DH + 8 * c) * 2, xso = (lane >> 5) * D * 2 + (lane & 31) * 4, qso = ((64 * r8 + 8 * w) * H * DH + h * DH) * 2;
  unsigned tof = ((unsigned)(b * H + h) * ((0x03u >> w) & 1u) << 21) + ((unsigned)h * ((0x04u >> w) & 1u) << 17);
  asm volatile("" : "+v"(qwo), "+v"(xso), "+v"(tof));
  asm volatile("s_waitcnt lgkmcnt(0)\n\ts_barrier" : "+s"(ka), "+s"(kb), "+s"(tptr), "+s"(qso) :: "memory");
#define KAP(i) ((i) < 8 ? (unsigned long long)ka[2 * (i) % 16] | ((unsigned long long)ka[(2 * (i) + 1) % 16] << 32) : (unsigned long long)kb[2 * (i) % 4] | ((unsigned long long)kb[(2 * (i) + 1) % 4] << 32))
  const unsigned long long px = KAP(0), pws = KAP(9);
  const unsigned long long ep = __hip_atomic_load((const GAS unsigned long long*)(tptr + tof), __ATOMIC_RELAXED, __HIP_MEMORY_SCOPE_AGENT);
  SB();
  const unsigned xst = __builtin_amdgcn_raw_buffer_load_b32(RSRC(px, 2 * D * 2), xso, 128 * w, 0);
  SB();
  const __amdgpu_buffer_rsrc_t rq = RSRC(KAP(1), D * H * DH * 2);
  const v4u sw0 = __builtin_amdgcn_raw_buffer_load_b128(rq, qwo, qso, 2), sw1 = __builtin_amdgcn_raw_buffer_load_b128(rq, qwo, qso + H * DH * 2, 2);
  v4u xq0, xq1;
  asm volatile("s_load_dwordx4 %0, %2, 0x0\n\ts_load_dwordx4 %1, %2, 0x400" : "=&s"(xq0), "=&s"(xq1) : "s"(px + (64 * r8 + 8 * w) * 2));
  SB();
  if (w == 0) {
    asm volatile("" :: "v"(ep));
    __hip_atomic_store(ldsC + 10, now16() | 1u, __ATOMIC_RELAXED, __HIP_MEMORY_SCOPE_WORKGROUP);
    MARK(40);
  }
  SB();
  const __amdgpu_buffer_rsrc_t rw = RSRC(KAP(4) + (size_t)h * DH * D * 2, DH * D * 2);
  const v4u pf0 = __builtin_amdgcn_raw_buffer_load_b128(rw, lo16, (16 * r8 + 2 * w) * 1024, 0), pf1 = __builtin_amdgcn_raw_buffer_load_b128(rw, lo16 + 1024, (16 * r8 + 2 * w) * 1024, 0);
  SB();
  const int fb = xcd * 3 + (j >> 3);
  const bool has_f1 = (j < 24) && (fb < 22);
  const bool is_fin = j >= 24;
  const int cid = xcd * 8 + (j - 24);
  const bool has_f2 = is_fin && cid < NFFN;
  const int row0 = 64 * r8 + 8 * w + 2 * g;
  const int fcol = 128 * (fb < 11 ? fb : fb - 11) + 8 * c;
  int fx1 = (row0 * F + fcol) * 2, fx2 = (32 * cid + 4 * w) * D * 2 + lo16;
  asm volatile("" : "+v"(fx1), "+v"(fx2));
  const int fxo = has_f1 ? (fcol < F ? fx1 : OOB - 4 * F * 2) : fx2, fxo2 = has_f1 ? OOB - 4096 : fx2, fst = has_f1 ? F * 2 : D * 2;
  unsigned f_lo = fb < 11 ? ka[10] : ka[12], f_hi = fb < 11 ? ka[11] : ka[13];
  asm volatile("" : "+s"(f_lo), "+s"(f_hi));
  const __amdgpu_buffer_rsrc_t rf = RSRC((unsigned long long)(has_f1 ? f_lo : ka[14]) | ((unsigned long long)(has_f1 ? f_hi : ka[15]) << 32), (has_f1 || has_f2) ? D * F * 2 : 0);
  MARK(41);
  v4u ffw[4];
  ffw[0] = __builtin_amdgcn_raw_buffer_load_b128(rf, fxo, 0, 2);
  ffw[1] = __builtin_amdgcn_raw_buffer_load_b128(rf, fxo, fst, 2);
  ffw[2] = __builtin_amdgcn_raw_buffer_load_b128(rf, fxo2 + 2048, 0, 2);
  ffw[3] = __builtin_amdgcn_raw_buffer_load_b128(rf, fxo2 + 3072, 0, 2);
  SB();
  MARK(42);
  const __amdgpu_buffer_rsrc_t rs = RSRC(pws, WS_END);

  const __amdgpu_buffer_rsrc_t rk = RSRC(KAP(2) + (size_t)(b * H + h) * SK * DH * 2, SK * DH * 2);
  const __amdgpu_buffer_rsrc_t rv = RSRC(KAP(3) + (size_t)(b * H + h) * SK * DH * 2, SK * DH * 2);
#define GOFF(gi_) ((((gi_) & 15) << 17) | (((gi_) & 4) << 12) | (((gi_) & 8) << 9))
  v4u rg[2 * GROUPS][U];

  constexpr float C2 = 0.08838834764831845f * 1.4426950408889634f;
  float sp[4] = {0.f, 0.f, 0.f, 0.f};
  float mw = 0.f;
  f32x2 bs[8];
  unsigned tagA, tagB, qp[4], slot0;
  v2u wor[32]; v4u wb[4][4], fu, qu, mu, e0, e1; unsigned short xo; float gte = 0.f;
  unsigned xs0, xs1;
  int fuoff, ysto, yoff;
  const int wvo = g * 8192 + w * 128 + c * 8;
  const int qoff = s1off(h, w, b) + lo16;
  const int pid = (b * H + h) * NSPLIT + s, muoff = WS_ML + ((pid & ~3) + (lane & 3)) * 16;
  const int bc = (cid >> 5) & 1, cu0 = (cid & 31) * 8;
  const int gp = tid >> 2, qd = tid & 3, ocol = bc * D + ucol(cu0 + (c >> 1)) + (c & 1);
  const int dpoff = WS_DP + ((gp < NFFN ? gp : 0) * 512 + bc * 256 + cu0 + 2 * qd) * 16;

  auto qk = [&](auto gic) __attribute__((always_inline)) {
    constexpr int gi = decltype(gic)::value;
    float d[U];
#pragma unroll
    for (int u = 0; u < U; ++u) d[u] = dot2(rg[gi][u][3], qp[3], dot2(rg[gi][u][2], qp[2], dot2(rg[gi][u][1], qp[1], dot2(rg[gi][u][0], qp[0], 0.f))));
    const bool o1 = c & 1, o2 = c & 2;
    const float x0 = (o1 ? d[1] : d[0]) + dpp<0xB1>(o1 ? d[0] : d[1]), x1 = (o1 ? d[3] : d[2]) + dpp<0xB1>(o1 ? d[2] : d[3]);
    float x = (o2 ? x1 : x0) + dpp<0x4E>(o2 ? x0 : x1);
    x += dpp<0x128>(x); x += dpp<0x124>(x);
    sp[gi / 4] = __builtin_bit_cast(float, __builtin_amdgcn_update_dpp(__builtin_bit_cast(int, sp[gi / 4]), __builtin_bit_cast(int, x), 0xE4, 0xf, 1 << (gi % 4), false));
    asm volatile("" : "+v"(sp[gi / 4]));
    SB();
  };
  auto pv = [&](auto gic) __attribute__((always_inline)) {
    constexpr int gi = decltype(gic)::value;
    const float p[4] = {dpp<0x150 + 4 * (gi % 4)>(sp[gi / 4]), dpp<0x151 + 4 * (gi % 4)>(sp[gi / 4]), dpp<0x152 + 4 * (gi % 4)>(sp[gi / 4]), dpp<0x153 + 4 * (gi % 4)>(sp[gi / 4])};
    float x[8];
#pragma unroll
    for (int k = 0; k < 4; ++k) { x[2 * k] = p[0] * lo(rg[GROUPS + gi][0][k]); x[2 * k + 1] = p[0] * hi(rg[GROUPS + gi][0][k]); }
#pragma unroll
    for (int u = 1; u < U; ++u) {
#pragma unroll
      for (int k = 0; k < 4; ++k) { x[2 * k] = fmaf(p[u], lo(rg[GROUPS + gi][u][k]), x[2 * k]); x[2 * k + 1] = fmaf(p[u], hi(rg[GROUPS + gi][u][k]), x[2 * k + 1]); }
    }
    const f32x2 y = fold8(x);
    if constexpr (gi < 8) bs[gi] = y; else bs[gi - 8] += y;
    if constexpr (gi >= 12) bs[gi - 12] += bs[gi - 8];
    if constexpr (gi >= 14) bs[gi - 14] += bs[gi - 12];
    asm volatile("" : "+v"(bs[gi >= 14 ? gi - 14 : gi >= 12 ? gi - 12 : gi % 8]));
    SB();
  };

  {
    unsigned tk;
    while ((tk = __builtin_amdgcn_readfirstlane(__hip_atomic_load(ldsC + 10, __ATOMIC_RELAXED, __HIP_MEMORY_SCOPE_WORKGROUP))) == 0u) __builtin_amdgcn_s_sleep(1);
    slot0 = __builtin_amdgcn_readfirstlane((__builtin_amdgcn_readfirstlane(tk) + (unsigned)T0X) / (unsigned)(TPX / 8) + (unsigned)w);
  }
  const int kvo = ((slot0 & 1) << 13) | ((slot0 & 6) << 14) | ((s & 1) << 12) | ((s >> 1) << 14);
  unsigned due = __builtin_amdgcn_readfirstlane(slot0 * (unsigned)(TPX / 8));

  auto point = [&](auto pc) __attribute__((always_inline)) {
    constexpr int p = decltype(pc)::value;
    const int so = kvo ^ GOFF((slot0 >> 3) + p);
    PACE(p);
    SB();
#pragma unroll
    for (int u = 0; u < U; ++u) rg[p][u] = __builtin_amdgcn_raw_buffer_load_b128(p < GROUPS ? rk : rv, lo16 + u * 1024, so, 2);
    SB();
    if constexpr (p == 4) qu = ld_unit(rs, qoff);
    if constexpr (p == 7) fu = ld_unit(rs, fuoff);
    if constexpr (p == 29) mu = ld_unit(rs, muoff);
#pragma unroll
    for (int i = won(p); i < won(p + 1); ++i) wor[i] = __builtin_amdgcn_raw_buffer_load_b64(rw, wvo, (32 * (i >> 3) + (i & 7)) * 1024, 0);
    if constexpr (p == 17) {
      e0 = ld_unit(rs, is_fin ? dpoff : OOB); e1 = ld_unit(rs, is_fin ? dpoff + 16 : OOB);
      xo = ((const GAS unsigned short*)px)[ocol];
    }
    SB();
    if constexpr (p == PI0_) {
    asm volatile("s_waitcnt lgkmcnt(0)" : "+s"(xq0), "+s"(xq1));
    xs0 = sel4(xq0, g); xs1 = sel4(xq1, g);
    float sm = row16_sum(lo(xst) + hi(xst)); sm = fold16(sm, sm) * (1.f / 64);
    const float d0 = lo(xst) - sm, d1 = hi(xst) - sm;
    float m2 = row16_sum(fmaf(d0, d0, d1 * d1)); m2 = fold16(m2, m2);
    if ((lane & 31) == 0) *(f32x2*)(ldsS + (lane >> 5) * 16 + 2 * w) = f32x2{sm, m2};
    float t[24];
#pragma unroll
    for (int k = 0; k < 4; ++k) {
      const float al = lo(sw0[k]), ah = hi(sw0[k]), bl = lo(sw1[k]), bh = hi(sw1[k]);
      t[2 * k] = fmaf(hi(xs0), bl, lo(xs0) * al); t[2 * k + 1] = fmaf(hi(xs0), bh, lo(xs0) * ah);
      t[8 + 2 * k] = fmaf(hi(xs1), bl, lo(xs1) * al); t[8 + 2 * k + 1] = fmaf(hi(xs1), bh, lo(xs1) * ah);
      t[16 + 2 * k] = al + bl; t[16 + 2 * k + 1] = ah + bh;
    }
#pragma unroll
    for (int k = 0; k < 8; ++k) t[k] = fold16(fold32(t[k], t[16 + k]), fold32(t[8 + k], t[16 + k]));
    *(f32x4*)(ldsY + w * 512 + g * 128 + 8 * c) = f32x4{t[0], t[1], t[2], t[3]};
    *(f32x4*)(ldsY + w * 512 + g * 128 + 8 * c + 4) = f32x4{t[4], t[5], t[6], t[7]};
    if (tid == 448) *(unsigned long long*)(ldsC + 8) = ep;
    asm volatile("" :: "v"(ep));
    if (last_wave(ldsC, lane)) {
      f32x4 st[8];
#pragma unroll
      for (int i = 0; i < 8; ++i) st[i] = *(const f32x4*)(ldsS + 4 * i);
      tagA = __builtin_amdgcn_readfirstlane(ldsC[8]) + 1u; tagB = __builtin_amdgcn_readfirstlane(ldsC[9]) + 0x9E3779B9u;
      const float mean0 = ((st[0][0] + st[0][2]) + (st[1][0] + st[1][2]) + (st[2][0] + st[2][2]) + (st[3][0] + st[3][2])) * 0.125f;
      const float mean1 = ((st[4][0] + st[4][2]) + (st[5][0] + st[5][2]) + (st[6][0] + st[6][2]) + (st[7][0] + st[7][2])) * 0.125f;
      float q0 = 0.f, q1 = 0.f, d0 = 0.f, d1 = 0.f;
#pragma unroll
      for (int i = 0; i < 4; ++i) {
        q0 += st[i][1] + st[i][3]; q1 += st[4 + i][1] + st[4 + i][3];
        const float a0 = st[i][0] - mean0, b0 = st[i][2] - mean0, a1 = st[4 + i][0] - mean1, b1 = st[4 + i][2] - mean1;
        d0 += fmaf(a0, a0, b0 * b0); d1 += fmaf(a1, a1, b1 * b1);
      }
      const float rstd0 = __builtin_amdgcn_rsqf(fmaf(64.f, d0, q0) * (1.f / D) + 1e-5f), rstd1 = __builtin_amdgcn_rsqf(fmaf(64.f, d1, q1) * (1.f / D) + 1e-5f);
      if (lane == 0) *(f32x4*)(ldsC + 12) = f32x4{mean0, rstd0, mean1, rstd1};
      last_wave(ldsC, lane);
      f32x2 z0 = {0.f, 0.f}, z1 = {0.f, 0.f}, cs = {0.f, 0.f};
#pragma unroll
      for (int ww = 0; ww < NWAVE; ++ww) {
        z0 += *(const f32x2*)(ldsY + ww * 512 + 2 * lane); z1 += *(const f32x2*)(ldsY + ww * 512 + 128 + 2 * lane); cs += *(const f32x2*)(ldsY + ww * 512 + 256 + 2 * lane); }
      st_unit(rs, s1off(h, r8, 0) + lo16, (z0[0] - mean0 * cs[0]) * rstd0, (z0[1] - mean0 * cs[1]) * rstd0, tagA, tagB);
      st_unit(rs, s1off(h, r8, 1) + lo16, (z1[0] - mean1 * cs[0]) * rstd1, (z1[1] - mean1 * cs[1]) * rstd1, tagA, tagB);
    }
  }
    if constexpr (p == PI0_ + 2) {
      wait_all(ldsC, NWAVE + 1);
      tagA = __builtin_amdgcn_readfirstlane(ldsC[8]) + 1u; tagB = __builtin_amdgcn_readfirstlane(ldsC[9]) + 0x9E3779B9u;
      asm volatile("" :: "v"(pf0), "v"(pf1), "v"(pfc));
      if (has_f1) {
        const f32x4 ln = *(const f32x4*)(ldsC + 12);
        const float n00 = (lo(xs0) - ln[0]) * ln[1], n01 = (hi(xs0) - ln[0]) * ln[1], n10 = (lo(xs1) - ln[2]) * ln[3], n11 = (hi(xs1) - ln[2]) * ln[3];
        float t[8];
#pragma unroll
        for (int k = 0; k < 4; ++k) {
          const float al = lo(ffw[0][k]), ah = hi(ffw[0][k]), bl = lo(ffw[1][k]), bh = hi(ffw[1][k]);
          t[2 * k] = fold32(fmaf(n01, bl, n00 * al), fmaf(n11, bl, n10 * al)); t[2 * k + 1] = fold32(fmaf(n01, bh, n00 * ah), fmaf(n11, bh, n10 * ah));
        }
        *(f32x4*)(ldsY + 4096 + w * 256 + (g >> 1) * 128 + 8 * c + 4 * (g & 1)) = f32x4{fold16(t[0], t[4]), fold16(t[1], t[5]), fold16(t[2], t[6]), fold16(t[3], t[7])};
        last_wave(ldsC + 2, lane);
      }
    }
    if constexpr (p == 5) {
      int gq = g; asm volatile("" : "+v"(gq));
      const int fbs = (gq >> 1) * 11 + (cid >> 2), hs = (fbs / 3) * 4 + fbs % 3;
      fuoff = has_f2 ? s1off(hs, w, 2 + (g & 1)) + (16 * (cid & 3) + c) * 16 : OOB;
      asm volatile("" : "+v"(fuoff));
    }
    if constexpr (p == 4) {
      if (has_f1 && w < 2) {
        wait_all(ldsC + 2);
        f32x2 z = {0.f, 0.f};
#pragma unroll
        for (int ww = 0; ww < NWAVE; ++ww) z += *(const f32x2*)(ldsY + 4096 + ww * 256 + 2 * tid);
        st_unit(rs, s1off(h, r8, 2 + w) + lo16, z[0], z[1], tagA, tagB);
      }
    }
    if constexpr (p == 6) {
      await_unit(qu, rs, qoff, tagA, tagB);
      *(f32x2*)(ldsQ + w * 128 + 2 * lane) = f32x2{__uint_as_float(qu[0]), __uint_as_float(qu[2])};
      last_wave(ldsC + 1, lane);
    }
    if constexpr (p == 8) {
      wait_all(ldsC + 1);
      f32x4 q0 = {0, 0, 0, 0}, q1 = {0, 0, 0, 0};
#pragma unroll
      for (int ww = 0; ww < NWAVE; ++ww) { q0 += *(const f32x4*)(ldsQ + ww * 128 + 8 * c); q1 += *(const f32x4*)(ldsQ + ww * 128 + 8 * c + 4); }
      qp[0] = pk_bf16(q0[0], q0[1]); qp[1] = pk_bf16(q0[2], q0[3]); qp[2] = pk_bf16(q1[0], q1[1]); qp[3] = pk_bf16(q1[2], q1[3]);
      qk(IC<0>{});
    }
    if constexpr (p == 9) { qk(IC<1>{}); qk(IC<2>{}); qk(IC<3>{}); }
    if constexpr (p == 10) { qk(IC<4>{}); qk(IC<5>{}); }
    if constexpr (p >= 10 && p < GROUPS + 4) qk(IC<p - 4>{});
    if constexpr (p == 10) {
      if (has_f2) {
        await_unit(fu, rs, fuoff, tagA, tagB);
        *(f32x2*)(ldsF + w * 128 + g * 32 + 2 * c) = f32x2{__uint_as_float(fu[0]), __uint_as_float(fu[2])};
        last_wave(ldsC + 5, lane);
      }
    }
    if constexpr (p == 12) {
      if (has_f2) {
        wait_all(ldsC + 5);
        float gs = 0.f, us = 0.f;
        const int hx = ((lane >> 2) & 1) * 32 + 4 * w + (lane & 3);
#pragma unroll
        for (int ww = 0; ww < NWAVE; ++ww) { gs += ldsF[ww * 128 + hx]; us += ldsF[ww * 128 + 64 + hx]; }
        const float hv = gelu_tanh(gs) * us;
        float dd[16];
#pragma unroll
        for (int k = 0; k < 16; ++k) dd[k] = 0.f;
#pragma unroll
        for (int r = 0; r < 4; ++r) {
          const float h0 = __builtin_bit_cast(float, __builtin_amdgcn_readlane(__builtin_bit_cast(int, hv), r));
          const float h1 = __builtin_bit_cast(float, __builtin_amdgcn_readlane(__builtin_bit_cast(int, hv), 4 + r));
#pragma unroll
          for (int k = 0; k < 4; ++k) {
            const float wl = lo(ffw[r][k]), wh = hi(ffw[r][k]);
            dd[2 * k] = fmaf(h0, wl, dd[2 * k]); dd[2 * k + 1] = fmaf(h0, wh, dd[2 * k + 1]);
            dd[8 + 2 * k] = fmaf(h1, wl, dd[8 + 2 * k]); dd[8 + 2 * k + 1] = fmaf(h1, wh, dd[8 + 2 * k + 1]);
          }
        }
        *(f32x4*)(ldsY + w * 1024 + 8 * lane) = f32x4{dd[0], dd[1], dd[2], dd[3]};
        *(f32x4*)(ldsY + w * 1024 + 8 * lane + 4) = f32x4{dd[4], dd[5], dd[6], dd[7]};
        *(f32x4*)(ldsY + w * 1024 + 512 + 8 * lane) = f32x4{dd[8], dd[9], dd[10], dd[11]};
        *(f32x4*)(ldsY + w * 1024 + 512 + 8 * lane + 4) = f32x4{dd[12], dd[13], dd[14], dd[15]};
        last_wave(ldsC + 3, lane);
      }
    }
    if constexpr (p == 14) {
      if (has_f2) {
        wait_all(ldsC + 3);
        f32x2 dv = {0.f, 0.f};
#pragma unroll
        for (int ww = 0; ww < NWAVE; ++ww) dv += *(const f32x2*)(ldsY + ww * 1024 + (tid & 256) * 2 + ucol(tid & 255));
        st_unit(rs, WS_DP + (cid * 512 + tid) * 16, dv[0], dv[1], tagA, tagB);
      }
    }
    if constexpr (p == 23) {
      if (is_fin) {
        await_unit(e0, rs, dpoff, tagA, tagB);
        await_unit(e1, rs, dpoff + 16, tagA, tagB);
        const bool hd = gp < NFFN;
        const float r = fold4(hd ? __uint_as_float(e0[0]) : 0.f, hd ? __uint_as_float(e0[2]) : 0.f, hd ? __uint_as_float(e1[0]) : 0.f, hd ? __uint_as_float(e1[2]) : 0.f);
        if (c < 4) ldsF[w * 16 + 4 * c + g] = r;
        last_wave(ldsC + 2, lane);
      }
    }
    if constexpr (p == GROUPS + 3) {
      float x = fmaxf(fmaxf(sp[0], sp[1]), fmaxf(sp[2], sp[3]));
      x = fmaxf(x, dpp<0x128>(x)); x = fmaxf(x, dpp<0x124>(x)); x = fmaxf(x, dpp<0x122>(x)); x = fmaxf(x, dpp<0x121>(x));
      mw = xrow_max(x) * C2;
#pragma unroll
      for (int i = 0; i < 4; ++i) sp[i] = __builtin_amdgcn_exp2f(fmaf(sp[i], C2, -mw));
      const float lw = xrow_sum(row16_sum((sp[0] + sp[2]) + (sp[1] + sp[3])));
      if (lane == 0) { ldsM[slot0 & 7] = mw; ldsM[8 + (slot0 & 7)] = lw; }
      last_wave(ldsC + 4, lane);
    }
    if constexpr (p == 21) {
      if (w == 0) {
        wait_all(ldsC + 4);
        const float mi = ldsM[lane & 7], li = ldsM[8 + (lane & 7)];
        float M = fmaxf(mi, dpp<0x124>(mi)); M = fmaxf(M, dpp<0x122>(M)); M = fmaxf(M, dpp<0x121>(M));
        float L = __builtin_amdgcn_exp2f(mi - M) * li; L += dpp<0x124>(L); L += dpp<0x122>(L); L += dpp<0x121>(L);
        if (lane == 0) st_unit(rs, WS_ML + pid * 16, M, L, tagA, tagB);
      }
    }
    if constexpr (p == 26) {
      if (is_fin && w == 0) {
        wait_all(ldsC + 2);
        const float dv = ldsF[c] + ldsF[16 + c] + ldsF[32 + c];
        gte = dv * __builtin_amdgcn_rcpf(1.f + __expf(-dv));
        asm volatile("" : "+v"(gte));
      }
    }
    if constexpr (p == 31) {
      await_unit(mu, rs, muoff, tagA, tagB);
      const float mi = __uint_as_float(mu[0]);
      float M = fmaxf(mi, dpp<0xB1>(mi)); M = fmaxf(M, dpp<0x4E>(M));
      float L = __builtin_amdgcn_exp2f(mi - M) * __uint_as_float(mu[2]); L += dpp<0xB1>(L); L += dpp<0x4E>(L);
      mw = __builtin_amdgcn_exp2f(mw - M) * __builtin_amdgcn_rcpf(L);
      ysto = (g & 1) ? OOB : WS_Y + (pid * 256 + 32 * w + 16 * (g >> 1) + c) * 16;
      yoff = WS_Y + ((bc * 128 + gp) * 256 + cu0 + 2 * qd) * 16;
      asm volatile("" : "+v"(mw), "+v"(ysto), "+v"(yoff));
    }
    if constexpr (p >= 20 && p < 28) pv(IC<p - 20>{});
    if constexpr (p == 28 || p == 29) { pv(IC<2 * p - 48>{}); pv(IC<2 * p - 47>{}); }
    if constexpr (p == 30) pv(IC<12>{});
    if constexpr (p == 31) { pv(IC<13>{}); pv(IC<14>{}); pv(IC<15>{}); }
    if constexpr (p == 30) {
#pragma unroll
      for (int i = 0; i < 16; ++i) {
        const v2u r0 = wor[2 * i], r1 = wor[2 * i + 1];
        wb[0][i >> 2][i & 3] = __builtin_amdgcn_perm(r1[0], r0[0], 0x05040100u); wb[1][i >> 2][i & 3] = __builtin_amdgcn_perm(r1[0], r0[0], 0x07060302u);
        wb[2][i >> 2][i & 3] = __builtin_amdgcn_perm(r1[1], r0[1], 0x05040100u); wb[3][i >> 2][i & 3] = __builtin_amdgcn_perm(r1[1], r0[1], 0x07060302u);
      }
#pragma unroll
      for (int i = 0; i < 4; ++i) asm volatile("" : "+v"(wb[i][0]), "+v"(wb[i][1]), "+v"(wb[i][2]), "+v"(wb[i][3]));
    }
    SB();
  };
  static_for<2 * GROUPS>(point);
  MARK(34);

  {
    const f32x2 o2 = (bs[0] + bs[1]) * mw;
    const unsigned oh = pk_bf16(o2[0], o2[1]), ol = pk_bf16(o2[0] - lo(oh), o2[1] - hi(oh));
    const int ar = (slot0 & 7) * 68 + 4 * c + g;
    ldsA[ar] = oh; ldsA[8 * 68 + ar] = ol;
  }
  __syncthreads();
  MARK(35);
  {
    v4u af[4];
#pragma unroll
    for (int ks = 0; ks < 4; ++ks) af[ks] = *(const v4u*)(ldsA + c * 68 + 16 * ks + 4 * g);
    SB();
    f32x4 d[4];
#pragma unroll
    for (int t = 0; t < 4; ++t) d[t] = f32x4{0.f, 0.f, 0.f, 0.f};
#pragma unroll
    for (int ks = 0; ks < 4; ++ks)
#pragma unroll
      for (int t = 0; t < 4; ++t) d[t] = __builtin_amdgcn_mfma_f32_16x16x32_bf16(__builtin_bit_cast(bf16x8, af[ks]), __builtin_bit_cast(bf16x8, wb[t][ks]), d[t], 0, 0, 0);
    float y[4];
#pragma unroll
    for (int t = 0; t < 4; ++t) y[t] = (d[t][0] + d[t][1]) + (d[t][2] + d[t][3]);
    float u0 = fold32(y[0], y[2]), u1 = fold32(y[1], y[3]);
    u0 = fold16(u0, u0); u1 = fold16(u1, u1);
    st_unit<YSA_>(rs, ysto, u0, u1, tagA, tagB);
  }
  MARK(36);

  if (is_fin) {
    __builtin_amdgcn_s_barrier();
    if (PDLY_) __builtin_amdgcn_s_sleep(PDLY_);
    MARK(38);
    v4u y0, y1;
    bool ok = false;
#pragma unroll 1
    for (unsigned spin = 0; spin < SPIN_CAP; ++spin) {
      asm volatile("" ::: "memory");
      if (!ok) {
        y0 = ld_unit(rs, yoff); y1 = ld_unit(rs, yoff + 16);
        ok = (((y0[1] ^ tagA) | (y1[1] ^ tagA)) | ((y0[3] ^ tagB) | (y1[3] ^ tagB))) == 0u;
      }
      if (__all(ok)) break;
      __builtin_amdgcn_s_sleep(RSL_);
    }
    MARK(39);
    const float r = fold4(__uint_as_float(y0[0]), __uint_as_float(y0[2]), __uint_as_float(y1[0]), __uint_as_float(y1[2]));
    if (c < 4) ldsF[128 + w * 16 + 4 * c + g] = r;
    __syncthreads();
    if (tid < 16) {
      float sv = 0.f;
#pragma unroll
      for (int ww = 0; ww < NWAVE; ++ww) sv += ldsF[128 + ww * 16 + tid];
      const float o = __uint_as_float((unsigned)xo << 16) + sv * gte;
      const __bf16 ob = (__bf16)o;
      ((GAS unsigned short*)KAP(8))[ocol] = __builtin_bit_cast(unsigned short, ob);
      if (cid == 0 && tid == 0)
        __hip_atomic_store((GAS unsigned long long*)(pws + WS_EPOCH), ((unsigned long long)tagB << 32) | tagA, __ATOMIC_RELAXED, __HIP_MEMORY_SCOPE_AGENT);
    }
  }
  asm volatile(".Lk_all_end:");
}

extern "C" void kernel_launch(void* const* d_in, const int* in_sizes, int n_in, void* d_out, int out_size, void* d_ws, size_t ws_size,
                              hipStream_t stream) {
  Args a;
  a.x = (const unsigned short*)d_in[0];
  a.k = (const unsigned short*)d_in[1];
  a.v = (const unsigned short*)d_in[2];
  a.wq = (const unsigned short*)d_in[3];
  a.wo = (const unsigned short*)d_in[4];
  a.wg = (const unsigned short*)d_in[6];
  a.wu = (const unsigned short*)d_in[7];
  a.wd = (const unsigned short*)d_in[8];
  a.out = (unsigned short*)d_out;
  a.ws = (unsigned char*)d_ws;
  hipLaunchKernelGGL(k_all, dim3(256), dim3(NTHR), 0, stream, a);
}
```

```cpp
#include <hip/hip_runtime.h>
#include <hip/hip_bf16.h>
#include <stdint.h>

typedef unsigned v4u __attribute__((ext_vector_type(4)));
typedef float f32x4 __attribute__((ext_vector_type(4)));
typedef float f32x2 __attribute__((ext_vector_type(2)));
typedef __bf16 bf16x2 __attribute__((ext_vector_type(2)));
typedef __bf16 bf16x8 __attribute__((ext_vector_type(8)));
typedef unsigned v16u __attribute__((ext_vector_type(16)));
typedef unsigned v2u __attribute__((ext_vector_type(2)));
#define GAS __attribute__((address_space(1)))
#define CAS __attribute__((address_space(4)))

constexpr int D = 512, H = 32, SK = 8192, DH = 128, F = 1376;
constexpr int NSPLIT = 4, KEYS_PER_WG = SK / NSPLIT;
constexpr int NWAVE = 8, NTHR = NWAVE * 64;
constexpr int NFFN = F / 32;
constexpr int U = 4;
constexpr int GROUPS = KEYS_PER_WG / NWAVE / (4 * U);
#define TP_ 1728
#define T0_ 2100
#define EX1_ 125
#define EX2_ 100
constexpr unsigned step_of(int p) { return TP_ + (p < 9 ? EX1_ : p >= 14 && p < 18 ? EX2_ : 0); }
constexpr int LATE_PRIO = 400;
constexpr int won(int p) { return p <= 1 ? 0 : p >= 28 ? 32 : (p - 1) * 32 / 27; }
static_assert(TP_ % 8 == 0, "a slot is TP / 8");

constexpr int WS_EPOCH = 0;
constexpr int WS_ML = 4096;
constexpr int WS_Y = WS_ML + 256 * 16;
constexpr int WS_DP = WS_Y + 256 * 256 * 16;
constexpr int WS_S1 = WS_DP + NFFN * 512 * 16;
constexpr int WS_END = WS_S1 + 32 * 8 * 4 * 64 * 16;
constexpr int OOB = 0x7ffffff0;
constexpr unsigned SPIN_CAP = 400000;

#define SB() __builtin_amdgcn_sched_barrier(0)

__device__ __forceinline__ float dot2(unsigned k, unsigned q, float acc) {
  return __builtin_amdgcn_fdot2_f32_bf16(__builtin_bit_cast(bf16x2, k), __builtin_bit_cast(bf16x2, q), acc, false);
}
template <int CTRL> __device__ __forceinline__ float dpp(float x) {
  return __builtin_bit_cast(float, __builtin_amdgcn_mov_dpp(__builtin_bit_cast(int, x), CTRL, 0xf, 0xf, true));
}
__device__ __forceinline__ float row16_sum(float x) {
  x += dpp<0x128>(x);
  x += dpp<0x124>(x);
  x += dpp<0x122>(x);
  x += dpp<0x121>(x);
  return x;
}
__device__ __forceinline__ float fold32(float a, float b) {
  auto t = __builtin_amdgcn_permlane32_swap(__float_as_uint(a), __float_as_uint(b), false, false);
  return __uint_as_float(t[0]) + __uint_as_float(t[1]);
}
__device__ __forceinline__ float fold16(float a, float b) {
  auto t = __builtin_amdgcn_permlane16_swap(__float_as_uint(a), __float_as_uint(b), false, false);
  return __uint_as_float(t[0]) + __uint_as_float(t[1]);
}
__device__ __forceinline__ float xrow_sum(float x) { x = fold16(x, x); return fold32(x, x); }
__device__ __forceinline__ float xrow_max(float x) {
  auto s = __builtin_amdgcn_permlane16_swap(__float_as_uint(x), __float_as_uint(x), false, false);
  x = fmaxf(__uint_as_float(s[0]), __uint_as_float(s[1]));
  auto t = __builtin_amdgcn_permlane32_swap(__float_as_uint(x), __float_as_uint(x), false, false);
  return fmaxf(__uint_as_float(t[0]), __uint_as_float(t[1]));
}
__device__ __forceinline__ f32x2 fold8(const float (&v)[8]) {
  const float u0 = fold32(v[0], v[4]), u1 = fold32(v[1], v[5]), u2 = fold32(v[2], v[6]), u3 = fold32(v[3], v[7]);
  return f32x2{fold16(u0, u2), fold16(u1, u3)};
}
__device__ __forceinline__ float fold4(float v0, float v1, float v2, float v3) {
  float r = fold16(fold32(v0, v2), fold32(v1, v3));
  r += dpp<0x124>(r); r += dpp<0x128>(r);
  return r;
}
__device__ __forceinline__ float lo(unsigned u) { return __uint_as_float(u << 16); }
__device__ __forceinline__ float hi(unsigned u) { return __uint_as_float(u & 0xffff0000u); }
__device__ __forceinline__ unsigned pk_bf16(float a, float b) {
  bf16x2 r = {(__bf16)a, (__bf16)b};
  return __builtin_bit_cast(unsigned, r);
}
__device__ __forceinline__ unsigned sel4(v4u v, int g) { const unsigned a = (g & 1) ? v[1] : v[0], b = (g & 1) ? v[3] : v[2]; return (g & 2) ? b : a; }
__device__ __forceinline__ int ucol(int u) { return 64 * (u >> 5) + 4 * (u & 15) + 2 * ((u >> 4) & 1); }
__device__ __forceinline__ int s1off(int hg, int r, int k) { return WS_S1 + (hg * 8 + ((r + hg) & 7)) * 4096 + ((k ^ r ^ (hg >> 3)) & 3) * 1024; }
__device__ __forceinline__ float gelu_tanh(float x) {
  const float u = 0.7978845608028654f * (x + 0.044715f * x * x * x);
  const float e = __expf(2.f * u);
  const float t = 1.f - 2.f * __builtin_amdgcn_rcpf(e + 1.f);
  return 0.5f * x * (1.f + t);
}
__device__ __forceinline__ void pace_wait(unsigned& due16, unsigned step16) {
  asm volatile("1:\n\ts_memrealtime s[90:91]\n\ts_waitcnt lgkmcnt(0)\n\ts_lshl_b32 s90, s90, 4\n\ts_sub_u32 s91, s90, %0\n\ts_cmp_ge_i32 s91, 0\n\ts_cbranch_scc1 2f\n\ts_sleep 2\n\ts_branch 1b\n"
               "2:\n\ts_add_u32 %0, %0, %1\n\ts_setprio 0\n\ts_cmp_lt_i32 s91, %2\n\ts_cbranch_scc1 3f\n\ts_setprio 2\n3:"
               : "+s"(due16) : "s"(step16), "i"(LATE_PRIO) : "s90", "s91", "scc");
}
__device__ __forceinline__ unsigned now16() { return (unsigned)__builtin_amdgcn_s_memrealtime() << 4; }
constexpr int AUX_SC1 = 16;
__device__ __forceinline__ void st_unit(__amdgpu_buffer_rsrc_t rs, int off, float v0, float v1, unsigned ta, unsigned tb) {
  __builtin_amdgcn_raw_buffer_store_b128(v4u{__float_as_uint(v0), ta, __float_as_uint(v1), tb}, rs, off, 0, AUX_SC1);
}
__device__ __forceinline__ v4u ld_unit(__amdgpu_buffer_rsrc_t rs, int off) { return __builtin_amdgcn_raw_buffer_load_b128(rs, off, 0, AUX_SC1); }
__device__ __forceinline__ void await_unit(v4u& u, __amdgpu_buffer_rsrc_t rs, int off, unsigned ta, unsigned tb) {
  if (!__all(((u[1] ^ ta) | (u[3] ^ tb)) == 0u)) {
#pragma unroll 1
    for (unsigned spin = 0; spin < SPIN_CAP; ++spin) {
      __builtin_amdgcn_s_sleep(1);
      asm volatile("" ::: "memory");
      u = ld_unit(rs, off);
      if (__all(((u[1] ^ ta) | (u[3] ^ tb)) == 0u)) break;
    }
  }
}

__device__ __forceinline__ bool last_wave(unsigned* cnt, int lane) {
  asm volatile("s_waitcnt lgkmcnt(0)" ::: "memory");
  unsigned old = 0;
  if (lane == 0) old = __hip_atomic_fetch_add(cnt, 1u, __ATOMIC_RELAXED, __HIP_MEMORY_SCOPE_WORKGROUP);
  old = __builtin_amdgcn_readfirstlane(old);
  asm volatile("" ::: "memory");
  return old == NWAVE - 1;
}

__device__ __forceinline__ void wait_all(unsigned* cnt, unsigned n = NWAVE) {
  while (__builtin_amdgcn_readfirstlane(__hip_atomic_load(cnt, __ATOMIC_RELAXED, __HIP_MEMORY_SCOPE_WORKGROUP)) < n) __builtin_amdgcn_s_sleep(1);
  asm volatile("" ::: "memory");
}
template <int I> struct IC { static constexpr int value = I; };
template <int N, int I = 0, typename Fn> __device__ __forceinline__ void static_for(Fn& fn) { if constexpr (I < N) { fn(IC<I>{}); static_for<N, I + 1>(fn); } }

struct Args {
  const unsigned short* x; const unsigned short* wq;
  const unsigned short* k; const unsigned short* v; const unsigned short* wo; const unsigned short* wg;
  const unsigned short* wu; const unsigned short* wd; unsigned short* out; unsigned char* ws;
};

__global__ void __launch_bounds__(NTHR, 2) k_all(Args a) {
  __shared__ __attribute__((aligned(16))) float lds[8 * 1024 + 16 * 68 + 16 + 8 * 128 + 8 * 128 + 32 + 16];
  float* ldsY = lds;
  unsigned* ldsA = (unsigned*)(lds + 8 * 1024);
  float* ldsM = lds + 8 * 1024 + 16 * 68;
  float* ldsF = ldsM + 16;
  float* ldsQ = ldsF + 8 * 128;
  float* ldsS = ldsQ + 8 * 128;
  unsigned* ldsC = (unsigned*)(ldsS + 32);
  const int bid = blockIdx.x;
  const int xcd = bid & 7, j = bid >> 3;
  const int tid = threadIdx.x, lane = tid & 63, w = __builtin_amdgcn_readfirstlane(tid >> 6), lo16 = lane * 16;
  v16u ka; v4u kb; unsigned long long tptr, pc0; unsigned csz;
  asm volatile("s_load_dwordx16 %0, %5, 0x0\n\ts_load_dwordx4 %1, %5, 0x40\n\ts_load_dwordx2 %2, %5, %6 offset:0x10\n\t"
               "s_getpc_b64 %3\n.Lk_all_pc0:\n\ts_mov_b32 %4, (.Lk_all_end-.Lk_all_pc0)&0xfffffff0"
               : "=&s"(ka), "=&s"(kb), "=&s"(tptr), "=&s"(pc0), "=&s"(csz) : "s"(__builtin_amdgcn_kernarg_segment_ptr()), "s"(8 * w));
#define RSRC(p_, n_) __builtin_amdgcn_make_buffer_rsrc((void*)(p_), 0, (n_), 0x00020000)
  const v4u pfc = __builtin_amdgcn_raw_buffer_load_b128(RSRC(pc0 & ~127ull, w == 4 ? csz : 0u), lo16, j * 1024, 0);
  ldsC[tid & 15] = 0u;
  SB();
  const int h = xcd * 4 + (j >> 3), b = (j >> 2) & 1, s = j & 3;
  const int g = lane >> 4, c = lane & 15;
  const int r8 = j & 7;
  int qwo = (2 * g * H * DH + 8 * c) * 2, xso = (lane >> 5) * D * 2 + (lane & 31) * 4, qso = ((64 * r8 + 8 * w) * H * DH + h * DH) * 2;
  unsigned tof = ((unsigned)(b * H + h) * ((0x03u >> w) & 1u) << 21) + ((unsigned)h * ((0x04u >> w) & 1u) << 17);
  asm volatile("" : "+v"(qwo), "+v"(xso), "+v"(tof));
  asm volatile("s_waitcnt lgkmcnt(0)\n\ts_barrier" : "+s"(ka), "+s"(kb), "+s"(tptr), "+s"(qso) :: "memory");
#define KAP(i) ((i) < 8 ? (unsigned long long)ka[2 * (i) % 16] | ((unsigned long long)ka[(2 * (i) + 1) % 16] << 32) : (unsigned long long)kb[2 * (i) % 4] | ((unsigned long long)kb[(2 * (i) + 1) % 4] << 32))
  const unsigned long long px = KAP(0), pws = KAP(9);
  const unsigned long long ep = __hip_atomic_load((const GAS unsigned long long*)(tptr + tof), __ATOMIC_RELAXED, __HIP_MEMORY_SCOPE_AGENT);
  SB();
  const unsigned xst = __builtin_amdgcn_raw_buffer_load_b32(RSRC(px, 2 * D * 2), xso, 128 * w, 0);
  SB();
  const __amdgpu_buffer_rsrc_t rq = RSRC(KAP(1), D * H * DH * 2);
  const v4u sw0 = __builtin_amdgcn_raw_buffer_load_b128(rq, qwo, qso, 2), sw1 = __builtin_amdgcn_raw_buffer_load_b128(rq, qwo, qso + H * DH * 2, 2);
  v4u xq0, xq1;
  asm volatile("s_load_dwordx4 %0, %2, 0x0\n\ts_load_dwordx4 %1, %2, 0x400" : "=&s"(xq0), "=&s"(xq1) : "s"(px + (64 * r8 + 8 * w) * 2));
  SB();
  if (w == 0) {
    asm volatile("" :: "v"(ep));
    __hip_atomic_store(ldsC + 10, now16() | 1u, __ATOMIC_RELAXED, __HIP_MEMORY_SCOPE_WORKGROUP);
  }
  SB();
  const __amdgpu_buffer_rsrc_t rw = RSRC(KAP(4) + (size_t)h * DH * D * 2, DH * D * 2);
  const v4u pf0 = __builtin_amdgcn_raw_buffer_load_b128(rw, lo16, (16 * r8 + 2 * w) * 1024, 0), pf1 = __builtin_amdgcn_raw_buffer_load_b128(rw, lo16 + 1024, (16 * r8 + 2 * w) * 1024, 0);
  SB();
  const int fb = xcd * 3 + (j >> 3);
  const bool has_f1 = (j < 24) && (fb < 22);
  const bool is_fin = j >= 24;
  const int cid = xcd * 8 + (j - 24);
  const bool has_f2 = is_fin && cid < NFFN;
  const int row0 = 64 * r8 + 8 * w + 2 * g;
  const int fcol = 128 * (fb < 11 ? fb : fb - 11) + 8 * c;
  int fx1 = (row0 * F + fcol) * 2, fx2 = (32 * cid + 4 * w) * D * 2 + lo16;
  asm volatile("" : "+v"(fx1), "+v"(fx2));
  const int fxo = has_f1 ? (fcol < F ? fx1 : OOB - 4 * F * 2) : fx2, fxo2 = has_f1 ? OOB - 4096 : fx2, fst = has_f1 ? F * 2 : D * 2;
  unsigned f_lo = fb < 11 ? ka[10] : ka[12], f_hi = fb < 11 ? ka[11] : ka[13];
  asm volatile("" : "+s"(f_lo), "+s"(f_hi));
  const __amdgpu_buffer_rsrc_t rf = RSRC((unsigned long long)(has_f1 ? f_lo : ka[14]) | ((unsigned long long)(has_f1 ? f_hi : ka[15]) << 32), (has_f1 || has_f2) ? D * F * 2 : 0);
  v4u ffw[4];
  ffw[0] = __builtin_amdgcn_raw_buffer_load_b128(rf, fxo, 0, 2);
  ffw[1] = __builtin_amdgcn_raw_buffer_load_b128(rf, fxo, fst, 2);
  ffw[2] = __builtin_amdgcn_raw_buffer_load_b128(rf, fxo2 + 2048, 0, 2);
  ffw[3] = __builtin_amdgcn_raw_buffer_load_b128(rf, fxo2 + 3072, 0, 2);
  SB();
  const __amdgpu_buffer_rsrc_t rs = RSRC(pws, WS_END);

  const __amdgpu_buffer_rsrc_t rk = RSRC(KAP(2) + (size_t)(b * H + h) * SK * DH * 2, SK * DH * 2);
  const __amdgpu_buffer_rsrc_t rv = RSRC(KAP(3) + (size_t)(b * H + h) * SK * DH * 2, SK * DH * 2);
#define GOFF(gi_) (((gi_) & 1) * ((1 << 20) | (1 << 12)) | ((gi_) & 2) * ((1 << 18) | (1 << 13)) | (((gi_) & 4) << 16) | (((gi_) & 8) << 14))
  v4u rg[2 * GROUPS][U];

  constexpr float C2 = 0.08838834764831845f * 1.4426950408889634f;
  float sp[4] = {0.f, 0.f, 0.f, 0.f};
  float mw = 0.f;
  f32x2 bs[8];
  unsigned tagA, tagB, qp[4], slot0;
  v2u wor[32]; v4u wb[4][4], fu, qu, mu, e0, e1; unsigned short xo; float gte = 0.f;
  unsigned xs0, xs1;
  int fuoff, ysto, yoff;
  const int wvo = g * 8192 + w * 128 + c * 8;
  const int qoff = s1off(h, w, b) + lo16;
  const int pid = (b * H + h) * NSPLIT + s, muoff = WS_ML + ((pid & ~3) + (lane & 3)) * 16;
  const int bc = (cid >> 5) & 1, cu0 = (cid & 31) * 8;
  const int gp = tid >> 2, qd = tid & 3, ocol = bc * D + ucol(cu0 + (c >> 1)) + (c & 1);
  const int dpoff = WS_DP + ((gp < NFFN ? gp : 0) * 512 + bc * 256 + cu0 + 2 * qd) * 16;

  auto qk = [&](auto gic) __attribute__((always_inline)) {
    constexpr int gi = decltype(gic)::value;
    float d[U];
#pragma unroll
    for (int u = 0; u < U; ++u) d[u] = dot2(rg[gi][u][3], qp[3], dot2(rg[gi][u][2], qp[2], dot2(rg[gi][u][1], qp[1], dot2(rg[gi][u][0], qp[0], 0.f))));
    const bool o1 = c & 1, o2 = c & 2;
    const float x0 = (o1 ? d[1] : d[0]) + dpp<0xB1>(o1 ? d[0] : d[1]), x1 = (o1 ? d[3] : d[2]) + dpp<0xB1>(o1 ? d[2] : d[3]);
    float x = (o2 ? x1 : x0) + dpp<0x4E>(o2 ? x0 : x1);
    x += dpp<0x128>(x); x += dpp<0x124>(x);
    sp[gi / 4] = __builtin_bit_cast(float, __builtin_amdgcn_update_dpp(__builtin_bit_cast(int, sp[gi / 4]), __builtin_bit_cast(int, x), 0xE4, 0xf, 1 << (gi % 4), false));
    asm volatile("" : "+v"(sp[gi / 4]));
    SB();
  };
  auto pv = [&](auto gic) __attribute__((always_inline)) {
    constexpr int gi = decltype(gic)::value;
    const float p[4] = {dpp<0x150 + 4 * (gi % 4)>(sp[gi / 4]), dpp<0x151 + 4 * (gi % 4)>(sp[gi / 4]), dpp<0x152 + 4 * (gi % 4)>(sp[gi / 4]), dpp<0x153 + 4 * (gi % 4)>(sp[gi / 4])};
    float x[8];
#pragma unroll
    for (int k = 0; k < 4; ++k) { x[2 * k] = p[0] * lo(rg[GROUPS + gi][0][k]); x[2 * k + 1] = p[0] * hi(rg[GROUPS + gi][0][k]); }
#pragma unroll
    for (int u = 1; u < U; ++u) {
#pragma unroll
      for (int k = 0; k < 4; ++k) { x[2 * k] = fmaf(p[u], lo(rg[GROUPS + gi][u][k]), x[2 * k]); x[2 * k + 1] = fmaf(p[u], hi(rg[GROUPS + gi][u][k]), x[2 * k + 1]); }
    }
    const f32x2 y = fold8(x);
    if constexpr (gi < 8) bs[gi] = y; else bs[gi - 8] += y;
    if constexpr (gi >= 12) bs[gi - 12] += bs[gi - 8];
    if constexpr (gi >= 14) bs[gi - 14] += bs[gi - 12];
    asm volatile("" : "+v"(bs[gi >= 14 ? gi - 14 : gi >= 12 ? gi - 12 : gi % 8]));
    SB();
  };

  {
    unsigned tk;
    while ((tk = __builtin_amdgcn_readfirstlane(__hip_atomic_load(ldsC + 10, __ATOMIC_RELAXED, __HIP_MEMORY_SCOPE_WORKGROUP))) == 0u) __builtin_amdgcn_s_sleep(1);
    slot0 = __builtin_amdgcn_readfirstlane((__builtin_amdgcn_readfirstlane(tk) + (unsigned)T0_) / (unsigned)(TP_ / 8) + (unsigned)w);
  }
  const int kvo = ((slot0 & 1) << 13) | ((slot0 & 6) << 14) | ((s & 1) << 12) | ((s >> 1) << 14);
  unsigned due = __builtin_amdgcn_readfirstlane(slot0 * (unsigned)(TP_ / 8));

  auto point = [&](auto pc) __attribute__((always_inline)) {
    constexpr int p = decltype(pc)::value;
    int so = kvo ^ GOFF((slot0 >> 3) + p);
    asm volatile("" : "+s"(so));
    pace_wait(due, step_of(p));
    SB();
#pragma unroll
    for (int u = 0; u < U; ++u) rg[p][u] = __builtin_amdgcn_raw_buffer_load_b128(p < GROUPS ? rk : rv, lo16 + u * 1024, so, 2);
    SB();
    if constexpr (p == 4) qu = ld_unit(rs, qoff);
    if constexpr (p == 7) fu = ld_unit(rs, fuoff);
    if constexpr (p == 29) mu = ld_unit(rs, muoff);
#pragma unroll
    for (int i = won(p); i < won(p + 1); ++i) wor[i] = __builtin_amdgcn_raw_buffer_load_b64(rw, wvo, (32 * (i >> 3) + (i & 7)) * 1024, 0);
    if constexpr (p == 17) {
      e0 = ld_unit(rs, is_fin ? dpoff : OOB); e1 = ld_unit(rs, is_fin ? dpoff + 16 : OOB);
      xo = ((const GAS unsigned short*)px)[ocol];
    }
    SB();
    if constexpr (p == 0) {
    asm volatile("s_waitcnt lgkmcnt(0)" : "+s"(xq0), "+s"(xq1));
    xs0 = sel4(xq0, g); xs1 = sel4(xq1, g);
    float sm = row16_sum(lo(xst) + hi(xst)); sm = fold16(sm, sm) * (1.f / 64);
    const float d0 = lo(xst) - sm, d1 = hi(xst) - sm;
    float m2 = row16_sum(fmaf(d0, d0, d1 * d1)); m2 = fold16(m2, m2);
    if ((lane & 31) == 0) *(f32x2*)(ldsS + (lane >> 5) * 16 + 2 * w) = f32x2{sm, m2};
    float t[24];
#pragma unroll
    for (int k = 0; k < 4; ++k) {
      const float al = lo(sw0[k]), ah = hi(sw0[k]), bl = lo(sw1[k]), bh = hi(sw1[k]);
      t[2 * k] = fmaf(hi(xs0), bl, lo(xs0) * al); t[2 * k + 1] = fmaf(hi(xs0), bh, lo(xs0) * ah);
      t[8 + 2 * k] = fmaf(hi(xs1), bl, lo(xs1) * al); t[8 + 2 * k + 1] = fmaf(hi(xs1), bh, lo(xs1) * ah);
      t[16 + 2 * k] = al + bl; t[16 + 2 * k + 1] = ah + bh;
    }
#pragma unroll
    for (int k = 0; k < 8; ++k) t[k] = fold16(fold32(t[k], t[16 + k]), fold32(t[8 + k], t[16 + k]));
    *(f32x4*)(ldsY + w * 512 + g * 128 + 8 * c) = f32x4{t[0], t[1], t[2], t[3]};
    *(f32x4*)(ldsY + w * 512 + g * 128 + 8 * c + 4) = f32x4{t[4], t[5], t[6], t[7]};
    if (tid == 448) *(unsigned long long*)(ldsC + 8) = ep;
    asm volatile("" :: "v"(ep));
    if (last_wave(ldsC, lane)) {
      f32x4 st[8];
#pragma unroll
      for (int i = 0; i < 8; ++i) st[i] = *(const f32x4*)(ldsS + 4 * i);
      tagA = __builtin_amdgcn_readfirstlane(ldsC[8]) + 1u; tagB = __builtin_amdgcn_readfirstlane(ldsC[9]) + 0x9E3779B9u;
      const float mean0 = ((st[0][0] + st[0][2]) + (st[1][0] + st[1][2]) + (st[2][0] + st[2][2]) + (st[3][0] + st[3][2])) * 0.125f;
      const float mean1 = ((st[4][0] + st[4][2]) + (st[5][0] + st[5][2]) + (st[6][0] + st[6][2]) + (st[7][0] + st[7][2])) * 0.125f;
      float q0 = 0.f, q1 = 0.f, d0 = 0.f, d1 = 0.f;
#pragma unroll
      for (int i = 0; i < 4; ++i) {
        q0 += st[i][1] + st[i][3]; q1 += st[4 + i][1] + st[4 + i][3];
        const float a0 = st[i][0] - mean0, b0 = st[i][2] - mean0, a1 = st[4 + i][0] - mean1, b1 = st[4 + i][2] - mean1;
        d0 += fmaf(a0, a0, b0 * b0); d1 += fmaf(a1, a1, b1 * b1);
      }
      const float rstd0 = __builtin_amdgcn_rsqf(fmaf(64.f, d0, q0) * (1.f / D) + 1e-5f), rstd1 = __builtin_amdgcn_rsqf(fmaf(64.f, d1, q1) * (1.f / D) + 1e-5f);
      if (lane == 0) *(f32x4*)(ldsC + 12) = f32x4{mean0, rstd0, mean1, rstd1};
      last_wave(ldsC, lane);
      f32x2 z0 = {0.f, 0.f}, z1 = {0.f, 0.f}, cs = {0.f, 0.f};
#pragma unroll
      for (int ww = 0; ww < NWAVE; ++ww) {
        z0 += *(const f32x2*)(ldsY + ww * 512 + 2 * lane); z1 += *(const f32x2*)(ldsY + ww * 512 + 128 + 2 * lane); cs += *(const f32x2*)(ldsY + ww * 512 + 256 + 2 * lane); }
      st_unit(rs, s1off(h, r8, 0) + lo16, (z0[0] - mean0 * cs[0]) * rstd0, (z0[1] - mean0 * cs[1]) * rstd0, tagA, tagB);
      st_unit(rs, s1off(h, r8, 1) + lo16, (z1[0] - mean1 * cs[0]) * rstd1, (z1[1] - mean1 * cs[1]) * rstd1, tagA, tagB);
    }
  }
    if constexpr (p == 2) {
      wait_all(ldsC, NWAVE + 1);
      tagA = __builtin_amdgcn_readfirstlane(ldsC[8]) + 1u; tagB = __builtin_amdgcn_readfirstlane(ldsC[9]) + 0x9E3779B9u;
      asm volatile("" :: "v"(pf0), "v"(pf1), "v"(pfc));
      if (has_f1) {
        const f32x4 ln = *(const f32x4*)(ldsC + 12);
        const float n00 = (lo(xs0) - ln[0]) * ln[1], n01 = (hi(xs0) - ln[0]) * ln[1], n10 = (lo(xs1) - ln[2]) * ln[3], n11 = (hi(xs1) - ln[2]) * ln[3];
        float t[8];
#pragma unroll
        for (int k = 0; k < 4; ++k) {
          const float al = lo(ffw[0][k]), ah = hi(ffw[0][k]), bl = lo(ffw[1][k]), bh = hi(ffw[1][k]);
          t[2 * k] = fold32(fmaf(n01, bl, n00 * al), fmaf(n11, bl, n10 * al)); t[2 * k + 1] = fold32(fmaf(n01, bh, n00 * ah), fmaf(n11, bh, n10 * ah));
        }
        *(f32x4*)(ldsY + 4096 + w * 256 + (g >> 1) * 128 + 8 * c + 4 * (g & 1)) = f32x4{fold16(t[0], t[4]), fold16(t[1], t[5]), fold16(t[2], t[6]), fold16(t[3], t[7])};
        last_wave(ldsC + 2, lane);
      }
    }
    if constexpr (p == 5) {
      int gq = g; asm volatile("" : "+v"(gq));
      const int fbs = (gq >> 1) * 11 + (cid >> 2), hs = (fbs / 3) * 4 + fbs % 3;
      fuoff = has_f2 ? s1off(hs, w, 2 + (g & 1)) + (16 * (cid & 3) + c) * 16 : OOB;
      asm volatile("" : "+v"(fuoff));
    }
    if constexpr (p == 4) {
      if (has_f1 && w < 2) {
        wait_all(ldsC + 2);
        f32x2 z = {0.f, 0.f};
#pragma unroll
        for (int ww = 0; ww < NWAVE; ++ww) z += *(const f32x2*)(ldsY + 4096 + ww * 256 + 2 * tid);
        st_unit(rs, s1off(h, r8, 2 + w) + lo16, z[0], z[1], tagA, tagB);
      }
    }
    if constexpr (p == 6) {
      await_unit(qu, rs, qoff, tagA, tagB);
      *(f32x2*)(ldsQ + w * 128 + 2 * lane) = f32x2{__uint_as_float(qu[0]), __uint_as_float(qu[2])};
      last_wave(ldsC + 1, lane);
    }
    if constexpr (p == 8) {
      wait_all(ldsC + 1);
      f32x4 q0 = {0, 0, 0, 0}, q1 = {0, 0, 0, 0};
#pragma unroll
      for (int ww = 0; ww < NWAVE; ++ww) { q0 += *(const f32x4*)(ldsQ + ww * 128 + 8 * c); q1 += *(const f32x4*)(ldsQ + ww * 128 + 8 * c + 4); }
      qp[0] = pk_bf16(q0[0], q0[1]); qp[1] = pk_bf16(q0[2], q0[3]); qp[2] = pk_bf16(q1[0], q1[1]); qp[3] = pk_bf16(q1[2], q1[3]);
      qk(IC<0>{});
    }
    if constexpr (p == 9) { qk(IC<1>{}); qk(IC<2>{}); qk(IC<3>{}); }
    if constexpr (p == 10) { qk(IC<4>{}); qk(IC<5>{}); }
    if constexpr (p >= 10 && p < GROUPS + 4) qk(IC<p - 4>{});
    if constexpr (p == 10) {
      if (has_f2) {
        await_unit(fu, rs, fuoff, tagA, tagB);
        *(f32x2*)(ldsF + w * 128 + g * 32 + 2 * c) = f32x2{__uint_as_float(fu[0]), __uint_as_float(fu[2])};
        last_wave(ldsC + 5, lane);
      }
    }
    if constexpr (p == 12) {
      if (has_f2) {
        wait_all(ldsC + 5);
        float gs = 0.f, us = 0.f;
        const int hx = ((lane >> 2) & 1) * 32 + 4 * w + (lane & 3);
#pragma unroll
        for (int ww = 0; ww < NWAVE; ++ww) { gs += ldsF[ww * 128 + hx]; us += ldsF[ww * 128 + 64 + hx]; }
        const float hv = gelu_tanh(gs) * us;
        float dd[16];
#pragma unroll
        for (int k = 0; k < 16; ++k) dd[k] = 0.f;
#pragma unroll
        for (int r = 0; r < 4; ++r) {
          const float h0 = __builtin_bit_cast(float, __builtin_amdgcn_readlane(__builtin_bit_cast(int, hv), r));
          const float h1 = __builtin_bit_cast(float, __builtin_amdgcn_readlane(__builtin_bit_cast(int, hv), 4 + r));
#pragma unroll
          for (int k = 0; k < 4; ++k) {
            const float wl = lo(ffw[r][k]), wh = hi(ffw[r][k]);
            dd[2 * k] = fmaf(h0, wl, dd[2 * k]); dd[2 * k + 1] = fmaf(h0, wh, dd[2 * k + 1]);
            dd[8 + 2 * k] = fmaf(h1, wl, dd[8 + 2 * k]); dd[8 + 2 * k + 1] = fmaf(h1, wh, dd[8 + 2 * k + 1]);
          }
        }
        *(f32x4*)(ldsY + w * 1024 + 8 * lane) = f32x4{dd[0], dd[1], dd[2], dd[3]};
        *(f32x4*)(ldsY + w * 1024 + 8 * lane + 4) = f32x4{dd[4], dd[5], dd[6], dd[7]};
        *(f32x4*)(ldsY + w * 1024 + 512 + 8 * lane) = f32x4{dd[8], dd[9], dd[10], dd[11]};
        *(f32x4*)(ldsY + w * 1024 + 512 + 8 * lane + 4) = f32x4{dd[12], dd[13], dd[14], dd[15]};
        last_wave(ldsC + 3, lane);
      }
    }
    if constexpr (p == 14) {
      if (has_f2) {
        wait_all(ldsC + 3);
        f32x2 dv = {0.f, 0.f};
#pragma unroll
        for (int ww = 0; ww < NWAVE; ++ww) dv += *(const f32x2*)(ldsY + ww * 1024 + (tid & 256) * 2 + ucol(tid & 255));
        st_unit(rs, WS_DP + (cid * 512 + tid) * 16, dv[0], dv[1], tagA, tagB);
      }
    }
    if constexpr (p == 23) {
      if (is_fin) {
        await_unit(e0, rs, dpoff, tagA, tagB);
        await_unit(e1, rs, dpoff + 16, tagA, tagB);
        const bool hd = gp < NFFN;
        const float r = fold4(hd ? __uint_as_float(e0[0]) : 0.f, hd ? __uint_as_float(e0[2]) : 0.f, hd ? __uint_as_float(e1[0]) : 0.f, hd ? __uint_as_float(e1[2]) : 0.f);
        if (c < 4) ldsF[w * 16 + 4 * c + g] = r;
        last_wave(ldsC + 2, lane);
      }
    }
    if constexpr (p == GROUPS + 3) {
      float x = fmaxf(fmaxf(sp[0], sp[1]), fmaxf(sp[2], sp[3]));
      x = fmaxf(x, dpp<0x128>(x)); x = fmaxf(x, dpp<0x124>(x)); x = fmaxf(x, dpp<0x122>(x)); x = fmaxf(x, dpp<0x121>(x));
      mw = xrow_max(x) * C2;
#pragma unroll
      for (int i = 0; i < 4; ++i) sp[i] = __builtin_amdgcn_exp2f(fmaf(sp[i], C2, -mw));
      const float lw = xrow_sum(row16_sum((sp[0] + sp[2]) + (sp[1] + sp[3])));
      if (lane == 0) { ldsM[slot0 & 7] = mw; ldsM[8 + (slot0 & 7)] = lw; }
      last_wave(ldsC + 4, lane);
    }
    if constexpr (p == 21) {
      if (w == 0) {
        wait_all(ldsC + 4);
        const float mi = ldsM[lane & 7], li = ldsM[8 + (lane & 7)];
        float M = fmaxf(mi, dpp<0x124>(mi)); M = fmaxf(M, dpp<0x122>(M)); M = fmaxf(M, dpp<0x121>(M));
        float L = __builtin_amdgcn_exp2f(mi - M) * li; L += dpp<0x124>(L); L += dpp<0x122>(L); L += dpp<0x121>(L);
        if (lane == 0) st_unit(rs, WS_ML + pid * 16, M, L, tagA, tagB);
      }
    }
    if constexpr (p == 26) {
      if (is_fin && w == 0) {
        wait_all(ldsC + 2);
        const float dv = ldsF[c] + ldsF[16 + c] + ldsF[32 + c];
        gte = dv * __builtin_amdgcn_rcpf(1.f + __expf(-dv));
        asm volatile("" : "+v"(gte));
      }
    }
    if constexpr (p == 31) {
      await_unit(mu, rs, muoff, tagA, tagB);
      const float mi = __uint_as_float(mu[0]);
      float M = fmaxf(mi, dpp<0xB1>(mi)); M = fmaxf(M, dpp<0x4E>(M));
      float L = __builtin_amdgcn_exp2f(mi - M) * __uint_as_float(mu[2]); L += dpp<0xB1>(L); L += dpp<0x4E>(L);
      mw = __builtin_amdgcn_exp2f(mw - M) * __builtin_amdgcn_rcpf(L);
      ysto = (g & 1) ? OOB : WS_Y + (pid * 256 + 32 * w + 16 * (g >> 1) + c) * 16;
      yoff = WS_Y + ((bc * 128 + gp) * 256 + cu0 + 2 * qd) * 16;
      asm volatile("" : "+v"(mw), "+v"(ysto), "+v"(yoff));
    }
    if constexpr (p >= 20 && p < 28) pv(IC<p - 20>{});
    if constexpr (p == 28 || p == 29) { pv(IC<2 * p - 48>{}); pv(IC<2 * p - 47>{}); }
    if constexpr (p == 30) pv(IC<12>{});
    if constexpr (p == 31) { pv(IC<13>{}); pv(IC<14>{}); pv(IC<15>{}); }
    if constexpr (p == 30) {
#pragma unroll
      for (int i = 0; i < 16; ++i) {
        const v2u r0 = wor[2 * i], r1 = wor[2 * i + 1];
        wb[0][i >> 2][i & 3] = __builtin_amdgcn_perm(r1[0], r0[0], 0x05040100u); wb[1][i >> 2][i & 3] = __builtin_amdgcn_perm(r1[0], r0[0], 0x07060302u);
        wb[2][i >> 2][i & 3] = __builtin_amdgcn_perm(r1[1], r0[1], 0x05040100u); wb[3][i >> 2][i & 3] = __builtin_amdgcn_perm(r1[1], r0[1], 0x07060302u);
      }
#pragma unroll
      for (int i = 0; i < 4; ++i) asm volatile("" : "+v"(wb[i][0]), "+v"(wb[i][1]), "+v"(wb[i][2]), "+v"(wb[i][3]));
    }
    SB();
  };
  static_for<2 * GROUPS>(point);

  {
    const f32x2 o2 = (bs[0] + bs[1]) * mw;
    const unsigned oh = pk_bf16(o2[0], o2[1]), ol = pk_bf16(o2[0] - lo(oh), o2[1] - hi(oh));
    const int ar = (slot0 & 7) * 68 + 4 * c + g;
    ldsA[ar] = oh; ldsA[8 * 68 + ar] = ol;
  }
  __syncthreads();
  {
    v4u af[4];
#pragma unroll
    for (int ks = 0; ks < 4; ++ks) af[ks] = *(const v4u*)(ldsA + c * 68 + 16 * ks + 4 * g);
    SB();
    f32x4 d[4];
#pragma unroll
    for (int t = 0; t < 4; ++t) d[t] = f32x4{0.f, 0.f, 0.f, 0.f};
#pragma unroll
    for (int ks = 0; ks < 4; ++ks)
#pragma unroll
      for (int t = 0; t < 4; ++t) d[t] = __builtin_amdgcn_mfma_f32_16x16x32_bf16(__builtin_bit_cast(bf16x8, af[ks]), __builtin_bit_cast(bf16x8, wb[t][ks]), d[t], 0, 0, 0);
    float y[4];
#pragma unroll
    for (int t = 0; t < 4; ++t) y[t] = (d[t][0] + d[t][1]) + (d[t][2] + d[t][3]);
    float u0 = fold32(y[0], y[2]), u1 = fold32(y[1], y[3]);
    u0 = fold16(u0, u0); u1 = fold16(u1, u1);
    st_unit(rs, ysto, u0, u1, tagA, tagB);
  }

  if (is_fin) {
    __builtin_amdgcn_s_barrier();
    __builtin_amdgcn_s_sleep(13);
    v4u y0, y1;
    bool ok = false;
#pragma unroll 1
    for (unsigned spin = 0; spin < SPIN_CAP; ++spin) {
      asm volatile("" ::: "memory");
      if (!ok) {
        y0 = ld_unit(rs, yoff); y1 = ld_unit(rs, yoff + 16);
        ok = (((y0[1] ^ tagA) | (y1[1] ^ tagA)) | ((y0[3] ^ tagB) | (y1[3] ^ tagB))) == 0u;
      }
      if (__all(ok)) break;
      __builtin_amdgcn_s_sleep(1);
    }
    const float r = fold4(__uint_as_float(y0[0]), __uint_as_float(y0[2]), __uint_as_float(y1[0]), __uint_as_float(y1[2]));
    if (c < 4) ldsF[128 + w * 16 + 4 * c + g] = r;
    __syncthreads();
    if (tid < 16) {
      float sv = 0.f;
#pragma unroll
      for (int ww = 0; ww < NWAVE; ++ww) sv += ldsF[128 + ww * 16 + tid];
      const float o = __uint_as_float((unsigned)xo << 16) + sv * gte;
      const __bf16 ob = (__bf16)o;
      ((GAS unsigned short*)KAP(8))[ocol] = __builtin_bit_cast(unsigned short, ob);
      if (cid == 0 && tid == 0)
        __hip_atomic_store((GAS unsigned long long*)(pws + WS_EPOCH), ((unsigned long long)tagB << 32) | tagA, __ATOMIC_RELAXED, __HIP_MEMORY_SCOPE_AGENT);
    }
  }
  asm volatile(".Lk_all_end:");
}

extern "C" void kernel_launch(void* const* d_in, const int* in_sizes, int n_in, void* d_out, int out_size, void* d_ws, size_t ws_size,
                              hipStream_t stream) {
  Args a;
  a.x = (const unsigned short*)d_in[0];
  a.k = (const unsigned short*)d_in[1];
  a.v = (const unsigned short*)d_in[2];
  a.wq = (const unsigned short*)d_in[3];
  a.wo = (const unsigned short*)d_in[4];
  a.wg = (const unsigned short*)d_in[6];
  a.wu = (const unsigned short*)d_in[7];
  a.wd = (const unsigned short*)d_in[8];
  a.out = (unsigned short*)d_out;
  a.ws = (unsigned char*)d_ws;
  hipLaunchKernelGGL(k_all, dim3(256), dim3(NTHR), 0, stream, a);
}
```

```cpp
#include <hip/hip_runtime.h>
#include <hip/hip_bf16.h>
#include <stdint.h>

typedef unsigned v4u __attribute__((ext_vector_type(4)));
typedef float f32x4 __attribute__((ext_vector_type(4)));
typedef float f32x2 __attribute__((ext_vector_type(2)));
typedef __bf16 bf16x2 __attribute__((ext_vector_type(2)));
typedef __bf16 bf16x8 __attribute__((ext_vector_type(8)));
typedef unsigned v16u __attribute__((ext_vector_type(16)));
typedef unsigned v2u __attribute__((ext_vector_type(2)));
#define GAS __attribute__((address_space(1)))
#define CAS __attribute__((address_space(4)))

constexpr int D = 512, H = 32, SK = 8192, DH = 128, F = 1376;
constexpr int NSPLIT = 4, KEYS_PER_WG = SK / NSPLIT;
constexpr int NWAVE = 8, NTHR = NWAVE * 64;
constexpr int NFFN = F / 32;
constexpr int U = 4;
constexpr int GROUPS = KEYS_PER_WG / NWAVE / (4 * U);
#define PWO_ 1
constexpr int won(int p) { return p <= PWO_ ? 0 : p >= 28 ? 32 : (p - PWO_) * 32 / (28 - PWO_); }
#define MSA_ 16
#define YSA_ 16
#define EX1_ 60
#define EX2_ 0
#define STEP(pt) (((unsigned)TPX & ~7u) + (unsigned)(EXT(pt)))
#define EXT(pt) ((pt) < 9 ? EX1_ : (pt) >= 14 && (pt) < 18 ? EX2_ : 0)
#define PI0_ 0
#define RSL_ 1
#define GORD_ 1
#define PEL_ 31
#define PDP_ 28
#define PDLY_ 11
#define LATE_PRIO_ 400
#define TP_ 1728
#define T0_ 2500

constexpr int WS_EPOCH = 0;
constexpr int WS_ML = 4096;
constexpr int WS_Y = WS_ML + 256 * 16;
constexpr int WS_DP = WS_Y + 256 * 256 * 16;
constexpr int WS_S1 = WS_DP + NFFN * 256 * 16;
constexpr int WS_END = WS_S1 + 32 * 8 * 2 * 64 * 16;
constexpr int OOB = 0x7ffffff0;
constexpr unsigned SPIN_CAP = 400000;

#define SB() __builtin_amdgcn_sched_barrier(0)

__device__ __forceinline__ float dot2(unsigned k, unsigned q, float acc) {
  return __builtin_amdgcn_fdot2_f32_bf16(__builtin_bit_cast(bf16x2, k), __builtin_bit_cast(bf16x2, q), acc, false);
}
template <int CTRL> __device__ __forceinline__ float dpp(float x) {
  return __builtin_bit_cast(float, __builtin_amdgcn_mov_dpp(__builtin_bit_cast(int, x), CTRL, 0xf, 0xf, true));
}
__device__ __forceinline__ float row16_sum(float x) {
  x += dpp<0x128>(x);
  x += dpp<0x124>(x);
  x += dpp<0x122>(x);
  x += dpp<0x121>(x);
  return x;
}
__device__ __forceinline__ float fold32(float a, float b) {
  auto t = __builtin_amdgcn_permlane32_swap(__float_as_uint(a), __float_as_uint(b), false, false);
  return __uint_as_float(t[0]) + __uint_as_float(t[1]);
}
__device__ __forceinline__ float fold16(float a, float b) {
  auto t = __builtin_amdgcn_permlane16_swap(__float_as_uint(a), __float_as_uint(b), false, false);
  return __uint_as_float(t[0]) + __uint_as_float(t[1]);
}
__device__ __forceinline__ float xrow_sum(float x) { x = fold16(x, x); return fold32(x, x); }
__device__ __forceinline__ float xrow_max(float x) {
  auto s = __builtin_amdgcn_permlane16_swap(__float_as_uint(x), __float_as_uint(x), false, false);
  x = fmaxf(__uint_as_float(s[0]), __uint_as_float(s[1]));
  auto t = __builtin_amdgcn_permlane32_swap(__float_as_uint(x), __float_as_uint(x), false, false);
  return fmaxf(__uint_as_float(t[0]), __uint_as_float(t[1]));
}
__device__ __forceinline__ f32x2 fold8(const float (&v)[8]) {
  const float u0 = fold32(v[0], v[4]), u1 = fold32(v[1], v[5]), u2 = fold32(v[2], v[6]), u3 = fold32(v[3], v[7]);
  return f32x2{fold16(u0, u2), fold16(u1, u3)};
}
__device__ __forceinline__ float fold4(float v0, float v1, float v2, float v3) {
  float r = fold16(fold32(v0, v2), fold32(v1, v3));
  r += dpp<0x124>(r); r += dpp<0x128>(r);
  return r;
}
__device__ __forceinline__ float lo(unsigned u) { return __uint_as_float(u << 16); }
__device__ __forceinline__ float hi(unsigned u) { return __uint_as_float(u & 0xffff0000u); }
__device__ __forceinline__ unsigned pk_bf16(float a, float b) {
  bf16x2 r = {(__bf16)a, (__bf16)b};
  return __builtin_bit_cast(unsigned, r);
}
__device__ __forceinline__ unsigned sel4(v4u v, int g) { const unsigned a = (g & 1) ? v[1] : v[0], b = (g & 1) ? v[3] : v[2]; return (g & 2) ? b : a; }
__device__ __forceinline__ int ucol(int u) { return 64 * (u >> 5) + 4 * (u & 15) + 2 * ((u >> 4) & 1); }
__device__ __forceinline__ int s1off(int hg, int r, int k) { return WS_S1 + (hg * 8 + ((r + hg) & 7)) * 2048 + ((k ^ (hg >> 3)) & 1) * 1024; }
__device__ __forceinline__ float gelu_tanh(float x) {
  const float u = 0.7978845608028654f * (x + 0.044715f * x * x * x);
  const float e = __expf(2.f * u);
  const float t = 1.f - 2.f * __builtin_amdgcn_rcpf(e + 1.f);
  return 0.5f * x * (1.f + t);
}
__device__ __forceinline__ void pace_wait(unsigned& due16, unsigned step16) {
  asm volatile("1:\n\ts_memrealtime s[90:91]\n\ts_waitcnt lgkmcnt(0)\n\ts_lshl_b32 s90, s90, 4\n\ts_sub_u32 s91, s90, %0\n\ts_cmp_ge_i32 s91, 0\n\ts_cbranch_scc1 2f\n\ts_sleep 2\n\ts_branch 1b\n"
               "2:\n\ts_add_u32 %0, %0, %1\n\ts_setprio 0\n\ts_cmp_lt_i32 s91, %2\n\ts_cbranch_scc1 3f\n\ts_setprio 2\n3:"
               : "+s"(due16) : "s"(step16), "i"(LATE_PRIO_) : "s90", "s91", "scc");
}
__device__ __forceinline__ unsigned now16() { return (unsigned)__builtin_amdgcn_s_memrealtime() << 4; }
#define MARK(i)
#define TPX TP_
#define T0X T0_
#define GORDX GORD_
#define PACE(pt) pace_wait(due, STEP(pt))
__device__ __forceinline__ unsigned next_tag(unsigned e) { return ((e + 1u) & 1023u) | ((e + 1024u) & (1023u << 10)) | ((e + (1u << 20)) & 0xfff00000u); }
constexpr int AUX_SC1 = 16;
template <int AUX = MSA_> __device__ __forceinline__ void st_unit(__amdgpu_buffer_rsrc_t rs, int off, float v0, float v1, unsigned ta, unsigned tb) {
  __builtin_amdgcn_raw_buffer_store_b128(v4u{__float_as_uint(v0), ta, __float_as_uint(v1), tb}, rs, off, 0, AUX);
}
__device__ __forceinline__ v4u ld_unit(__amdgpu_buffer_rsrc_t rs, int off) { return __builtin_amdgcn_raw_buffer_load_b128(rs, off, 0, AUX_SC1); }
__device__ __forceinline__ unsigned enc22(float v, unsigned t) { return ((__float_as_uint(v) + 0x200u) & 0xfffffc00u) | t; }
__device__ __forceinline__ void st_dense(__amdgpu_buffer_rsrc_t rs, int off, f32x4 v, unsigned ta, unsigned tb) {
  __builtin_amdgcn_raw_buffer_store_b128(v4u{enc22(v[0], ta & 1023u), enc22(v[1], (ta >> 10) & 1023u), enc22(v[2], (ta >> 20) & 1023u), enc22(v[3], tb & 1023u)}, rs, off, 0, AUX_SC1);
}
__device__ __forceinline__ bool ok_dense(v4u u, unsigned ta, unsigned tb) { return ((((u[0] ^ ta) | (u[1] ^ (ta >> 10))) | ((u[2] ^ (ta >> 20)) | (u[3] ^ tb))) & 1023u) == 0u; }
__device__ __forceinline__ f32x4 await_dense(v4u u, __amdgpu_buffer_rsrc_t rs, int off, unsigned ta, unsigned tb) {
  if (!__all(ok_dense(u, ta, tb))) {
#pragma unroll 1
    for (unsigned spin = 0; spin < SPIN_CAP; ++spin) {
      __builtin_amdgcn_s_sleep(1);
      asm volatile("" ::: "memory");
      u = ld_unit(rs, off);
      if (__all(ok_dense(u, ta, tb))) break;
    }
  }
  return __builtin_bit_cast(f32x4, u & 0xfffffc00u);
}
__device__ __forceinline__ void await_unit(v4u& u, __amdgpu_buffer_rsrc_t rs, int off, unsigned ta, unsigned tb) {
  if (!__all(((u[1] ^ ta) | (u[3] ^ tb)) == 0u)) {
#pragma unroll 1
    for (unsigned spin = 0; spin < SPIN_CAP; ++spin) {
      __builtin_amdgcn_s_sleep(1);
      asm volatile("" ::: "memory");
      u = ld_unit(rs, off);
      if (__all(((u[1] ^ ta) | (u[3] ^ tb)) == 0u)) break;
    }
  }
}

__device__ __forceinline__ bool last_wave(unsigned* cnt, int lane) {
  asm volatile("s_waitcnt lgkmcnt(0)" ::: "memory");
  unsigned old = 0;
  if (lane == 0) old = __hip_atomic_fetch_add(cnt, 1u, __ATOMIC_RELAXED, __HIP_MEMORY_SCOPE_WORKGROUP);
  old = __builtin_amdgcn_readfirstlane(old);
  asm volatile("" ::: "memory");
  return old == NWAVE - 1;
}

__device__ __forceinline__ void wait_all(unsigned* cnt, unsigned n = NWAVE) {
  while (__builtin_amdgcn_readfirstlane(__hip_atomic_load(cnt, __ATOMIC_RELAXED, __HIP_MEMORY_SCOPE_WORKGROUP)) < n) __builtin_amdgcn_s_sleep(1);
  asm volatile("" ::: "memory");
}
template <int I> struct IC { static constexpr int value = I; };
template <int N, int I = 0, typename Fn> __device__ __forceinline__ void static_for(Fn& fn) { if constexpr (I < N) { fn(IC<I>{}); static_for<N, I + 1>(fn); } }

struct Args {
  const unsigned short* x; const unsigned short* wq;
  const unsigned short* k; const unsigned short* v; const unsigned short* wo; const unsigned short* wg;
  const unsigned short* wu; const unsigned short* wd; unsigned short* out; unsigned char* ws;
};

__global__ void __launch_bounds__(NTHR, 2) k_all(Args a) {
  __shared__ __attribute__((aligned(16))) float lds[8 * 1024 + 16 * 68 + 16 + 8 * 128 + 8 * 128 + 32 + 16];
  float* ldsY = lds;
  unsigned* ldsA = (unsigned*)(lds + 8 * 1024);
  float* ldsM = lds + 8 * 1024 + 16 * 68;
  float* ldsF = ldsM + 16;
  float* ldsQ = ldsF + 8 * 128;
  float* ldsS = ldsQ + 8 * 128;
  unsigned* ldsC = (unsigned*)(ldsS + 32);
  MARK(32);
  const int bid = blockIdx.x;
  const int xcd = bid & 7, j = bid >> 3;
  const int tid = threadIdx.x, lane = tid & 63, w = __builtin_amdgcn_readfirstlane(tid >> 6), lo16 = lane * 16;
  v16u ka; v4u kb; unsigned long long tptr, pc0; unsigned csz;
  asm volatile("s_load_dwordx16 %0, %5, 0x0\n\ts_load_dwordx4 %1, %5, 0x40\n\ts_load_dwordx2 %2, %5, %6 offset:0x10\n\t"
               "s_getpc_b64 %3\n.Lk_all_pc0:\n\ts_mov_b32 %4, (.Lk_all_end-.Lk_all_pc0)&0xfffffff0"
               : "=&s"(ka), "=&s"(kb), "=&s"(tptr), "=&s"(pc0), "=&s"(csz) : "s"(__builtin_amdgcn_kernarg_segment_ptr()), "s"(8 * w));
#define RSRC(p_, n_) __builtin_amdgcn_make_buffer_rsrc((void*)(p_), 0, (n_), 0x00020000)
  const v4u pfc = __builtin_amdgcn_raw_buffer_load_b128(RSRC(pc0 & ~127ull, w == 4 ? csz : 0u), lo16, j * 1024, 0);
  ldsC[tid & 15] = 0u;
  SB();
  const int h = xcd * 4 + (j >> 3), b = (j >> 2) & 1, s = j & 3;
  const int g = lane >> 4, c = lane & 15;
  const int r8 = j & 7;
  int qwo = (2 * g * H * DH + 8 * c) * 2, xso = (lane >> 5) * D * 2 + (lane & 31) * 4, qso = ((64 * r8 + 8 * w) * H * DH + h * DH) * 2;
  unsigned tof = ((unsigned)(b * H + h) * ((0x03u >> w) & 1u) << 21) + ((unsigned)h * ((0x04u >> w) & 1u) << 17);
  asm volatile("" : "+v"(qwo), "+v"(xso), "+v"(tof));
  asm volatile("s_waitcnt lgkmcnt(0)\n\ts_barrier" : "+s"(ka), "+s"(kb), "+s"(tptr), "+s"(qso) :: "memory");
#define KAP(i) ((i) < 8 ? (unsigned long long)ka[2 * (i) % 16] | ((unsigned long long)ka[(2 * (i) + 1) % 16] << 32) : (unsigned long long)kb[2 * (i) % 4] | ((unsigned long long)kb[(2 * (i) + 1) % 4] << 32))
  const unsigned long long px = KAP(0), pws = KAP(9);
  const unsigned long long ep = __hip_atomic_load((const GAS unsigned long long*)(tptr + tof), __ATOMIC_RELAXED, __HIP_MEMORY_SCOPE_AGENT);
  SB();
  const unsigned xst = __builtin_amdgcn_raw_buffer_load_b32(RSRC(px, 2 * D * 2), xso, 128 * w, 0);
  SB();
  const __amdgpu_buffer_rsrc_t rq = RSRC(KAP(1), D * H * DH * 2);
  const v4u sw0 = __builtin_amdgcn_raw_buffer_load_b128(rq, qwo, qso, 2), sw1 = __builtin_amdgcn_raw_buffer_load_b128(rq, qwo, qso + H * DH * 2, 2);
  v4u xq0, xq1;
  asm volatile("s_load_dwordx4 %0, %2, 0x0\n\ts_load_dwordx4 %1, %2, 0x400" : "=&s"(xq0), "=&s"(xq1) : "s"(px + (64 * r8 + 8 * w) * 2));
  SB();
  if (w == 0) {
    asm volatile("" :: "v"(ep));
    __hip_atomic_store(ldsC + 10, now16() | 1u, __ATOMIC_RELAXED, __HIP_MEMORY_SCOPE_WORKGROUP);
    MARK(40);
  }
  SB();
  const __amdgpu_buffer_rsrc_t rw = RSRC(KAP(4) + (size_t)h * DH * D * 2, DH * D * 2);
  const v4u pf0 = __builtin_amdgcn_raw_buffer_load_b128(rw, lo16, (16 * r8 + 2 * w) * 1024, 0), pf1 = __builtin_amdgcn_raw_buffer_load_b128(rw, lo16 + 1024, (16 * r8 + 2 * w) * 1024, 0);
  SB();
  const int fb = xcd * 3 + (j >> 3);
  const bool has_f1 = (j < 24) && (fb < 22);
  const bool is_fin = j >= 24;
  const int cid = xcd * 8 + (j - 24);
  const bool has_f2 = is_fin && cid < NFFN;
  const int row0 = 64 * r8 + 8 * w + 2 * g;
  const int fcol = 128 * (fb < 11 ? fb : fb - 11) + 8 * c;
  int fx1 = (row0 * F + fcol) * 2, fx2 = (32 * cid + 4 * w) * D * 2 + lo16;
  asm volatile("" : "+v"(fx1), "+v"(fx2));
  const int fxo = has_f1 ? (fcol < F ? fx1 : OOB - 4 * F * 2) : fx2, fxo2 = has_f1 ? OOB - 4096 : fx2, fst = has_f1 ? F * 2 : D * 2;
  unsigned f_lo = fb < 11 ? ka[10] : ka[12], f_hi = fb < 11 ? ka[11] : ka[13];
  asm volatile("" : "+s"(f_lo), "+s"(f_hi));
  const __amdgpu_buffer_rsrc_t rf = RSRC((unsigned long long)(has_f1 ? f_lo : ka[14]) | ((unsigned long long)(has_f1 ? f_hi : ka[15]) << 32), (has_f1 || has_f2) ? D * F * 2 : 0);
  MARK(41);
  v4u ffw[4];
  ffw[0] = __builtin_amdgcn_raw_buffer_load_b128(rf, fxo, 0, 2);
  ffw[1] = __builtin_amdgcn_raw_buffer_load_b128(rf, fxo, fst, 2);
  ffw[2] = __builtin_amdgcn_raw_buffer_load_b128(rf, fxo2 + 2048, 0, 2);
  ffw[3] = __builtin_amdgcn_raw_buffer_load_b128(rf, fxo2 + 3072, 0, 2);
  SB();
  MARK(42);
  const __amdgpu_buffer_rsrc_t rs = RSRC(pws, WS_END);

  const __amdgpu_buffer_rsrc_t rk = RSRC(KAP(2) + (size_t)(b * H + h) * SK * DH * 2, SK * DH * 2);
  const __amdgpu_buffer_rsrc_t rv = RSRC(KAP(3) + (size_t)(b * H + h) * SK * DH * 2, SK * DH * 2);
#define GOFF1(gi_) (((gi_) & 1) * ((1 << 20) | (1 << 12)) | ((gi_) & 2) * ((1 << 18) | (1 << 13)) | (((gi_) & 4) << 16) | (((gi_) & 8) << 14))
#define GOFF0(gi_) ((((gi_) & 15) << 17) | (((gi_) & 4) << 12) | (((gi_) & 8) << 9))
#define GOFF(gi_) (GORDX ? GOFF1(gi_) : GOFF0(gi_))
  v4u rg[2 * GROUPS][U];

  constexpr float C2 = 0.08838834764831845f * 1.4426950408889634f;
  float sp[4] = {0.f, 0.f, 0.f, 0.f};
  float mw = 0.f;
  f32x2 bs[8];
  unsigned tagA, tagB, qp[4], slot0;
  v2u wor[32]; v4u wb[4][4], fu, qu, mu, e0; unsigned short xo;
  unsigned xs0, xs1;
  int fuoff, ysto, yoff;
  const int wvo = g * 8192 + w * 128 + c * 8;
  const int qoff = s1off(h, w, 0) + (b * 32 + (lane & 31)) * 16;
  const int pid = (b * H + h) * NSPLIT + s, muoff = WS_ML + ((pid & ~3) + (lane & 3)) * 16;
  const int bc = (cid >> 5) & 1, cu0 = (cid & 31) * 8;
  const int gp = tid >> 2, qd = tid & 3, ocol = bc * D + ucol(cu0 + (c >> 1)) + (c & 1);
  const int dpoff = (is_fin && w < 3) ? WS_DP + ((gp < NFFN ? gp : NFFN - 1) * 256 + bc * 128 + cu0 / 2 + qd) * 16 : OOB;

  auto qk = [&](auto gic) __attribute__((always_inline)) {
    constexpr int gi = decltype(gic)::value;
    float d[U];
#pragma unroll
    for (int u = 0; u < U; ++u) d[u] = dot2(rg[gi][u][3], qp[3], dot2(rg[gi][u][2], qp[2], dot2(rg[gi][u][1], qp[1], dot2(rg[gi][u][0], qp[0], 0.f))));
    const bool o1 = c & 1, o2 = c & 2;
    const float x0 = (o1 ? d[1] : d[0]) + dpp<0xB1>(o1 ? d[0] : d[1]), x1 = (o1 ? d[3] : d[2]) + dpp<0xB1>(o1 ? d[2] : d[3]);
    float x = (o2 ? x1 : x0) + dpp<0x4E>(o2 ? x0 : x1);
    x += dpp<0x128>(x); x += dpp<0x124>(x);
    sp[gi / 4] = __builtin_bit_cast(float, __builtin_amdgcn_update_dpp(__builtin_bit_cast(int, sp[gi / 4]), __builtin_bit_cast(int, x), 0xE4, 0xf, 1 << (gi % 4), false));
    asm volatile("" : "+v"(sp[gi / 4]));
    SB();
  };
  auto pv = [&](auto gic) __attribute__((always_inline)) {
    constexpr int gi = decltype(gic)::value;
    const float p[4] = {dpp<0x150 + 4 * (gi % 4)>(sp[gi / 4]), dpp<0x151 + 4 * (gi % 4)>(sp[gi / 4]), dpp<0x152 + 4 * (gi % 4)>(sp[gi / 4]), dpp<0x153 + 4 * (gi % 4)>(sp[gi / 4])};
    float x[8];
#pragma unroll
    for (int k = 0; k < 4; ++k) { x[2 * k] = p[0] * lo(rg[GROUPS + gi][0][k]); x[2 * k + 1] = p[0] * hi(rg[GROUPS + gi][0][k]); }
#pragma unroll
    for (int u = 1; u < U; ++u) {
#pragma unroll
      for (int k = 0; k < 4; ++k) { x[2 * k] = fmaf(p[u], lo(rg[GROUPS + gi][u][k]), x[2 * k]); x[2 * k + 1] = fmaf(p[u], hi(rg[GROUPS + gi][u][k]), x[2 * k + 1]); }
    }
    const f32x2 y = fold8(x);
    if constexpr (gi < 8) bs[gi] = y; else bs[gi - 8] += y;
    if constexpr (gi >= 12) bs[gi - 12] += bs[gi - 8];
    if constexpr (gi >= 14) bs[gi - 14] += bs[gi - 12];
    asm volatile("" : "+v"(bs[gi >= 14 ? gi - 14 : gi >= 12 ? gi - 12 : gi % 8]));
    SB();
  };

  {
    unsigned tk;
    while ((tk = __builtin_amdgcn_readfirstlane(__hip_atomic_load(ldsC + 10, __ATOMIC_RELAXED, __HIP_MEMORY_SCOPE_WORKGROUP))) == 0u) __builtin_amdgcn_s_sleep(1);
    slot0 = __builtin_amdgcn_readfirstlane((__builtin_amdgcn_readfirstlane(tk) + (unsigned)T0X) / (unsigned)(TPX / 8) + (unsigned)w);
  }
  const int kvo = ((slot0 & 1) << 13) | ((slot0 & 6) << 14) | ((s & 1) << 12) | ((s >> 1) << 14);
  unsigned due = __builtin_amdgcn_readfirstlane(slot0 * (unsigned)(TPX / 8));

  auto point = [&](auto pc) __attribute__((always_inline)) {
    constexpr int p = decltype(pc)::value;
    int so = kvo ^ GOFF((slot0 >> 3) + p);
    asm volatile("" : "+s"(so));
    PACE(p);
    SB();
#pragma unroll
    for (int u = 0; u < U; ++u) rg[p][u] = __builtin_amdgcn_raw_buffer_load_b128(p < GROUPS ? rk : rv, lo16 + u * 1024, so, 2);
    SB();
    if constexpr (p == 4) qu = ld_unit(rs, qoff);
    if constexpr (p == 7) fu = ld_unit(rs, fuoff);
    if constexpr (p == 29) mu = ld_unit(rs, muoff);
#pragma unroll
    for (int i = won(p); i < won(p + 1); ++i) wor[i] = __builtin_amdgcn_raw_buffer_load_b64(rw, wvo, (32 * (i >> 3) + (i & 7)) * 1024, 0);
    if constexpr (p == PEL_) {
      e0 = ld_unit(rs, dpoff);
      xo = ((const GAS unsigned short*)px)[ocol];
    }
    SB();
    if constexpr (p == PI0_) {
    asm volatile("s_waitcnt lgkmcnt(0)" : "+s"(xq0), "+s"(xq1));
    xs0 = sel4(xq0, g); xs1 = sel4(xq1, g);
    float sm = row16_sum(lo(xst) + hi(xst)); sm = fold16(sm, sm) * (1.f / 64);
    const float d0 = lo(xst) - sm, d1 = hi(xst) - sm;
    float m2 = row16_sum(fmaf(d0, d0, d1 * d1)); m2 = fold16(m2, m2);
    if ((lane & 31) == 0) *(f32x2*)(ldsS + (lane >> 5) * 16 + 2 * w) = f32x2{sm, m2};
    float t[24];
#pragma unroll
    for (int k = 0; k < 4; ++k) {
      const float al = lo(sw0[k]), ah = hi(sw0[k]), bl = lo(sw1[k]), bh = hi(sw1[k]);
      t[2 * k] = fmaf(hi(xs0), bl, lo(xs0) * al); t[2 * k + 1] = fmaf(hi(xs0), bh, lo(xs0) * ah);
      t[8 + 2 * k] = fmaf(hi(xs1), bl, lo(xs1) * al); t[8 + 2 * k + 1] = fmaf(hi(xs1), bh, lo(xs1) * ah);
      t[16 + 2 * k] = al + bl; t[16 + 2 * k + 1] = ah + bh;
    }
#pragma unroll
    for (int k = 0; k < 8; ++k) t[k] = fold16(fold32(t[k], t[16 + k]), fold32(t[8 + k], t[16 + k]));
    *(f32x4*)(ldsY + w * 512 + g * 128 + 8 * c) = f32x4{t[0], t[1], t[2], t[3]};
    *(f32x4*)(ldsY + w * 512 + g * 128 + 8 * c + 4) = f32x4{t[4], t[5], t[6], t[7]};
    if (tid == 448) *(unsigned long long*)(ldsC + 8) = ep;
    asm volatile("" :: "v"(ep));
    if (last_wave(ldsC, lane)) {
      f32x4 st[8];
#pragma unroll
      for (int i = 0; i < 8; ++i) st[i] = *(const f32x4*)(ldsS + 4 * i);
      tagA = next_tag(__builtin_amdgcn_readfirstlane(ldsC[8])); tagB = __builtin_amdgcn_readfirstlane(ldsC[9]) + 0x9E3779B9u;
      const float mean0 = ((st[0][0] + st[0][2]) + (st[1][0] + st[1][2]) + (st[2][0] + st[2][2]) + (st[3][0] + st[3][2])) * 0.125f;
      const float mean1 = ((st[4][0] + st[4][2]) + (st[5][0] + st[5][2]) + (st[6][0] + st[6][2]) + (st[7][0] + st[7][2])) * 0.125f;
      float q0 = 0.f, q1 = 0.f, d0 = 0.f, d1 = 0.f;
#pragma unroll
      for (int i = 0; i < 4; ++i) {
        q0 += st[i][1] + st[i][3]; q1 += st[4 + i][1] + st[4 + i][3];
        const float a0 = st[i][0] - mean0, b0 = st[i][2] - mean0, a1 = st[4 + i][0] - mean1, b1 = st[4 + i][2] - mean1;
        d0 += fmaf(a0, a0, b0 * b0); d1 += fmaf(a1, a1, b1 * b1);
      }
      const float rstd0 = __builtin_amdgcn_rsqf(fmaf(64.f, d0, q0) * (1.f / D) + 1e-5f), rstd1 = __builtin_amdgcn_rsqf(fmaf(64.f, d1, q1) * (1.f / D) + 1e-5f);
      if (lane == 0) *(f32x4*)(ldsC + 12) = f32x4{mean0, rstd0, mean1, rstd1};
      last_wave(ldsC, lane);
      f32x4 z = {0.f, 0.f, 0.f, 0.f}, cs = {0.f, 0.f, 0.f, 0.f};
#pragma unroll
      for (int ww = 0; ww < NWAVE; ++ww) { z += *(const f32x4*)(ldsY + ww * 512 + 4 * lane); cs += *(const f32x4*)(ldsY + ww * 512 + 256 + 4 * (lane & 31)); }
      st_dense(rs, s1off(h, r8, 0) + lo16, (z - cs * (lane < 32 ? mean0 : mean1)) * (lane < 32 ? rstd0 : rstd1), tagA, tagB);
    }
  }
    if constexpr (p == PI0_ + 2) {
      wait_all(ldsC, NWAVE + 1);
      tagA = next_tag(__builtin_amdgcn_readfirstlane(ldsC[8])); tagB = __builtin_amdgcn_readfirstlane(ldsC[9]) + 0x9E3779B9u;
      asm volatile("" :: "v"(pf0), "v"(pf1), "v"(pfc));
      if (has_f1) {
        const f32x4 ln = *(const f32x4*)(ldsC + 12);
        const float n00 = (lo(xs0) - ln[0]) * ln[1], n01 = (hi(xs0) - ln[0]) * ln[1], n10 = (lo(xs1) - ln[2]) * ln[3], n11 = (hi(xs1) - ln[2]) * ln[3];
        float t[8];
#pragma unroll
        for (int k = 0; k < 4; ++k) {
          const float al = lo(ffw[0][k]), ah = hi(ffw[0][k]), bl = lo(ffw[1][k]), bh = hi(ffw[1][k]);
          t[2 * k] = fold32(fmaf(n01, bl, n00 * al), fmaf(n11, bl, n10 * al)); t[2 * k + 1] = fold32(fmaf(n01, bh, n00 * ah), fmaf(n11, bh, n10 * ah));
        }
        *(f32x4*)(ldsY + 4096 + w * 256 + (g >> 1) * 128 + 8 * c + 4 * (g & 1)) = f32x4{fold16(t[0], t[4]), fold16(t[1], t[5]), fold16(t[2], t[6]), fold16(t[3], t[7])};
        last_wave(ldsC + 2, lane);
      }
    }
    if constexpr (p == 5) {
      int gq = g; asm volatile("" : "+v"(gq));
      const int fbs = (gq >> 1) * 11 + (cid >> 2), hs = (fbs / 3) * 4 + fbs % 3;
      fuoff = has_f2 ? s1off(hs, w, 1) + ((g & 1) * 32 + 8 * (cid & 3) + (c & 7)) * 16 : OOB;
      asm volatile("" : "+v"(fuoff));
    }
    if constexpr (p == 4) {
      if (has_f1 && w == 0) {
        wait_all(ldsC + 2);
        f32x4 z = {0.f, 0.f, 0.f, 0.f};
#pragma unroll
        for (int ww = 0; ww < NWAVE; ++ww) z += *(const f32x4*)(ldsY + 4096 + ww * 256 + 4 * lane);
        st_dense(rs, s1off(h, r8, 1) + lo16, z, tagA, tagB);
      }
    }
    if constexpr (p == 6) {
      *(f32x4*)(ldsQ + w * 128 + 4 * (lane & 31)) = await_dense(qu, rs, qoff, tagA, tagB);
      last_wave(ldsC + 1, lane);
    }
    if constexpr (p == 8) {
      wait_all(ldsC + 1);
      f32x4 q0 = {0, 0, 0, 0}, q1 = {0, 0, 0, 0};
#pragma unroll
      for (int ww = 0; ww < NWAVE; ++ww) { q0 += *(const f32x4*)(ldsQ + ww * 128 + 8 * c); q1 += *(const f32x4*)(ldsQ + ww * 128 + 8 * c + 4); }
      qp[0] = pk_bf16(q0[0], q0[1]); qp[1] = pk_bf16(q0[2], q0[3]); qp[2] = pk_bf16(q1[0], q1[1]); qp[3] = pk_bf16(q1[2], q1[3]);
      qk(IC<0>{});
    }
    if constexpr (p == 9) { qk(IC<1>{}); qk(IC<2>{}); qk(IC<3>{}); }
    if constexpr (p == 10) { qk(IC<4>{}); qk(IC<5>{}); }
    if constexpr (p >= 10 && p < GROUPS + 4) qk(IC<p - 4>{});
    if constexpr (p == 10) {
      if (has_f2) {
        *(f32x4*)(ldsF + w * 128 + g * 32 + 4 * (c & 7)) = await_dense(fu, rs, fuoff, tagA, tagB);
        last_wave(ldsC + 5, lane);
      }
    }
    if constexpr (p == 12) {
      if (has_f2) {
        wait_all(ldsC + 5);
        float gs = 0.f, us = 0.f;
        const int hx = ((lane >> 2) & 1) * 32 + 4 * w + (lane & 3);
#pragma unroll
        for (int ww = 0; ww < NWAVE; ++ww) { gs += ldsF[ww * 128 + hx]; us += ldsF[ww * 128 + 64 + hx]; }
        const float hv = gelu_tanh(gs) * us;
        float dd[16];
#pragma unroll
        for (int k = 0; k < 16; ++k) dd[k] = 0.f;
#pragma unroll
        for (int r = 0; r < 4; ++r) {
          const float h0 = __builtin_bit_cast(float, __builtin_amdgcn_readlane(__builtin_bit_cast(int, hv), r));
          const float h1 = __builtin_bit_cast(float, __builtin_amdgcn_readlane(__builtin_bit_cast(int, hv), 4 + r));
#pragma unroll
          for (int k = 0; k < 4; ++k) {
            const float wl = lo(ffw[r][k]), wh = hi(ffw[r][k]);
            dd[2 * k] = fmaf(h0, wl, dd[2 * k]); dd[2 * k + 1] = fmaf(h0, wh, dd[2 * k + 1]);
            dd[8 + 2 * k] = fmaf(h1, wl, dd[8 + 2 * k]); dd[8 + 2 * k + 1] = fmaf(h1, wh, dd[8 + 2 * k + 1]);
          }
        }
        *(f32x4*)(ldsY + w * 1024 + 8 * lane) = f32x4{dd[0], dd[1], dd[2], dd[3]};
        *(f32x4*)(ldsY + w * 1024 + 8 * lane + 4) = f32x4{dd[4], dd[5], dd[6], dd[7]};
        *(f32x4*)(ldsY + w * 1024 + 512 + 8 * lane) = f32x4{dd[8], dd[9], dd[10], dd[11]};
        *(f32x4*)(ldsY + w * 1024 + 512 + 8 * lane + 4) = f32x4{dd[12], dd[13], dd[14], dd[15]};
        last_wave(ldsC + 3, lane);
      }
    }
    if constexpr (p == PDP_) {
      if (has_f2 && w < 4) {
        wait_all(ldsC + 3);
        f32x2 d0 = {0.f, 0.f}, d1 = {0.f, 0.f};
        const int cb = (tid & 128) * 4 + ucol(2 * (tid & 127));
#pragma unroll
        for (int ww = 0; ww < NWAVE; ++ww) { d0 += *(const f32x2*)(ldsY + ww * 1024 + cb); d1 += *(const f32x2*)(ldsY + ww * 1024 + cb + 4); }
        st_dense(rs, WS_DP + (cid * 256 + tid) * 16, f32x4{d0[0], d0[1], d1[0], d1[1]}, tagA, tagB);
      }
    }
    if constexpr (p == GROUPS + 3) {
      float x = fmaxf(fmaxf(sp[0], sp[1]), fmaxf(sp[2], sp[3]));
      x = fmaxf(x, dpp<0x128>(x)); x = fmaxf(x, dpp<0x124>(x)); x = fmaxf(x, dpp<0x122>(x)); x = fmaxf(x, dpp<0x121>(x));
      mw = xrow_max(x) * C2;
#pragma unroll
      for (int i = 0; i < 4; ++i) sp[i] = __builtin_amdgcn_exp2f(fmaf(sp[i], C2, -mw));
      const float lw = xrow_sum(row16_sum((sp[0] + sp[2]) + (sp[1] + sp[3])));
      if (lane == 0) { ldsM[slot0 & 7] = mw; ldsM[8 + (slot0 & 7)] = lw; }
      last_wave(ldsC + 4, lane);
    }
    if constexpr (p == 21) {
      if (w == 0) {
        wait_all(ldsC + 4);
        const float mi = ldsM[lane & 7], li = ldsM[8 + (lane & 7)];
        float M = fmaxf(mi, dpp<0x124>(mi)); M = fmaxf(M, dpp<0x122>(M)); M = fmaxf(M, dpp<0x121>(M));
        float L = __builtin_amdgcn_exp2f(mi - M) * li; L += dpp<0x124>(L); L += dpp<0x122>(L); L += dpp<0x121>(L);
        if (lane == 0) st_unit(rs, WS_ML + pid * 16, M, L, tagA, tagB);
      }
    }
    if constexpr (p == 31) {
      await_unit(mu, rs, muoff, tagA, tagB);
      const float mi = __uint_as_float(mu[0]);
      float M = fmaxf(mi, dpp<0xB1>(mi)); M = fmaxf(M, dpp<0x4E>(M));
      float L = __builtin_amdgcn_exp2f(mi - M) * __uint_as_float(mu[2]); L += dpp<0xB1>(L); L += dpp<0x4E>(L);
      mw = __builtin_amdgcn_exp2f(mw - M) * __builtin_amdgcn_rcpf(L);
      ysto = (g & 1) ? OOB : WS_Y + (pid * 256 + 32 * w + 16 * (g >> 1) + c) * 16;
      yoff = WS_Y + ((bc * 128 + gp) * 256 + cu0 + 2 * qd) * 16;
      asm volatile("" : "+v"(mw), "+v"(ysto), "+v"(yoff));
    }
    if constexpr (p >= 20 && p < 28) pv(IC<p - 20>{});
    if constexpr (p == 28 || p == 29) { pv(IC<2 * p - 48>{}); pv(IC<2 * p - 47>{}); }
    if constexpr (p == 30) pv(IC<12>{});
    if constexpr (p == 31) { pv(IC<13>{}); pv(IC<14>{}); pv(IC<15>{}); }
    if constexpr (p == 30) {
#pragma unroll
      for (int i = 0; i < 16; ++i) {
        const v2u r0 = wor[2 * i], r1 = wor[2 * i + 1];
        wb[0][i >> 2][i & 3] = __builtin_amdgcn_perm(r1[0], r0[0], 0x05040100u); wb[1][i >> 2][i & 3] = __builtin_amdgcn_perm(r1[0], r0[0], 0x07060302u);
        wb[2][i >> 2][i & 3] = __builtin_amdgcn_perm(r1[1], r0[1], 0x05040100u); wb[3][i >> 2][i & 3] = __builtin_amdgcn_perm(r1[1], r0[1], 0x07060302u);
      }
#pragma unroll
      for (int i = 0; i < 4; ++i) asm volatile("" : "+v"(wb[i][0]), "+v"(wb[i][1]), "+v"(wb[i][2]), "+v"(wb[i][3]));
    }
    SB();
  };
  static_for<2 * GROUPS>(point);
  MARK(34);

  {
    const f32x2 o2 = (bs[0] + bs[1]) * mw;
    const unsigned oh = pk_bf16(o2[0], o2[1]), ol = pk_bf16(o2[0] - lo(oh), o2[1] - hi(oh));
    const int ar = (slot0 & 7) * 68 + 4 * c + g;
    ldsA[ar] = oh; ldsA[8 * 68 + ar] = ol;
    if (is_fin && w < 3) {
      f32x4 e = await_dense(e0, rs, dpoff, tagA, tagB);
      if (gp >= NFFN) e = f32x4{0.f, 0.f, 0.f, 0.f};
      const float r = fold4(e[0], e[1], e[2], e[3]);
      if (c < 4) ldsF[w * 16 + 4 * c + g] = r;
    }
  }
  __syncthreads();
  MARK(35);
  {
    v4u af[4];
#pragma unroll
    for (int ks = 0; ks < 4; ++ks) af[ks] = *(const v4u*)(ldsA + c * 68 + 16 * ks + 4 * g);
    SB();
    f32x4 d[4];
#pragma unroll
    for (int t = 0; t < 4; ++t) d[t] = f32x4{0.f, 0.f, 0.f, 0.f};
#pragma unroll
    for (int ks = 0; ks < 4; ++ks)
#pragma unroll
      for (int t = 0; t < 4; ++t) d[t] = __builtin_amdgcn_mfma_f32_16x16x32_bf16(__builtin_bit_cast(bf16x8, af[ks]), __builtin_bit_cast(bf16x8, wb[t][ks]), d[t], 0, 0, 0);
    float y[4];
#pragma unroll
    for (int t = 0; t < 4; ++t) y[t] = (d[t][0] + d[t][1]) + (d[t][2] + d[t][3]);
    float u0 = fold32(y[0], y[2]), u1 = fold32(y[1], y[3]);
    u0 = fold16(u0, u0); u1 = fold16(u1, u1);
    st_unit<YSA_>(rs, ysto, u0, u1, tagA, tagB);
  }
  MARK(36);

  if (is_fin) {
    __builtin_amdgcn_s_barrier();
    const float dv = ldsF[c] + ldsF[16 + c] + ldsF[32 + c];
    float gte = dv * __builtin_amdgcn_rcpf(1.f + __expf(-dv));
    asm volatile("" : "+v"(gte));
    if (PDLY_) __builtin_amdgcn_s_sleep(PDLY_);
    MARK(38);
    v4u y0, y1;
    bool ok = false;
#pragma unroll 1
    for (unsigned spin = 0; spin < SPIN_CAP; ++spin) {
      asm volatile("" ::: "memory");
      if (!ok) {
        y0 = ld_unit(rs, yoff); y1 = ld_unit(rs, yoff + 16);
        ok = (((y0[1] ^ tagA) | (y1[1] ^ tagA)) | ((y0[3] ^ tagB) | (y1[3] ^ tagB))) == 0u;
      }
      if (__all(ok)) break;
      __builtin_amdgcn_s_sleep(RSL_);
    }
    MARK(39);
    const float r = fold4(__uint_as_float(y0[0]), __uint_as_float(y0[2]), __uint_as_float(y1[0]), __uint_as_float(y1[2]));
    if (c < 4) ldsF[128 + w * 16 + 4 * c + g] = r;
    __syncthreads();
    if (tid < 16) {
      float sv = 0.f;
#pragma unroll
      for (int ww = 0; ww < NWAVE; ++ww) sv += ldsF[128 + ww * 16 + tid];
      const float o = __uint_as_float((unsigned)xo << 16) + sv * gte;
      const __bf16 ob = (__bf16)o;
      ((GAS unsigned short*)KAP(8))[ocol] = __builtin_bit_cast(unsigned short, ob);
      if (cid == 0 && tid == 0)
        __hip_atomic_store((GAS unsigned long long*)(pws + WS_EPOCH), ((unsigned long long)tagB << 32) | tagA, __ATOMIC_RELAXED, __HIP_MEMORY_SCOPE_AGENT);
    }
  }
  asm volatile(".Lk_all_end:");
}

extern "C" void kernel_launch(void* const* d_in, const int* in_sizes, int n_in, void* d_out, int out_size, void* d_ws, size_t ws_size,
                              hipStream_t stream) {
  Args a;
  a.x = (const unsigned short*)d_in[0];
  a.k = (const unsigned short*)d_in[1];
  a.v = (const unsigned short*)d_in[2];
  a.wq = (const unsigned short*)d_in[3];
  a.wo = (const unsigned short*)d_in[4];
  a.wg = (const unsigned short*)d_in[6];
  a.wu = (const unsigned short*)d_in[7];
  a.wd = (const unsigned short*)d_in[8];
  a.out = (unsigned short*)d_out;
  a.ws = (unsigned char*)d_ws;
  hipLaunchKernelGGL(k_all, dim3(256), dim3(NTHR), 0, stream, a);
}
```

```cpp
#include <hip/hip_runtime.h>
#include <hip/hip_bf16.h>
#include <stdint.h>

typedef unsigned v4u __attribute__((ext_vector_type(4)));
typedef float f32x4 __attribute__((ext_vector_type(4)));
typedef float f32x2 __attribute__((ext_vector_type(2)));
typedef __bf16 bf16x2 __attribute__((ext_vector_type(2)));
typedef __bf16 bf16x8 __attribute__((ext_vector_type(8)));
typedef unsigned v16u __attribute__((ext_vector_type(16)));
typedef unsigned v2u __attribute__((ext_vector_type(2)));
#define GAS __attribute__((address_space(1)))
#define CAS __attribute__((address_space(4)))

constexpr int D = 512, H = 32, SK = 8192, DH = 128, F = 1376;
constexpr int NSPLIT = 4, KEYS_PER_WG = SK / NSPLIT;
constexpr int NWAVE = 8, NTHR = NWAVE * 64;
constexpr int NFFN = F / 32;
constexpr int U = 4;
constexpr int GROUPS = KEYS_PER_WG / NWAVE / (4 * U);
#define PWO_ 1
constexpr int won(int p) { return p <= PWO_ ? 0 : p >= 28 ? 32 : (p - PWO_) * 32 / (28 - PWO_); }
#define MSA_ 16
#define YSA_ 16
#define EX1_ 60
#define EX2_ 0
#define STEP(pt) (((unsigned)TPX & ~7u) + (unsigned)(EXT(pt)))
#define EXT(pt) ((pt) < 9 ? EX1_ : (pt) >= 14 && (pt) < 18 ? EX2_ : 0)
#define PI0_ 0
#define RSL_ 1
#define GORD_ 1
#define PEL_ 31
#define PDP_ 28
#define YL_ 0
#define PDLY_ 11
#define LATE_PRIO_ 400
#define TP_ 1728
#define T0_ 2500

constexpr int WS_EPOCH = 0;
constexpr int WS_ML = 4096;
constexpr int WS_Y = WS_ML + 256 * 16;
constexpr int WS_DP = WS_Y + 256 * 128 * 16;
constexpr int WS_S1 = WS_DP + NFFN * 256 * 16;
constexpr int WS_END = WS_S1 + 32 * 8 * 2 * 64 * 16;
constexpr int OOB = 0x7ffffff0;
constexpr unsigned SPIN_CAP = 400000;

#define SB() __builtin_amdgcn_sched_barrier(0)

__device__ __forceinline__ float dot2(unsigned k, unsigned q, float acc) {
  return __builtin_amdgcn_fdot2_f32_bf16(__builtin_bit_cast(bf16x2, k), __builtin_bit_cast(bf16x2, q), acc, false);
}
template <int CTRL> __device__ __forceinline__ float dpp(float x) {
  return __builtin_bit_cast(float, __builtin_amdgcn_mov_dpp(__builtin_bit_cast(int, x), CTRL, 0xf, 0xf, true));
}
__device__ __forceinline__ float row16_sum(float x) {
  x += dpp<0x128>(x);
  x += dpp<0x124>(x);
  x += dpp<0x122>(x);
  x += dpp<0x121>(x);
  return x;
}
__device__ __forceinline__ float fold32(float a, float b) {
  auto t = __builtin_amdgcn_permlane32_swap(__float_as_uint(a), __float_as_uint(b), false, false);
  return __uint_as_float(t[0]) + __uint_as_float(t[1]);
}
__device__ __forceinline__ float fold16(float a, float b) {
  auto t = __builtin_amdgcn_permlane16_swap(__float_as_uint(a), __float_as_uint(b), false, false);
  return __uint_as_float(t[0]) + __uint_as_float(t[1]);
}
__device__ __forceinline__ float xrow_sum(float x) { x = fold16(x, x); return fold32(x, x); }
__device__ __forceinline__ float xrow_max(float x) {
  auto s = __builtin_amdgcn_permlane16_swap(__float_as_uint(x), __float_as_uint(x), false, false);
  x = fmaxf(__uint_as_float(s[0]), __uint_as_float(s[1]));
  auto t = __builtin_amdgcn_permlane32_swap(__float_as_uint(x), __float_as_uint(x), false, false);
  return fmaxf(__uint_as_float(t[0]), __uint_as_float(t[1]));
}
__device__ __forceinline__ f32x2 fold8(const float (&v)[8]) {
  const float u0 = fold32(v[0], v[4]), u1 = fold32(v[1], v[5]), u2 = fold32(v[2], v[6]), u3 = fold32(v[3], v[7]);
  return f32x2{fold16(u0, u2), fold16(u1, u3)};
}
__device__ __forceinline__ float fold4(float v0, float v1, float v2, float v3) {
  float r = fold16(fold32(v0, v2), fold32(v1, v3));
  r += dpp<0x124>(r); r += dpp<0x128>(r);
  return r;
}
__device__ __forceinline__ float lo(unsigned u) { return __uint_as_float(u << 16); }
__device__ __forceinline__ float hi(unsigned u) { return __uint_as_float(u & 0xffff0000u); }
__device__ __forceinline__ unsigned pk_bf16(float a, float b) {
  bf16x2 r = {(__bf16)a, (__bf16)b};
  return __builtin_bit_cast(unsigned, r);
}
__device__ __forceinline__ unsigned sel4(v4u v, int g) { const unsigned a = (g & 1) ? v[1] : v[0], b = (g & 1) ? v[3] : v[2]; return (g & 2) ? b : a; }
__device__ __forceinline__ int s1off(int hg, int r, int k) { return WS_S1 + (hg * 8 + ((r + hg) & 7)) * 2048 + ((k ^ (hg >> 3)) & 1) * 1024; }
__device__ __forceinline__ float gelu_tanh(float x) {
  const float u = 0.7978845608028654f * (x + 0.044715f * x * x * x);
  const float e = __expf(2.f * u);
  const float t = 1.f - 2.f * __builtin_amdgcn_rcpf(e + 1.f);
  return 0.5f * x * (1.f + t);
}
__device__ __forceinline__ void pace_wait(unsigned& due16, unsigned step16) {
  asm volatile("1:\n\ts_memrealtime s[90:91]\n\ts_waitcnt lgkmcnt(0)\n\ts_lshl_b32 s90, s90, 4\n\ts_sub_u32 s91, s90, %0\n\ts_cmp_ge_i32 s91, 0\n\ts_cbranch_scc1 2f\n\ts_sleep 2\n\ts_branch 1b\n"
               "2:\n\ts_add_u32 %0, %0, %1\n\ts_setprio 0\n\ts_cmp_lt_i32 s91, %2\n\ts_cbranch_scc1 3f\n\ts_setprio 2\n3:"
               : "+s"(due16) : "s"(step16), "i"(LATE_PRIO_) : "s90", "s91", "scc");
}
__device__ __forceinline__ unsigned now16() { return (unsigned)__builtin_amdgcn_s_memrealtime() << 4; }
#define MARK(i)
#define TPX TP_
#define T0X T0_
#define GORDX GORD_
#define PACE(pt) pace_wait(due, STEP(pt))
__device__ __forceinline__ unsigned next_tag(unsigned e) { return ((e + 1u) & 1023u) | ((e + 1024u) & (1023u << 10)) | ((e + (1u << 20)) & 0xfff00000u); }
constexpr int AUX_SC1 = 16;
template <int AUX = MSA_> __device__ __forceinline__ void st_unit(__amdgpu_buffer_rsrc_t rs, int off, float v0, float v1, unsigned ta, unsigned tb) {
  __builtin_amdgcn_raw_buffer_store_b128(v4u{__float_as_uint(v0), ta, __float_as_uint(v1), tb}, rs, off, 0, AUX);
}
__device__ __forceinline__ v4u ld_unit(__amdgpu_buffer_rsrc_t rs, int off) { return __builtin_amdgcn_raw_buffer_load_b128(rs, off, 0, AUX_SC1); }
__device__ __forceinline__ unsigned enc22(float v, unsigned t) { return ((__float_as_uint(v) + 0x200u) & 0xfffffc00u) | t; }
__device__ __forceinline__ void st_dense(__amdgpu_buffer_rsrc_t rs, int off, f32x4 v, unsigned ta, unsigned tb) {
  __builtin_amdgcn_raw_buffer_store_b128(v4u{enc22(v[0], ta & 1023u), enc22(v[1], (ta >> 10) & 1023u), enc22(v[2], (ta >> 20) & 1023u), enc22(v[3], tb & 1023u)}, rs, off, 0, AUX_SC1);
}
__device__ __forceinline__ bool ok_dense(v4u u, unsigned ta, unsigned tb) { return ((((u[0] ^ ta) | (u[1] ^ (ta >> 10))) | ((u[2] ^ (ta >> 20)) | (u[3] ^ tb))) & 1023u) == 0u; }
__device__ __forceinline__ f32x4 await_dense(v4u u, __amdgpu_buffer_rsrc_t rs, int off, unsigned ta, unsigned tb) {
  if (!__all(ok_dense(u, ta, tb))) {
#pragma unroll 1
    for (unsigned spin = 0; spin < SPIN_CAP; ++spin) {
      __builtin_amdgcn_s_sleep(1);
      asm volatile("" ::: "memory");
      u = ld_unit(rs, off);
      if (__all(ok_dense(u, ta, tb))) break;
    }
  }
  return __builtin_bit_cast(f32x4, u & 0xfffffc00u);
}
__device__ __forceinline__ void await_unit(v4u& u, __amdgpu_buffer_rsrc_t rs, int off, unsigned ta, unsigned tb) {
  if (!__all(((u[1] ^ ta) | (u[3] ^ tb)) == 0u)) {
#pragma unroll 1
    for (unsigned spin = 0; spin < SPIN_CAP; ++spin) {
      __builtin_amdgcn_s_sleep(1);
      asm volatile("" ::: "memory");
      u = ld_unit(rs, off);
      if (__all(((u[1] ^ ta) | (u[3] ^ tb)) == 0u)) break;
    }
  }
}

__device__ __forceinline__ bool last_wave(unsigned* cnt, int lane) {
  asm volatile("s_waitcnt lgkmcnt(0)" ::: "memory");
  unsigned old = 0;
  if (lane == 0) old = __hip_atomic_fetch_add(cnt, 1u, __ATOMIC_RELAXED, __HIP_MEMORY_SCOPE_WORKGROUP);
  old = __builtin_amdgcn_readfirstlane(old);
  asm volatile("" ::: "memory");
  return old == NWAVE - 1;
}

__device__ __forceinline__ void wait_all(unsigned* cnt, unsigned n = NWAVE) {
  while (__builtin_amdgcn_readfirstlane(__hip_atomic_load(cnt, __ATOMIC_RELAXED, __HIP_MEMORY_SCOPE_WORKGROUP)) < n) __builtin_amdgcn_s_sleep(1);
  asm volatile("" ::: "memory");
}
template <int I> struct IC { static constexpr int value = I; };
template <int N, int I = 0, typename Fn> __device__ __forceinline__ void static_for(Fn& fn) { if constexpr (I < N) { fn(IC<I>{}); static_for<N, I + 1>(fn); } }

struct Args {
  const unsigned short* x; const unsigned short* wq;
  const unsigned short* k; const unsigned short* v; const unsigned short* wo; const unsigned short* wg;
  const unsigned short* wu; const unsigned short* wd; unsigned short* out; unsigned char* ws;
};

__global__ void __launch_bounds__(NTHR, 2) k_all(Args a) {
  __shared__ __attribute__((aligned(16))) float lds[8 * 1024 + 16 * 68 + 16 + 8 * 128 + 8 * 128 + 32 + 16];
  float* ldsY = lds;
  unsigned* ldsA = (unsigned*)(lds + 8 * 1024);
  float* ldsM = lds + 8 * 1024 + 16 * 68;
  float* ldsF = ldsM + 16;
  float* ldsQ = ldsF + 8 * 128;
  float* ldsS = ldsQ + 8 * 128;
  unsigned* ldsC = (unsigned*)(ldsS + 32);
  MARK(32);
  const int bid = blockIdx.x;
  const int xcd = bid & 7, j = bid >> 3;
  const int tid = threadIdx.x, lane = tid & 63, w = __builtin_amdgcn_readfirstlane(tid >> 6), lo16 = lane * 16;
  v16u ka; v4u kb; unsigned long long tptr, pc0; unsigned csz;
  asm volatile("s_load_dwordx16 %0, %5, 0x0\n\ts_load_dwordx4 %1, %5, 0x40\n\ts_load_dwordx2 %2, %5, %6 offset:0x10\n\t"
               "s_getpc_b64 %3\n.Lk_all_pc0:\n\ts_mov_b32 %4, (.Lk_all_end-.Lk_all_pc0)&0xfffffff0"
               : "=&s"(ka), "=&s"(kb), "=&s"(tptr), "=&s"(pc0), "=&s"(csz) : "s"(__builtin_amdgcn_kernarg_segment_ptr()), "s"(8 * w));
#define RSRC(p_, n_) __builtin_amdgcn_make_buffer_rsrc((void*)(p_), 0, (n_), 0x00020000)
  const v4u pfc = __builtin_amdgcn_raw_buffer_load_b128(RSRC(pc0 & ~127ull, w == 4 ? csz : 0u), lo16, j * 1024, 0);
  ldsC[tid & 15] = 0u;
  SB();
  const int h = xcd * 4 + (j >> 3), b = (j >> 2) & 1, s = j & 3;
  const int g = lane >> 4, c = lane & 15;
  const int r8 = j & 7;
  int qwo = (2 * g * H * DH + 8 * c) * 2, xso = (lane >> 5) * D * 2 + (lane & 31) * 4, qso = ((64 * r8 + 8 * w) * H * DH + h * DH) * 2;
  unsigned tof = ((unsigned)(b * H + h) * ((0x03u >> w) & 1u) << 21) + ((unsigned)h * ((0x04u >> w) & 1u) << 17);
  asm volatile("" : "+v"(qwo), "+v"(xso), "+v"(tof));
  asm volatile("s_waitcnt lgkmcnt(0)\n\ts_barrier" : "+s"(ka), "+s"(kb), "+s"(tptr), "+s"(qso) :: "memory");
#define KAP(i) ((i) < 8 ? (unsigned long long)ka[2 * (i) % 16] | ((unsigned long long)ka[(2 * (i) + 1) % 16] << 32) : (unsigned long long)kb[2 * (i) % 4] | ((unsigned long long)kb[(2 * (i) + 1) % 4] << 32))
  const unsigned long long px = KAP(0), pws = KAP(9);
  const unsigned long long ep = __hip_atomic_load((const GAS unsigned long long*)(tptr + tof), __ATOMIC_RELAXED, __HIP_MEMORY_SCOPE_AGENT);
  SB();
  const unsigned xst = __builtin_amdgcn_raw_buffer_load_b32(RSRC(px, 2 * D * 2), xso, 128 * w, 0);
  SB();
  const __amdgpu_buffer_rsrc_t rq = RSRC(KAP(1), D * H * DH * 2);
  const v4u sw0 = __builtin_amdgcn_raw_buffer_load_b128(rq, qwo, qso, 2), sw1 = __builtin_amdgcn_raw_buffer_load_b128(rq, qwo, qso + H * DH * 2, 2);
  v4u xq0, xq1;
  asm volatile("s_load_dwordx4 %0, %2, 0x0\n\ts_load_dwordx4 %1, %2, 0x400" : "=&s"(xq0), "=&s"(xq1) : "s"(px + (64 * r8 + 8 * w) * 2));
  SB();
  if (w == 0) {
    asm volatile("" :: "v"(ep));
    __hip_atomic_store(ldsC + 10, now16() | 1u, __ATOMIC_RELAXED, __HIP_MEMORY_SCOPE_WORKGROUP);
    MARK(40);
  }
  SB();
  const __amdgpu_buffer_rsrc_t rw = RSRC(KAP(4) + (size_t)h * DH * D * 2, DH * D * 2);
  const v4u pf0 = __builtin_amdgcn_raw_buffer_load_b128(rw, lo16, (16 * r8 + 2 * w) * 1024, 0), pf1 = __builtin_amdgcn_raw_buffer_load_b128(rw, lo16 + 1024, (16 * r8 + 2 * w) * 1024, 0);
  SB();
  const int fb = xcd * 3 + (j >> 3);
  const bool has_f1 = (j < 24) && (fb < 22);
  const bool is_fin = j >= 24;
  const int cid = xcd * 8 + (j - 24);
  const bool has_f2 = is_fin && cid < NFFN;
  const int row0 = 64 * r8 + 8 * w + 2 * g;
  const int fcol = 128 * (fb < 11 ? fb : fb - 11) + 8 * c;
  int fx1 = (row0 * F + fcol) * 2, fx2 = (32 * cid + 4 * w) * D * 2 + lo16;
  asm volatile("" : "+v"(fx1), "+v"(fx2));
  const int fxo = has_f1 ? (fcol < F ? fx1 : OOB - 4 * F * 2) : fx2, fxo2 = has_f1 ? OOB - 4096 : fx2, fst = has_f1 ? F * 2 : D * 2;
  unsigned f_lo = fb < 11 ? ka[10] : ka[12], f_hi = fb < 11 ? ka[11] : ka[13];
  asm volatile("" : "+s"(f_lo), "+s"(f_hi));
  const __amdgpu_buffer_rsrc_t rf = RSRC((unsigned long long)(has_f1 ? f_lo : ka[14]) | ((unsigned long long)(has_f1 ? f_hi : ka[15]) << 32), (has_f1 || has_f2) ? D * F * 2 : 0);
  MARK(41);
  v4u ffw[4];
  ffw[0] = __builtin_amdgcn_raw_buffer_load_b128(rf, fxo, 0, 2);
  ffw[1] = __builtin_amdgcn_raw_buffer_load_b128(rf, fxo, fst, 2);
  ffw[2] = __builtin_amdgcn_raw_buffer_load_b128(rf, fxo2 + 2048, 0, 2);
  ffw[3] = __builtin_amdgcn_raw_buffer_load_b128(rf, fxo2 + 3072, 0, 2);
  SB();
  MARK(42);
  const __amdgpu_buffer_rsrc_t rs = RSRC(pws, WS_END);

  const __amdgpu_buffer_rsrc_t rk = RSRC(KAP(2) + (size_t)(b * H + h) * SK * DH * 2, SK * DH * 2);
  const __amdgpu_buffer_rsrc_t rv = RSRC(KAP(3) + (size_t)(b * H + h) * SK * DH * 2, SK * DH * 2);
#define GOFF1(gi_) (((gi_) & 1) * ((1 << 20) | (1 << 12)) | ((gi_) & 2) * ((1 << 18) | (1 << 13)) | (((gi_) & 4) << 16) | (((gi_) & 8) << 14))
#define GOFF0(gi_) ((((gi_) & 15) << 17) | (((gi_) & 4) << 12) | (((gi_) & 8) << 9))
#define GOFF(gi_) (GORDX ? GOFF1(gi_) : GOFF0(gi_))
  v4u rg[2 * GROUPS][U];

  constexpr float C2 = 0.08838834764831845f * 1.4426950408889634f;
  float sp[4] = {0.f, 0.f, 0.f, 0.f};
  float mw = 0.f;
  f32x2 bs[8];
  unsigned tagA, tagB, qp[4], slot0;
  v2u wor[32]; v4u wb[4][4], fu, qu, mu, e0; unsigned short xo;
  unsigned xs0, xs1;
  int fuoff, ysto, yoff;
  const int wvo = g * 8192 + w * 128 + c * 8;
  const int qoff = s1off(h, w, 0) + (b * 32 + (lane & 31)) * 16;
  const int pid = (b * H + h) * NSPLIT + s, muoff = WS_ML + ((pid & ~3) + (lane & 3)) * 16;
  const int bc = (cid >> 5) & 1, cu0 = (cid & 31) * 4;
  const int gp = tid >> 2, qd = tid & 3, ocol = bc * D + 4 * cu0 + c;
  const int dpoff = (is_fin && w < 3) ? WS_DP + ((gp < NFFN ? gp : NFFN - 1) * 256 + bc * 128 + cu0 + qd) * 16 : OOB;

  auto qk = [&](auto gic) __attribute__((always_inline)) {
    constexpr int gi = decltype(gic)::value;
    float d[U];
#pragma unroll
    for (int u = 0; u < U; ++u) d[u] = dot2(rg[gi][u][3], qp[3], dot2(rg[gi][u][2], qp[2], dot2(rg[gi][u][1], qp[1], dot2(rg[gi][u][0], qp[0], 0.f))));
    const bool o1 = c & 1, o2 = c & 2;
    const float x0 = (o1 ? d[1] : d[0]) + dpp<0xB1>(o1 ? d[0] : d[1]), x1 = (o1 ? d[3] : d[2]) + dpp<0xB1>(o1 ? d[2] : d[3]);
    float x = (o2 ? x1 : x0) + dpp<0x4E>(o2 ? x0 : x1);
    x += dpp<0x128>(x); x += dpp<0x124>(x);
    sp[gi / 4] = __builtin_bit_cast(float, __builtin_amdgcn_update_dpp(__builtin_bit_cast(int, sp[gi / 4]), __builtin_bit_cast(int, x), 0xE4, 0xf, 1 << (gi % 4), false));
    asm volatile("" : "+v"(sp[gi / 4]));
    SB();
  };
  auto pv = [&](auto gic) __attribute__((always_inline)) {
    constexpr int gi = decltype(gic)::value;
    const float p[4] = {dpp<0x150 + 4 * (gi % 4)>(sp[gi / 4]), dpp<0x151 + 4 * (gi % 4)>(sp[gi / 4]), dpp<0x152 + 4 * (gi % 4)>(sp[gi / 4]), dpp<0x153 + 4 * (gi % 4)>(sp[gi / 4])};
    float x[8];
#pragma unroll
    for (int k = 0; k < 4; ++k) { x[2 * k] = p[0] * lo(rg[GROUPS + gi][0][k]); x[2 * k + 1] = p[0] * hi(rg[GROUPS + gi][0][k]); }
#pragma unroll
    for (int u = 1; u < U; ++u) {
#pragma unroll
      for (int k = 0; k < 4; ++k) { x[2 * k] = fmaf(p[u], lo(rg[GROUPS + gi][u][k]), x[2 * k]); x[2 * k + 1] = fmaf(p[u], hi(rg[GROUPS + gi][u][k]), x[2 * k + 1]); }
    }
    const f32x2 y = fold8(x);
    if constexpr (gi < 8) bs[gi] = y; else bs[gi - 8] += y;
    if constexpr (gi >= 12) bs[gi - 12] += bs[gi - 8];
    if constexpr (gi >= 14) bs[gi - 14] += bs[gi - 12];
    asm volatile("" : "+v"(bs[gi >= 14 ? gi - 14 : gi >= 12 ? gi - 12 : gi % 8]));
    SB();
  };

  {
    unsigned tk;
    while ((tk = __builtin_amdgcn_readfirstlane(__hip_atomic_load(ldsC + 10, __ATOMIC_RELAXED, __HIP_MEMORY_SCOPE_WORKGROUP))) == 0u) __builtin_amdgcn_s_sleep(1);
    slot0 = __builtin_amdgcn_readfirstlane((__builtin_amdgcn_readfirstlane(tk) + (unsigned)T0X) / (unsigned)(TPX / 8) + (unsigned)w);
  }
  const int kvo = ((slot0 & 1) << 13) | ((slot0 & 6) << 14) | ((s & 1) << 12) | ((s >> 1) << 14);
  unsigned due = __builtin_amdgcn_readfirstlane(slot0 * (unsigned)(TPX / 8));

  auto point = [&](auto pc) __attribute__((always_inline)) {
    constexpr int p = decltype(pc)::value;
    int so = kvo ^ GOFF((slot0 >> 3) + p);
    asm volatile("" : "+s"(so));
    PACE(p);
    SB();
#pragma unroll
    for (int u = 0; u < U; ++u) rg[p][u] = __builtin_amdgcn_raw_buffer_load_b128(p < GROUPS ? rk : rv, lo16 + u * 1024, so, 2);
    SB();
    if constexpr (p == 4) qu = ld_unit(rs, qoff);
    if constexpr (p == 7) fu = ld_unit(rs, fuoff);
    if constexpr (p == 29) mu = ld_unit(rs, muoff);
#pragma unroll
    for (int i = won(p); i < won(p + 1); ++i) wor[i] = __builtin_amdgcn_raw_buffer_load_b64(rw, wvo, (32 * (i >> 3) + (i & 7)) * 1024, 0);
    if constexpr (p == PEL_) {
      e0 = ld_unit(rs, dpoff);
      xo = ((const GAS unsigned short*)px)[ocol];
    }
    SB();
    if constexpr (p == PI0_) {
    asm volatile("s_waitcnt lgkmcnt(0)" : "+s"(xq0), "+s"(xq1));
    xs0 = sel4(xq0, g); xs1 = sel4(xq1, g);
    float sm = row16_sum(lo(xst) + hi(xst)); sm = fold16(sm, sm) * (1.f / 64);
    const float d0 = lo(xst) - sm, d1 = hi(xst) - sm;
    float m2 = row16_sum(fmaf(d0, d0, d1 * d1)); m2 = fold16(m2, m2);
    if ((lane & 31) == 0) *(f32x2*)(ldsS + (lane >> 5) * 16 + 2 * w) = f32x2{sm, m2};
    float t[24];
#pragma unroll
    for (int k = 0; k < 4; ++k) {
      const float al = lo(sw0[k]), ah = hi(sw0[k]), bl = lo(sw1[k]), bh = hi(sw1[k]);
      t[2 * k] = fmaf(hi(xs0), bl, lo(xs0) * al); t[2 * k + 1] = fmaf(hi(xs0), bh, lo(xs0) * ah);
      t[8 + 2 * k] = fmaf(hi(xs1), bl, lo(xs1) * al); t[8 + 2 * k + 1] = fmaf(hi(xs1), bh, lo(xs1) * ah);
      t[16 + 2 * k] = al + bl; t[16 + 2 * k + 1] = ah + bh;
    }
#pragma unroll
    for (int k = 0; k < 8; ++k) t[k] = fold16(fold32(t[k], t[16 + k]), fold32(t[8 + k], t[16 + k]));
    *(f32x4*)(ldsY + w * 512 + g * 128 + 8 * c) = f32x4{t[0], t[1], t[2], t[3]};
    *(f32x4*)(ldsY + w * 512 + g * 128 + 8 * c + 4) = f32x4{t[4], t[5], t[6], t[7]};
    if (tid == 448) *(unsigned long long*)(ldsC + 8) = ep;
    asm volatile("" :: "v"(ep));
    if (last_wave(ldsC, lane)) {
      f32x4 st[8];
#pragma unroll
      for (int i = 0; i < 8; ++i) st[i] = *(const f32x4*)(ldsS + 4 * i);
      tagA = next_tag(__builtin_amdgcn_readfirstlane(ldsC[8])); tagB = __builtin_amdgcn_readfirstlane(ldsC[9]) + 0x9E3779B9u;
      const float mean0 = ((st[0][0] + st[0][2]) + (st[1][0] + st[1][2]) + (st[2][0] + st[2][2]) + (st[3][0] + st[3][2])) * 0.125f;
      const float mean1 = ((st[4][0] + st[4][2]) + (st[5][0] + st[5][2]) + (st[6][0] + st[6][2]) + (st[7][0] + st[7][2])) * 0.125f;
      float q0 = 0.f, q1 = 0.f, d0 = 0.f, d1 = 0.f;
#pragma unroll
      for (int i = 0; i < 4; ++i) {
        q0 += st[i][1] + st[i][3]; q1 += st[4 + i][1] + st[4 + i][3];
        const float a0 = st[i][0] - mean0, b0 = st[i][2] - mean0, a1 = st[4 + i][0] - mean1, b1 = st[4 + i][2] - mean1;
        d0 += fmaf(a0, a0, b0 * b0); d1 += fmaf(a1, a1, b1 * b1);
      }
      const float rstd0 = __builtin_amdgcn_rsqf(fmaf(64.f, d0, q0) * (1.f / D) + 1e-5f), rstd1 = __builtin_amdgcn_rsqf(fmaf(64.f, d1, q1) * (1.f / D) + 1e-5f);
      if (lane == 0) *(f32x4*)(ldsC + 12) = f32x4{mean0, rstd0, mean1, rstd1};
      last_wave(ldsC, lane);
      f32x4 z = {0.f, 0.f, 0.f, 0.f}, cs = {0.f, 0.f, 0.f, 0.f};
#pragma unroll
      for (int ww = 0; ww < NWAVE; ++ww) { z += *(const f32x4*)(ldsY + ww * 512 + 4 * lane); cs += *(const f32x4*)(ldsY + ww * 512 + 256 + 4 * (lane & 31)); }
      st_dense(rs, s1off(h, r8, 0) + lo16, (z - cs * (lane < 32 ? mean0 : mean1)) * (lane < 32 ? rstd0 : rstd1), tagA, tagB);
    }
  }
    if constexpr (p == PI0_ + 2) {
      wait_all(ldsC, NWAVE + 1);
      tagA = next_tag(__builtin_amdgcn_readfirstlane(ldsC[8])); tagB = __builtin_amdgcn_readfirstlane(ldsC[9]) + 0x9E3779B9u;
      asm volatile("" :: "v"(pf0), "v"(pf1), "v"(pfc));
      if (has_f1) {
        const f32x4 ln = *(const f32x4*)(ldsC + 12);
        const float n00 = (lo(xs0) - ln[0]) * ln[1], n01 = (hi(xs0) - ln[0]) * ln[1], n10 = (lo(xs1) - ln[2]) * ln[3], n11 = (hi(xs1) - ln[2]) * ln[3];
        float t[8];
#pragma unroll
        for (int k = 0; k < 4; ++k) {
          const float al = lo(ffw[0][k]), ah = hi(ffw[0][k]), bl = lo(ffw[1][k]), bh = hi(ffw[1][k]);
          t[2 * k] = fold32(fmaf(n01, bl, n00 * al), fmaf(n11, bl, n10 * al)); t[2 * k + 1] = fold32(fmaf(n01, bh, n00 * ah), fmaf(n11, bh, n10 * ah));
        }
        *(f32x4*)(ldsY + 4096 + w * 256 + (g >> 1) * 128 + 8 * c + 4 * (g & 1)) = f32x4{fold16(t[0], t[4]), fold16(t[1], t[5]), fold16(t[2], t[6]), fold16(t[3], t[7])};
        last_wave(ldsC + 2, lane);
      }
    }
    if constexpr (p == 5) {
      int gq = g; asm volatile("" : "+v"(gq));
      const int fbs = (gq >> 1) * 11 + (cid >> 2), hs = (fbs / 3) * 4 + fbs % 3;
      fuoff = has_f2 ? s1off(hs, w, 1) + ((g & 1) * 32 + 8 * (cid & 3) + (c & 7)) * 16 : OOB;
      asm volatile("" : "+v"(fuoff));
    }
    if constexpr (p == 4) {
      if (has_f1 && w == 0) {
        wait_all(ldsC + 2);
        f32x4 z = {0.f, 0.f, 0.f, 0.f};
#pragma unroll
        for (int ww = 0; ww < NWAVE; ++ww) z += *(const f32x4*)(ldsY + 4096 + ww * 256 + 4 * lane);
        st_dense(rs, s1off(h, r8, 1) + lo16, z, tagA, tagB);
      }
    }
    if constexpr (p == 6) {
      *(f32x4*)(ldsQ + w * 128 + 4 * (lane & 31)) = await_dense(qu, rs, qoff, tagA, tagB);
      last_wave(ldsC + 1, lane);
    }
    if constexpr (p == 8) {
      wait_all(ldsC + 1);
      f32x4 q0 = {0, 0, 0, 0}, q1 = {0, 0, 0, 0};
#pragma unroll
      for (int ww = 0; ww < NWAVE; ++ww) { q0 += *(const f32x4*)(ldsQ + ww * 128 + 8 * c); q1 += *(const f32x4*)(ldsQ + ww * 128 + 8 * c + 4); }
      qp[0] = pk_bf16(q0[0], q0[1]); qp[1] = pk_bf16(q0[2], q0[3]); qp[2] = pk_bf16(q1[0], q1[1]); qp[3] = pk_bf16(q1[2], q1[3]);
      qk(IC<0>{});
    }
    if constexpr (p == 9) { qk(IC<1>{}); qk(IC<2>{}); qk(IC<3>{}); }
    if constexpr (p == 10) { qk(IC<4>{}); qk(IC<5>{}); }
    if constexpr (p >= 10 && p < GROUPS + 4) qk(IC<p - 4>{});
    if constexpr (p == 10) {
      if (has_f2) {
        *(f32x4*)(ldsF + w * 128 + g * 32 + 4 * (c & 7)) = await_dense(fu, rs, fuoff, tagA, tagB);
        last_wave(ldsC + 5, lane);
      }
    }
    if constexpr (p == 12) {
      if (has_f2) {
        wait_all(ldsC + 5);
        float gs = 0.f, us = 0.f;
        const int hx = ((lane >> 2) & 1) * 32 + 4 * w + (lane & 3);
#pragma unroll
        for (int ww = 0; ww < NWAVE; ++ww) { gs += ldsF[ww * 128 + hx]; us += ldsF[ww * 128 + 64 + hx]; }
        const float hv = gelu_tanh(gs) * us;
        float dd[16];
#pragma unroll
        for (int k = 0; k < 16; ++k) dd[k] = 0.f;
#pragma unroll
        for (int r = 0; r < 4; ++r) {
          const float h0 = __builtin_bit_cast(float, __builtin_amdgcn_readlane(__builtin_bit_cast(int, hv), r));
          const float h1 = __builtin_bit_cast(float, __builtin_amdgcn_readlane(__builtin_bit_cast(int, hv), 4 + r));
#pragma unroll
          for (int k = 0; k < 4; ++k) {
            const float wl = lo(ffw[r][k]), wh = hi(ffw[r][k]);
            dd[2 * k] = fmaf(h0, wl, dd[2 * k]); dd[2 * k + 1] = fmaf(h0, wh, dd[2 * k + 1]);
            dd[8 + 2 * k] = fmaf(h1, wl, dd[8 + 2 * k]); dd[8 + 2 * k + 1] = fmaf(h1, wh, dd[8 + 2 * k + 1]);
          }
        }
        *(f32x4*)(ldsY + w * 1024 + 8 * lane) = f32x4{dd[0], dd[1], dd[2], dd[3]};
        *(f32x4*)(ldsY + w * 1024 + 8 * lane + 4) = f32x4{dd[4], dd[5], dd[6], dd[7]};
        *(f32x4*)(ldsY + w * 1024 + 512 + 8 * lane) = f32x4{dd[8], dd[9], dd[10], dd[11]};
        *(f32x4*)(ldsY + w * 1024 + 512 + 8 * lane + 4) = f32x4{dd[12], dd[13], dd[14], dd[15]};
        last_wave(ldsC + 3, lane);
      }
    }
    if constexpr (p == PDP_) {
      if (has_f2 && w < 4) {
        wait_all(ldsC + 3);
        f32x4 dv = {0.f, 0.f, 0.f, 0.f};
#pragma unroll
        for (int ww = 0; ww < NWAVE; ++ww) dv += *(const f32x4*)(ldsY + ww * 1024 + 4 * tid);
        st_dense(rs, WS_DP + (cid * 256 + tid) * 16, dv, tagA, tagB);
      }
    }
    if constexpr (p == GROUPS + 3) {
      float x = fmaxf(fmaxf(sp[0], sp[1]), fmaxf(sp[2], sp[3]));
      x = fmaxf(x, dpp<0x128>(x)); x = fmaxf(x, dpp<0x124>(x)); x = fmaxf(x, dpp<0x122>(x)); x = fmaxf(x, dpp<0x121>(x));
      mw = xrow_max(x) * C2;
#pragma unroll
      for (int i = 0; i < 4; ++i) sp[i] = __builtin_amdgcn_exp2f(fmaf(sp[i], C2, -mw));
      const float lw = xrow_sum(row16_sum((sp[0] + sp[2]) + (sp[1] + sp[3])));
      if (lane == 0) { ldsM[slot0 & 7] = mw; ldsM[8 + (slot0 & 7)] = lw; }
      last_wave(ldsC + 4, lane);
    }
    if constexpr (p == 21) {
      if (w == 0) {
        wait_all(ldsC + 4);
        const float mi = ldsM[lane & 7], li = ldsM[8 + (lane & 7)];
        float M = fmaxf(mi, dpp<0x124>(mi)); M = fmaxf(M, dpp<0x122>(M)); M = fmaxf(M, dpp<0x121>(M));
        float L = __builtin_amdgcn_exp2f(mi - M) * li; L += dpp<0x124>(L); L += dpp<0x122>(L); L += dpp<0x121>(L);
        if (lane == 0) st_unit(rs, WS_ML + pid * 16, M, L, tagA, tagB);
      }
    }
    if constexpr (p == 31) {
      await_unit(mu, rs, muoff, tagA, tagB);
      const float mi = __uint_as_float(mu[0]);
      float M = fmaxf(mi, dpp<0xB1>(mi)); M = fmaxf(M, dpp<0x4E>(M));
      float L = __builtin_amdgcn_exp2f(mi - M) * __uint_as_float(mu[2]); L += dpp<0xB1>(L); L += dpp<0x4E>(L);
      mw = __builtin_amdgcn_exp2f(mw - M) * __builtin_amdgcn_rcpf(L);
      ysto = g ? OOB : WS_Y + (YL_ == 0 ? pid * 128 + 16 * w + c : YL_ == 1 ? ((pid >> 1) * 32 + 4 * w + (c >> 2)) * 8 + (pid & 1) * 4 + (c & 3) : (((pid >> 7) * 32 + 4 * w + (c >> 2)) * 128 + (pid & 127)) * 4 + (c & 3)) * 16;
      yoff = WS_Y + (YL_ == 0 ? (bc * 128 + gp) * 128 + cu0 + qd : YL_ == 1 ? (((bc * 128 + gp) >> 1) * 32 + (cid & 31)) * 8 + (gp & 1) * 4 + qd : cid * 512 + tid) * 16;
      asm volatile("" : "+v"(mw), "+v"(ysto), "+v"(yoff));
    }
    if constexpr (p >= 20 && p < 28) pv(IC<p - 20>{});
    if constexpr (p == 28 || p == 29) { pv(IC<2 * p - 48>{}); pv(IC<2 * p - 47>{}); }
    if constexpr (p == 30) pv(IC<12>{});
    if constexpr (p == 31) { pv(IC<13>{}); pv(IC<14>{}); pv(IC<15>{}); }
    if constexpr (p == 30) {
#pragma unroll
      for (int i = 0; i < 16; ++i) {
        const v2u r0 = wor[2 * i], r1 = wor[2 * i + 1];
        wb[0][i >> 2][i & 3] = __builtin_amdgcn_perm(r1[0], r0[0], 0x05040100u); wb[1][i >> 2][i & 3] = __builtin_amdgcn_perm(r1[0], r0[0], 0x07060302u);
        wb[2][i >> 2][i & 3] = __builtin_amdgcn_perm(r1[1], r0[1], 0x05040100u); wb[3][i >> 2][i & 3] = __builtin_amdgcn_perm(r1[1], r0[1], 0x07060302u);
      }
#pragma unroll
      for (int i = 0; i < 4; ++i) asm volatile("" : "+v"(wb[i][0]), "+v"(wb[i][1]), "+v"(wb[i][2]), "+v"(wb[i][3]));
    }
    SB();
  };
  static_for<2 * GROUPS>(point);
  MARK(34);

  {
    const f32x2 o2 = (bs[0] + bs[1]) * mw;
    const unsigned oh = pk_bf16(o2[0], o2[1]), ol = pk_bf16(o2[0] - lo(oh), o2[1] - hi(oh));
    const int ar = (slot0 & 7) * 68 + 4 * c + g;
    ldsA[ar] = oh; ldsA[8 * 68 + ar] = ol;
    if (is_fin && w < 3) {
      f32x4 e = await_dense(e0, rs, dpoff, tagA, tagB);
      if (gp >= NFFN) e = f32x4{0.f, 0.f, 0.f, 0.f};
      const float r = fold4(e[0], e[1], e[2], e[3]);
      if (c < 4) ldsF[w * 16 + 4 * c + g] = r;
    }
  }
  __syncthreads();
  MARK(35);
  {
    v4u af[4];
#pragma unroll
    for (int ks = 0; ks < 4; ++ks) af[ks] = *(const v4u*)(ldsA + c * 68 + 16 * ks + 4 * g);
    SB();
    f32x4 d[4];
#pragma unroll
    for (int t = 0; t < 4; ++t) d[t] = f32x4{0.f, 0.f, 0.f, 0.f};
#pragma unroll
    for (int ks = 0; ks < 4; ++ks)
#pragma unroll
      for (int t = 0; t < 4; ++t) d[t] = __builtin_amdgcn_mfma_f32_16x16x32_bf16(__builtin_bit_cast(bf16x8, af[ks]), __builtin_bit_cast(bf16x8, wb[t][ks]), d[t], 0, 0, 0);
    float y[4];
#pragma unroll
    for (int t = 0; t < 4; ++t) y[t] = (d[t][0] + d[t][1]) + (d[t][2] + d[t][3]);
    float u0 = fold32(y[0], y[2]), u1 = fold32(y[1], y[3]);
    u0 = fold16(u0, u0); u1 = fold16(u1, u1);
    const auto r0 = __builtin_amdgcn_permlane32_swap(__float_as_uint(u0), __float_as_uint(u0), false, false), r1 = __builtin_amdgcn_permlane32_swap(__float_as_uint(u1), __float_as_uint(u1), false, false);
    st_dense(rs, ysto, f32x4{__uint_as_float(r0[0]), __uint_as_float(r1[0]), __uint_as_float(r0[1]), __uint_as_float(r1[1])}, tagA, tagB);
  }
  MARK(36);

  if (is_fin) {
    __builtin_amdgcn_s_barrier();
    const float dv = ldsF[c] + ldsF[16 + c] + ldsF[32 + c];
    float gte = dv * __builtin_amdgcn_rcpf(1.f + __expf(-dv));
    asm volatile("" : "+v"(gte));
    if (PDLY_) __builtin_amdgcn_s_sleep(PDLY_);
    MARK(38);
    v4u y0;
    bool ok = false;
#pragma unroll 1
    for (unsigned spin = 0; spin < SPIN_CAP; ++spin) {
      asm volatile("" ::: "memory");
      if (!ok) { y0 = ld_unit(rs, yoff); ok = ok_dense(y0, tagA, tagB); }
      if (__all(ok)) break;
      __builtin_amdgcn_s_sleep(RSL_);
    }
    MARK(39);
    const f32x4 yv = __builtin_bit_cast(f32x4, y0 & 0xfffffc00u);
    const float r = fold4(yv[0], yv[1], yv[2], yv[3]);
    if (c < 4) ldsF[128 + w * 16 + 4 * c + g] = r;
    __syncthreads();
    if (tid < 16) {
      float sv = 0.f;
#pragma unroll
      for (int ww = 0; ww < NWAVE; ++ww) sv += ldsF[128 + ww * 16 + tid];
      const float o = __uint_as_float((unsigned)xo << 16) + sv * gte;
      const __bf16 ob = (__bf16)o;
      ((GAS unsigned short*)KAP(8))[ocol] = __builtin_bit_cast(unsigned short, ob);
      if (cid == 0 && tid == 0)
        __hip_atomic_store((GAS unsigned long long*)(pws + WS_EPOCH), ((unsigned long long)tagB << 32) | tagA, __ATOMIC_RELAXED, __HIP_MEMORY_SCOPE_AGENT);
    }
  }
  asm volatile(".Lk_all_end:");
}

extern "C" void kernel_launch(void* const* d_in, const int* in_sizes, int n_in, void* d_out, int out_size, void* d_ws, size_t ws_size,
                              hipStream_t stream) {
  Args a;
  a.x = (const unsigned short*)d_in[0];
  a.k = (const unsigned short*)d_in[1];
  a.v = (const unsigned short*)d_in[2];
  a.wq = (const unsigned short*)d_in[3];
  a.wo = (const unsigned short*)d_in[4];
  a.wg = (const unsigned short*)d_in[6];
  a.wu = (const unsigned short*)d_in[7];
  a.wd = (const unsigned short*)d_in[8];
  a.out = (unsigned short*)d_out;
  a.ws = (unsigned char*)d_ws;
  hipLaunchKernelGGL(k_all, dim3(256), dim3(NTHR), 0, stream, a);
}
```

```cpp
#include <hip/hip_runtime.h>
#include <hip/hip_bf16.h>
#include <stdint.h>

typedef unsigned v4u __attribute__((ext_vector_type(4)));
typedef float f32x4 __attribute__((ext_vector_type(4)));
typedef float f32x2 __attribute__((ext_vector_type(2)));
typedef __bf16 bf16x2 __attribute__((ext_vector_type(2)));
typedef __bf16 bf16x8 __attribute__((ext_vector_type(8)));
typedef unsigned v16u __attribute__((ext_vector_type(16)));
typedef unsigned v2u __attribute__((ext_vector_type(2)));
#define GAS __attribute__((address_space(1)))
#define CAS __attribute__((address_space(4)))

constexpr int D = 512, H = 32, SK = 8192, DH = 128, F = 1376;
constexpr int NSPLIT = 4, KEYS_PER_WG = SK / NSPLIT;
constexpr int NWAVE = 8, NTHR = NWAVE * 64;
constexpr int NFFN = F / 32;
constexpr int U = 4;
constexpr int GROUPS = KEYS_PER_WG / NWAVE / (4 * U);
#define TP_ 1728
#define T0_ 2350
#define EX1_ 70
constexpr unsigned step_of(int p) { return TP_ + (p < 9 ? EX1_ : 0); }
constexpr int LATE_PRIO = 400;
constexpr int won(int p) { return p <= 1 ? 0 : p >= 28 ? 32 : (p - 1) * 32 / 27; }
static_assert(TP_ % 8 == 0, "a slot is TP / 8");

constexpr int WS_EPOCH = 0;
constexpr int WS_ML = 4096;
constexpr int WS_Y = WS_ML + 256 * 16;
constexpr int WS_DP = WS_Y + 256 * 128 * 16;
constexpr int WS_S1 = WS_DP + NFFN * 256 * 16;
constexpr int WS_END = WS_S1 + 32 * 8 * 2 * 64 * 16;
constexpr int OOB = 0x7ffffff0;
constexpr unsigned SPIN_CAP = 400000;

#define SB() __builtin_amdgcn_sched_barrier(0)

__device__ __forceinline__ float dot2(unsigned k, unsigned q, float acc) {
  return __builtin_amdgcn_fdot2_f32_bf16(__builtin_bit_cast(bf16x2, k), __builtin_bit_cast(bf16x2, q), acc, false);
}
template <int CTRL> __device__ __forceinline__ float dpp(float x) {
  return __builtin_bit_cast(float, __builtin_amdgcn_mov_dpp(__builtin_bit_cast(int, x), CTRL, 0xf, 0xf, true));
}
__device__ __forceinline__ float row16_sum(float x) {
  x += dpp<0x128>(x);
  x += dpp<0x124>(x);
  x += dpp<0x122>(x);
  x += dpp<0x121>(x);
  return x;
}
__device__ __forceinline__ float fold32(float a, float b) {
  auto t = __builtin_amdgcn_permlane32_swap(__float_as_uint(a), __float_as_uint(b), false, false);
  return __uint_as_float(t[0]) + __uint_as_float(t[1]);
}
__device__ __forceinline__ float fold16(float a, float b) {
  auto t = __builtin_amdgcn_permlane16_swap(__float_as_uint(a), __float_as_uint(b), false, false);
  return __uint_as_float(t[0]) + __uint_as_float(t[1]);
}
__device__ __forceinline__ float xrow_sum(float x) { x = fold16(x, x); return fold32(x, x); }
__device__ __forceinline__ float xrow_max(float x) {
  auto s = __builtin_amdgcn_permlane16_swap(__float_as_uint(x), __float_as_uint(x), false, false);
  x = fmaxf(__uint_as_float(s[0]), __uint_as_float(s[1]));
  auto t = __builtin_amdgcn_permlane32_swap(__float_as_uint(x), __float_as_uint(x), false, false);
  return fmaxf(__uint_as_float(t[0]), __uint_as_float(t[1]));
}
__device__ __forceinline__ f32x2 fold8(const float (&v)[8]) {
  const float u0 = fold32(v[0], v[4]), u1 = fold32(v[1], v[5]), u2 = fold32(v[2], v[6]), u3 = fold32(v[3], v[7]);
  return f32x2{fold16(u0, u2), fold16(u1, u3)};
}
__device__ __forceinline__ float fold4(float v0, float v1, float v2, float v3) {
  float r = fold16(fold32(v0, v2), fold32(v1, v3));
  r += dpp<0x124>(r); r += dpp<0x128>(r);
  return r;
}
__device__ __forceinline__ float lo(unsigned u) { return __uint_as_float(u << 16); }
__device__ __forceinline__ float hi(unsigned u) { return __uint_as_float(u & 0xffff0000u); }
__device__ __forceinline__ unsigned pk_bf16(float a, float b) {
  bf16x2 r = {(__bf16)a, (__bf16)b};
  return __builtin_bit_cast(unsigned, r);
}
__device__ __forceinline__ unsigned sel4(v4u v, int g) { const unsigned a = (g & 1) ? v[1] : v[0], b = (g & 1) ? v[3] : v[2]; return (g & 2) ? b : a; }
__device__ __forceinline__ int s1off(int hg, int r, int k) { return WS_S1 + (hg * 8 + ((r + hg) & 7)) * 2048 + ((k ^ (hg >> 3)) & 1) * 1024; }
__device__ __forceinline__ float gelu_tanh(float x) {
  const float u = 0.7978845608028654f * (x + 0.044715f * x * x * x);
  const float e = __expf(2.f * u);
  const float t = 1.f - 2.f * __builtin_amdgcn_rcpf(e + 1.f);
  return 0.5f * x * (1.f + t);
}
__device__ __forceinline__ void pace_wait(unsigned& due16, unsigned step16) {
  asm volatile("1:\n\ts_memrealtime s[90:91]\n\ts_waitcnt lgkmcnt(0)\n\ts_lshl_b32 s90, s90, 4\n\ts_sub_u32 s91, s90, %0\n\ts_cmp_ge_i32 s91, 0\n\ts_cbranch_scc1 2f\n\ts_sleep 2\n\ts_branch 1b\n"
               "2:\n\ts_add_u32 %0, %0, %1\n\ts_setprio 0\n\ts_cmp_lt_i32 s91, %2\n\ts_cbranch_scc1 3f\n\ts_setprio 2\n3:"
               : "+s"(due16) : "s"(step16), "i"(LATE_PRIO) : "s90", "s91", "scc");
}
__device__ __forceinline__ unsigned now16() { return (unsigned)__builtin_amdgcn_s_memrealtime() << 4; }
__device__ __forceinline__ unsigned next_tag(unsigned e) { return ((e + 1u) & 1023u) | ((e + 1024u) & (1023u << 10)) | ((e + (1u << 20)) & 0xfff00000u); }
constexpr int AUX_SC1 = 16;
__device__ __forceinline__ void st_unit(__amdgpu_buffer_rsrc_t rs, int off, float v0, float v1, unsigned ta, unsigned tb) {
  __builtin_amdgcn_raw_buffer_store_b128(v4u{__float_as_uint(v0), ta, __float_as_uint(v1), tb}, rs, off, 0, AUX_SC1);
}
__device__ __forceinline__ v4u ld_unit(__amdgpu_buffer_rsrc_t rs, int off) { return __builtin_amdgcn_raw_buffer_load_b128(rs, off, 0, AUX_SC1); }
__device__ __forceinline__ unsigned enc22(float v, unsigned t) { return ((__float_as_uint(v) + 0x200u) & 0xfffffc00u) | t; }
__device__ __forceinline__ void st_dense(__amdgpu_buffer_rsrc_t rs, int off, f32x4 v, unsigned ta, unsigned tb) {
  __builtin_amdgcn_raw_buffer_store_b128(v4u{enc22(v[0], ta & 1023u), enc22(v[1], (ta >> 10) & 1023u), enc22(v[2], (ta >> 20) & 1023u), enc22(v[3], tb & 1023u)}, rs, off, 0, AUX_SC1);
}
__device__ __forceinline__ bool ok_dense(v4u u, unsigned ta, unsigned tb) { return ((((u[0] ^ ta) | (u[1] ^ (ta >> 10))) | ((u[2] ^ (ta >> 20)) | (u[3] ^ tb))) & 1023u) == 0u; }
__device__ __forceinline__ f32x4 await_dense(v4u u, __amdgpu_buffer_rsrc_t rs, int off, unsigned ta, unsigned tb) {
  if (!__all(ok_dense(u, ta, tb))) {
#pragma unroll 1
    for (unsigned spin = 0; spin < SPIN_CAP; ++spin) {
      __builtin_amdgcn_s_sleep(1);
      asm volatile("" ::: "memory");
      u = ld_unit(rs, off);
      if (__all(ok_dense(u, ta, tb))) break;
    }
  }
  return __builtin_bit_cast(f32x4, u & 0xfffffc00u);
}
__device__ __forceinline__ void await_unit(v4u& u, __amdgpu_buffer_rsrc_t rs, int off, unsigned ta, unsigned tb) {
  if (!__all(((u[1] ^ ta) | (u[3] ^ tb)) == 0u)) {
#pragma unroll 1
    for (unsigned spin = 0; spin < SPIN_CAP; ++spin) {
      __builtin_amdgcn_s_sleep(1);
      asm volatile("" ::: "memory");
      u = ld_unit(rs, off);
      if (__all(((u[1] ^ ta) | (u[3] ^ tb)) == 0u)) break;
    }
  }
}

__device__ __forceinline__ bool last_wave(unsigned* cnt, int lane) {
  asm volatile("s_waitcnt lgkmcnt(0)" ::: "memory");
  unsigned old = 0;
  if (lane == 0) old = __hip_atomic_fetch_add(cnt, 1u, __ATOMIC_RELAXED, __HIP_MEMORY_SCOPE_WORKGROUP);
  old = __builtin_amdgcn_readfirstlane(old);
  asm volatile("" ::: "memory");
  return old == NWAVE - 1;
}

__device__ __forceinline__ void wait_all(unsigned* cnt, unsigned n = NWAVE) {
  while (__builtin_amdgcn_readfirstlane(__hip_atomic_load(cnt, __ATOMIC_RELAXED, __HIP_MEMORY_SCOPE_WORKGROUP)) < n) __builtin_amdgcn_s_sleep(1);
  asm volatile("" ::: "memory");
}
template <int I> struct IC { static constexpr int value = I; };
template <int N, int I = 0, typename Fn> __device__ __forceinline__ void static_for(Fn& fn) { if constexpr (I < N) { fn(IC<I>{}); static_for<N, I + 1>(fn); } }

struct Args {
  const unsigned short* x; const unsigned short* wq;
  const unsigned short* k; const unsigned short* v; const unsigned short* wo; const unsigned short* wg;
  const unsigned short* wu; const unsigned short* wd; unsigned short* out; unsigned char* ws;
};

__global__ void __launch_bounds__(NTHR, 2) k_all(Args a) {
  __shared__ __attribute__((aligned(16))) float lds[8 * 1024 + 16 * 68 + 16 + 8 * 128 + 8 * 128 + 32 + 16];
  float* ldsY = lds;
  unsigned* ldsA = (unsigned*)(lds + 8 * 1024);
  float* ldsM = lds + 8 * 1024 + 16 * 68;
  float* ldsF = ldsM + 16;
  float* ldsQ = ldsF + 8 * 128;
  float* ldsS = ldsQ + 8 * 128;
  unsigned* ldsC = (unsigned*)(ldsS + 32);
  const int bid = blockIdx.x;
  const int xcd = bid & 7, j = bid >> 3;
  const int tid = threadIdx.x, lane = tid & 63, w = __builtin_amdgcn_readfirstlane(tid >> 6), lo16 = lane * 16;
  v16u ka; v4u kb; unsigned long long tptr, pc0; unsigned csz;
  asm volatile("s_load_dwordx16 %0, %5, 0x0\n\ts_load_dwordx4 %1, %5, 0x40\n\ts_load_dwordx2 %2, %5, %6 offset:0x10\n\t"
               "s_getpc_b64 %3\n.Lk_all_pc0:\n\ts_mov_b32 %4, (.Lk_all_end-.Lk_all_pc0)&0xfffffff0"
               : "=&s"(ka), "=&s"(kb), "=&s"(tptr), "=&s"(pc0), "=&s"(csz) : "s"(__builtin_amdgcn_kernarg_segment_ptr()), "s"(8 * w));
#define RSRC(p_, n_) __builtin_amdgcn_make_buffer_rsrc((void*)(p_), 0, (n_), 0x00020000)
  const v4u pfc = __builtin_amdgcn_raw_buffer_load_b128(RSRC(pc0 & ~127ull, w == 4 ? csz : 0u), lo16, j * 1024, 0);
  ldsC[tid & 15] = 0u;
  SB();
  const int h = xcd * 4 + (j >> 3), b = (j >> 2) & 1, s = j & 3;
  const int g = lane >> 4, c = lane & 15;
  const int r8 = j & 7;
  int qwo = (2 * g * H * DH + 8 * c) * 2, xso = (lane >> 5) * D * 2 + (lane & 31) * 4, qso = ((64 * r8 + 8 * w) * H * DH + h * DH) * 2;
  unsigned tof = ((unsigned)(b * H + h) * ((0x03u >> w) & 1u) << 21) + ((unsigned)h * ((0x04u >> w) & 1u) << 17);
  asm volatile("" : "+v"(qwo), "+v"(xso), "+v"(tof));
  asm volatile("s_waitcnt lgkmcnt(0)\n\ts_barrier" : "+s"(ka), "+s"(kb), "+s"(tptr), "+s"(qso) :: "memory");
#define KAP(i) ((i) < 8 ? (unsigned long long)ka[2 * (i) % 16] | ((unsigned long long)ka[(2 * (i) + 1) % 16] << 32) : (unsigned long long)kb[2 * (i) % 4] | ((unsigned long long)kb[(2 * (i) + 1) % 4] << 32))
  const unsigned long long px = KAP(0), pws = KAP(9);
  const unsigned long long ep = __hip_atomic_load((const GAS unsigned long long*)(tptr + tof), __ATOMIC_RELAXED, __HIP_MEMORY_SCOPE_AGENT);
  SB();
  const unsigned xst = __builtin_amdgcn_raw_buffer_load_b32(RSRC(px, 2 * D * 2), xso, 128 * w, 0);
  SB();
  const __amdgpu_buffer_rsrc_t rq = RSRC(KAP(1), D * H * DH * 2);
  const v4u sw0 = __builtin_amdgcn_raw_buffer_load_b128(rq, qwo, qso, 2), sw1 = __builtin_amdgcn_raw_buffer_load_b128(rq, qwo, qso + H * DH * 2, 2);
  v4u xq0, xq1;
  asm volatile("s_load_dwordx4 %0, %2, 0x0\n\ts_load_dwordx4 %1, %2, 0x400" : "=&s"(xq0), "=&s"(xq1) : "s"(px + (64 * r8 + 8 * w) * 2));
  SB();
  const __amdgpu_buffer_rsrc_t rw = RSRC(KAP(4) + (size_t)h * DH * D * 2, DH * D * 2);
  const v4u pf0 = __builtin_amdgcn_raw_buffer_load_b128(rw, lo16, (16 * r8 + 2 * w) * 1024, 0), pf1 = __builtin_amdgcn_raw_buffer_load_b128(rw, lo16 + 1024, (16 * r8 + 2 * w) * 1024, 0);
  SB();
  const int fb = xcd * 3 + (j >> 3);
  const bool has_f1 = (j < 24) && (fb < 22);
  const bool is_fin = j >= 24;
  const int cid = xcd * 8 + (j - 24);
  const bool has_f2 = is_fin && cid < NFFN;
  const int row0 = 64 * r8 + 8 * w + 2 * g;
  const int fcol = 128 * (fb < 11 ? fb : fb - 11) + 8 * c;
  int fx1 = (row0 * F + fcol) * 2, fx2 = (32 * cid + 4 * w) * D * 2 + lo16;
  asm volatile("" : "+v"(fx1), "+v"(fx2));
  const int fxo = has_f1 ? (fcol < F ? fx1 : OOB - 4 * F * 2) : fx2, fxo2 = has_f1 ? OOB - 4096 : fx2, fst = has_f1 ? F * 2 : D * 2;
  unsigned f_lo = fb < 11 ? ka[10] : ka[12], f_hi = fb < 11 ? ka[11] : ka[13];
  asm volatile("" : "+s"(f_lo), "+s"(f_hi));
  const __amdgpu_buffer_rsrc_t rf = RSRC((unsigned long long)(has_f1 ? f_lo : ka[14]) | ((unsigned long long)(has_f1 ? f_hi : ka[15]) << 32), (has_f1 || has_f2) ? D * F * 2 : 0);
  v4u ffw[4];
  ffw[0] = __builtin_amdgcn_raw_buffer_load_b128(rf, fxo, 0, 2);
  ffw[1] = __builtin_amdgcn_raw_buffer_load_b128(rf, fxo, fst, 2);
  ffw[2] = __builtin_amdgcn_raw_buffer_load_b128(rf, fxo2 + 2048, 0, 2);
  ffw[3] = __builtin_amdgcn_raw_buffer_load_b128(rf, fxo2 + 3072, 0, 2);
  SB();
  if (w == 0) {
    asm volatile("" :: "v"(ep));
    __hip_atomic_store(ldsC + 10, now16() | 1u, __ATOMIC_RELAXED, __HIP_MEMORY_SCOPE_WORKGROUP);
  }
  SB();
  const __amdgpu_buffer_rsrc_t rs = RSRC(pws, WS_END);

  const __amdgpu_buffer_rsrc_t rk = RSRC(KAP(2) + (size_t)(b * H + h) * SK * DH * 2, SK * DH * 2);
  const __amdgpu_buffer_rsrc_t rv = RSRC(KAP(3) + (size_t)(b * H + h) * SK * DH * 2, SK * DH * 2);
#define GOFF(gi_) (((gi_) & 1) * ((1 << 20) | (1 << 12)) | ((gi_) & 2) * ((1 << 18) | (1 << 13)) | (((gi_) & 4) << 16) | (((gi_) & 8) << 14))
  v4u rg[2 * GROUPS][U];

  constexpr float C2 = 0.08838834764831845f * 1.4426950408889634f;
  float sp[4] = {0.f, 0.f, 0.f, 0.f};
  float mw = 0.f;
  f32x2 bs[8];
  unsigned tagA, tagB, qp[4], slot0;
  v2u wor[32]; v4u wb[4][4], fu, qu, mu, e0; unsigned short xo;
  unsigned xs0, xs1;
  int fuoff, ysto, yoff;
  const int wvo = g * 8192 + w * 128 + c * 8;
  const int qoff = s1off(h, w, 0) + (b * 32 + (lane & 31)) * 16;
  const int pid = (b * H + h) * NSPLIT + s, muoff = WS_ML + ((pid & ~3) + (lane & 3)) * 16;
  const int bc = (cid >> 5) & 1, cu0 = (cid & 31) * 4;
  const int gp = tid >> 2, qd = tid & 3, ocol = bc * D + 4 * cu0 + c;
  const int dpoff = (is_fin && w < 3) ? WS_DP + ((gp < NFFN ? gp : NFFN - 1) * 256 + bc * 128 + cu0 + qd) * 16 : OOB;

  auto qk = [&](auto gic) __attribute__((always_inline)) {
    constexpr int gi = decltype(gic)::value;
    float d[U];
#pragma unroll
    for (int u = 0; u < U; ++u) d[u] = dot2(rg[gi][u][3], qp[3], dot2(rg[gi][u][2], qp[2], dot2(rg[gi][u][1], qp[1], dot2(rg[gi][u][0], qp[0], 0.f))));
    const bool o1 = c & 1, o2 = c & 2;
    const float x0 = (o1 ? d[1] : d[0]) + dpp<0xB1>(o1 ? d[0] : d[1]), x1 = (o1 ? d[3] : d[2]) + dpp<0xB1>(o1 ? d[2] : d[3]);
    float x = (o2 ? x1 : x0) + dpp<0x4E>(o2 ? x0 : x1);
    x += dpp<0x128>(x); x += dpp<0x124>(x);
    sp[gi / 4] = __builtin_bit_cast(float, __builtin_amdgcn_update_dpp(__builtin_bit_cast(int, sp[gi / 4]), __builtin_bit_cast(int, x), 0xE4, 0xf, 1 << (gi % 4), false));
    asm volatile("" : "+v"(sp[gi / 4]));
    SB();
  };
  auto pv = [&](auto gic) __attribute__((always_inline)) {
    constexpr int gi = decltype(gic)::value;
    const float p[4] = {dpp<0x150 + 4 * (gi % 4)>(sp[gi / 4]), dpp<0x151 + 4 * (gi % 4)>(sp[gi / 4]), dpp<0x152 + 4 * (gi % 4)>(sp[gi / 4]), dpp<0x153 + 4 * (gi % 4)>(sp[gi / 4])};
    float x[8];
#pragma unroll
    for (int k = 0; k < 4; ++k) { x[2 * k] = p[0] * lo(rg[GROUPS + gi][0][k]); x[2 * k + 1] = p[0] * hi(rg[GROUPS + gi][0][k]); }
#pragma unroll
    for (int u = 1; u < U; ++u) {
#pragma unroll
      for (int k = 0; k < 4; ++k) { x[2 * k] = fmaf(p[u], lo(rg[GROUPS + gi][u][k]), x[2 * k]); x[2 * k + 1] = fmaf(p[u], hi(rg[GROUPS + gi][u][k]), x[2 * k + 1]); }
    }
    const f32x2 y = fold8(x);
    if constexpr (gi < 8) bs[gi] = y; else bs[gi - 8] += y;
    if constexpr (gi >= 12) bs[gi - 12] += bs[gi - 8];
    if constexpr (gi >= 14) bs[gi - 14] += bs[gi - 12];
    asm volatile("" : "+v"(bs[gi >= 14 ? gi - 14 : gi >= 12 ? gi - 12 : gi % 8]));
    SB();
  };

  {
    unsigned tk;
    while ((tk = __builtin_amdgcn_readfirstlane(__hip_atomic_load(ldsC + 10, __ATOMIC_RELAXED, __HIP_MEMORY_SCOPE_WORKGROUP))) == 0u) __builtin_amdgcn_s_sleep(1);
    slot0 = __builtin_amdgcn_readfirstlane((__builtin_amdgcn_readfirstlane(tk) + (unsigned)T0_) / (unsigned)(TP_ / 8) + (unsigned)w);
  }
  const int kvo = ((slot0 & 1) << 13) | ((slot0 & 6) << 14) | ((s & 1) << 12) | ((s >> 1) << 14);
  unsigned due = __builtin_amdgcn_readfirstlane(slot0 * (unsigned)(TP_ / 8));

  auto point = [&](auto pc) __attribute__((always_inline)) {
    constexpr int p = decltype(pc)::value;
    int so = kvo ^ GOFF((slot0 >> 3) + p);
    asm volatile("" : "+s"(so));
    pace_wait(due, step_of(p));
    SB();
#pragma unroll
    for (int u = 0; u < U; ++u) rg[p][u] = __builtin_amdgcn_raw_buffer_load_b128(p < GROUPS ? rk : rv, lo16 + u * 1024, so, 2);
    SB();
    if constexpr (p == 4) qu = ld_unit(rs, qoff);
    if constexpr (p == 7) fu = ld_unit(rs, fuoff);
    if constexpr (p == 29) mu = ld_unit(rs, muoff);
#pragma unroll
    for (int i = won(p); i < won(p + 1); ++i) wor[i] = __builtin_amdgcn_raw_buffer_load_b64(rw, wvo, (32 * (i >> 3) + (i & 7)) * 1024, 0);
    if constexpr (p == 31) {
      e0 = ld_unit(rs, dpoff);
      xo = ((const GAS unsigned short*)px)[ocol];
    }
    SB();
    if constexpr (p == 0) {
    asm volatile("s_waitcnt lgkmcnt(0)" : "+s"(xq0), "+s"(xq1));
    xs0 = sel4(xq0, g); xs1 = sel4(xq1, g);
    float sm = row16_sum(lo(xst) + hi(xst)); sm = fold16(sm, sm) * (1.f / 64);
    const float d0 = lo(xst) - sm, d1 = hi(xst) - sm;
    float m2 = row16_sum(fmaf(d0, d0, d1 * d1)); m2 = fold16(m2, m2);
    if ((lane & 31) == 0) *(f32x2*)(ldsS + (lane >> 5) * 16 + 2 * w) = f32x2{sm, m2};
    float t[24];
#pragma unroll
    for (int k = 0; k < 4; ++k) {
      const float al = lo(sw0[k]), ah = hi(sw0[k]), bl = lo(sw1[k]), bh = hi(sw1[k]);
      t[2 * k] = fmaf(hi(xs0), bl, lo(xs0) * al); t[2 * k + 1] = fmaf(hi(xs0), bh, lo(xs0) * ah);
      t[8 + 2 * k] = fmaf(hi(xs1), bl, lo(xs1) * al); t[8 + 2 * k + 1] = fmaf(hi(xs1), bh, lo(xs1) * ah);
      t[16 + 2 * k] = al + bl; t[16 + 2 * k + 1] = ah + bh;
    }
#pragma unroll
    for (int k = 0; k < 8; ++k) t[k] = fold16(fold32(t[k], t[16 + k]), fold32(t[8 + k], t[16 + k]));
    *(f32x4*)(ldsY + w * 512 + g * 128 + 8 * c) = f32x4{t[0], t[1], t[2], t[3]};
    *(f32x4*)(ldsY + w * 512 + g * 128 + 8 * c + 4) = f32x4{t[4], t[5], t[6], t[7]};
    if (tid == 448) *(unsigned long long*)(ldsC + 8) = ep;
    asm volatile("" :: "v"(ep));
    if (last_wave(ldsC, lane)) {
      f32x4 st[8];
#pragma unroll
      for (int i = 0; i < 8; ++i) st[i] = *(const f32x4*)(ldsS + 4 * i);
      tagA = next_tag(__builtin_amdgcn_readfirstlane(ldsC[8])); tagB = __builtin_amdgcn_readfirstlane(ldsC[9]) + 0x9E3779B9u;
      const float mean0 = ((st[0][0] + st[0][2]) + (st[1][0] + st[1][2]) + (st[2][0] + st[2][2]) + (st[3][0] + st[3][2])) * 0.125f;
      const float mean1 = ((st[4][0] + st[4][2]) + (st[5][0] + st[5][2]) + (st[6][0] + st[6][2]) + (st[7][0] + st[7][2])) * 0.125f;
      float q0 = 0.f, q1 = 0.f, d0 = 0.f, d1 = 0.f;
#pragma unroll
      for (int i = 0; i < 4; ++i) {
        q0 += st[i][1] + st[i][3]; q1 += st[4 + i][1] + st[4 + i][3];
        const float a0 = st[i][0] - mean0, b0 = st[i][2] - mean0, a1 = st[4 + i][0] - mean1, b1 = st[4 + i][2] - mean1;
        d0 += fmaf(a0, a0, b0 * b0); d1 += fmaf(a1, a1, b1 * b1);
      }
      const float rstd0 = __builtin_amdgcn_rsqf(fmaf(64.f, d0, q0) * (1.f / D) + 1e-5f), rstd1 = __builtin_amdgcn_rsqf(fmaf(64.f, d1, q1) * (1.f / D) + 1e-5f);
      if (lane == 0) *(f32x4*)(ldsC + 12) = f32x4{mean0, rstd0, mean1, rstd1};
      last_wave(ldsC, lane);
      f32x4 z = {0.f, 0.f, 0.f, 0.f}, cs = {0.f, 0.f, 0.f, 0.f};
#pragma unroll
      for (int ww = 0; ww < NWAVE; ++ww) { z += *(const f32x4*)(ldsY + ww * 512 + 4 * lane); cs += *(const f32x4*)(ldsY + ww * 512 + 256 + 4 * (lane & 31)); }
      st_dense(rs, s1off(h, r8, 0) + lo16, (z - cs * (lane < 32 ? mean0 : mean1)) * (lane < 32 ? rstd0 : rstd1), tagA, tagB);
    }
  }
    if constexpr (p == 2) {
      wait_all(ldsC, NWAVE + 1);
      tagA = next_tag(__builtin_amdgcn_readfirstlane(ldsC[8])); tagB = __builtin_amdgcn_readfirstlane(ldsC[9]) + 0x9E3779B9u;
      asm volatile("" :: "v"(pf0), "v"(pf1), "v"(pfc));
      if (has_f1) {
        const f32x4 ln = *(const f32x4*)(ldsC + 12);
        const float n00 = (lo(xs0) - ln[0]) * ln[1], n01 = (hi(xs0) - ln[0]) * ln[1], n10 = (lo(xs1) - ln[2]) * ln[3], n11 = (hi(xs1) - ln[2]) * ln[3];
        float t[8];
#pragma unroll
        for (int k = 0; k < 4; ++k) {
          const float al = lo(ffw[0][k]), ah = hi(ffw[0][k]), bl = lo(ffw[1][k]), bh = hi(ffw[1][k]);
          t[2 * k] = fold32(fmaf(n01, bl, n00 * al), fmaf(n11, bl, n10 * al)); t[2 * k + 1] = fold32(fmaf(n01, bh, n00 * ah), fmaf(n11, bh, n10 * ah));
        }
        *(f32x4*)(ldsY + 4096 + w * 256 + (g >> 1) * 128 + 8 * c + 4 * (g & 1)) = f32x4{fold16(t[0], t[4]), fold16(t[1], t[5]), fold16(t[2], t[6]), fold16(t[3], t[7])};
        last_wave(ldsC + 2, lane);
      }
    }
    if constexpr (p == 5) {
      int gq = g; asm volatile("" : "+v"(gq));
      const int fbs = (gq >> 1) * 11 + (cid >> 2), hs = (fbs / 3) * 4 + fbs % 3;
      fuoff = has_f2 ? s1off(hs, w, 1) + ((g & 1) * 32 + 8 * (cid & 3) + (c & 7)) * 16 : OOB;
      asm volatile("" : "+v"(fuoff));
    }
    if constexpr (p == 4) {
      if (has_f1 && w == 0) {
        wait_all(ldsC + 2);
        f32x4 z = {0.f, 0.f, 0.f, 0.f};
#pragma unroll
        for (int ww = 0; ww < NWAVE; ++ww) z += *(const f32x4*)(ldsY + 4096 + ww * 256 + 4 * lane);
        st_dense(rs, s1off(h, r8, 1) + lo16, z, tagA, tagB);
      }
    }
    if constexpr (p == 6) {
      *(f32x4*)(ldsQ + w * 128 + 4 * (lane & 31)) = await_dense(qu, rs, qoff, tagA, tagB);
      last_wave(ldsC + 1, lane);
    }
    if constexpr (p == 8) {
      wait_all(ldsC + 1);
      f32x4 q0 = {0, 0, 0, 0}, q1 = {0, 0, 0, 0};
#pragma unroll
      for (int ww = 0; ww < NWAVE; ++ww) { q0 += *(const f32x4*)(ldsQ + ww * 128 + 8 * c); q1 += *(const f32x4*)(ldsQ + ww * 128 + 8 * c + 4); }
      qp[0] = pk_bf16(q0[0], q0[1]); qp[1] = pk_bf16(q0[2], q0[3]); qp[2] = pk_bf16(q1[0], q1[1]); qp[3] = pk_bf16(q1[2], q1[3]);
      qk(IC<0>{});
    }
    if constexpr (p == 9) { qk(IC<1>{}); qk(IC<2>{}); qk(IC<3>{}); }
    if constexpr (p == 10) { qk(IC<4>{}); qk(IC<5>{}); }
    if constexpr (p >= 10 && p < GROUPS + 4) qk(IC<p - 4>{});
    if constexpr (p == 10) {
      if (has_f2) {
        *(f32x4*)(ldsF + w * 128 + g * 32 + 4 * (c & 7)) = await_dense(fu, rs, fuoff, tagA, tagB);
        last_wave(ldsC + 5, lane);
      }
    }
    if constexpr (p == 12) {
      if (has_f2) {
        wait_all(ldsC + 5);
        float gs = 0.f, us = 0.f;
        const int hx = ((lane >> 2) & 1) * 32 + 4 * w + (lane & 3);
#pragma unroll
        for (int ww = 0; ww < NWAVE; ++ww) { gs += ldsF[ww * 128 + hx]; us += ldsF[ww * 128 + 64 + hx]; }
        const float hv = gelu_tanh(gs) * us;
        float dd[16];
#pragma unroll
        for (int k = 0; k < 16; ++k) dd[k] = 0.f;
#pragma unroll
        for (int r = 0; r < 4; ++r) {
          const float h0 = __builtin_bit_cast(float, __builtin_amdgcn_readlane(__builtin_bit_cast(int, hv), r));
          const float h1 = __builtin_bit_cast(float, __builtin_amdgcn_readlane(__builtin_bit_cast(int, hv), 4 + r));
#pragma unroll
          for (int k = 0; k < 4; ++k) {
            const float wl = lo(ffw[r][k]), wh = hi(ffw[r][k]);
            dd[2 * k] = fmaf(h0, wl, dd[2 * k]); dd[2 * k + 1] = fmaf(h0, wh, dd[2 * k + 1]);
            dd[8 + 2 * k] = fmaf(h1, wl, dd[8 + 2 * k]); dd[8 + 2 * k + 1] = fmaf(h1, wh, dd[8 + 2 * k + 1]);
          }
        }
        *(f32x4*)(ldsY + w * 1024 + 8 * lane) = f32x4{dd[0], dd[1], dd[2], dd[3]};
        *(f32x4*)(ldsY + w * 1024 + 8 * lane + 4) = f32x4{dd[4], dd[5], dd[6], dd[7]};
        *(f32x4*)(ldsY + w * 1024 + 512 + 8 * lane) = f32x4{dd[8], dd[9], dd[10], dd[11]};
        *(f32x4*)(ldsY + w * 1024 + 512 + 8 * lane + 4) = f32x4{dd[12], dd[13], dd[14], dd[15]};
        last_wave(ldsC + 3, lane);
      }
    }
    if constexpr (p == 28) {
      if (has_f2 && w < 4) {
        wait_all(ldsC + 3);
        f32x4 dv = {0.f, 0.f, 0.f, 0.f};
#pragma unroll
        for (int ww = 0; ww < NWAVE; ++ww) dv += *(const f32x4*)(ldsY + ww * 1024 + 4 * tid);
        st_dense(rs, WS_DP + (cid * 256 + tid) * 16, dv, tagA, tagB);
      }
    }
    if constexpr (p == GROUPS + 3) {
      float x = fmaxf(fmaxf(sp[0], sp[1]), fmaxf(sp[2], sp[3]));
      x = fmaxf(x, dpp<0x128>(x)); x = fmaxf(x, dpp<0x124>(x)); x = fmaxf(x, dpp<0x122>(x)); x = fmaxf(x, dpp<0x121>(x));
      mw = xrow_max(x) * C2;
#pragma unroll
      for (int i = 0; i < 4; ++i) sp[i] = __builtin_amdgcn_exp2f(fmaf(sp[i], C2, -mw));
      const float lw = xrow_sum(row16_sum((sp[0] + sp[2]) + (sp[1] + sp[3])));
      if (lane == 0) { ldsM[slot0 & 7] = mw; ldsM[8 + (slot0 & 7)] = lw; }
      last_wave(ldsC + 4, lane);
    }
    if constexpr (p == 21) {
      if (w == 0) {
        wait_all(ldsC + 4);
        const float mi = ldsM[lane & 7], li = ldsM[8 + (lane & 7)];
        float M = fmaxf(mi, dpp<0x124>(mi)); M = fmaxf(M, dpp<0x122>(M)); M = fmaxf(M, dpp<0x121>(M));
        float L = __builtin_amdgcn_exp2f(mi - M) * li; L += dpp<0x124>(L); L += dpp<0x122>(L); L += dpp<0x121>(L);
        if (lane == 0) st_unit(rs, WS_ML + pid * 16, M, L, tagA, tagB);
      }
    }
    if constexpr (p == 31) {
      await_unit(mu, rs, muoff, tagA, tagB);
      const float mi = __uint_as_float(mu[0]);
      float M = fmaxf(mi, dpp<0xB1>(mi)); M = fmaxf(M, dpp<0x4E>(M));
      float L = __builtin_amdgcn_exp2f(mi - M) * __uint_as_float(mu[2]); L += dpp<0xB1>(L); L += dpp<0x4E>(L);
      mw = __builtin_amdgcn_exp2f(mw - M) * __builtin_amdgcn_rcpf(L);
      ysto = g ? OOB : WS_Y + (pid * 128 + 16 * w + c) * 16;
      yoff = WS_Y + ((bc * 128 + gp) * 128 + cu0 + qd) * 16;
      asm volatile("" : "+v"(mw), "+v"(ysto), "+v"(yoff));
    }
    if constexpr (p >= 20 && p < 28) pv(IC<p - 20>{});
    if constexpr (p == 28 || p == 29) { pv(IC<2 * p - 48>{}); pv(IC<2 * p - 47>{}); }
    if constexpr (p == 30) pv(IC<12>{});
    if constexpr (p == 31) { pv(IC<13>{}); pv(IC<14>{}); pv(IC<15>{}); }
    if constexpr (p == 30) {
#pragma unroll
      for (int i = 0; i < 16; ++i) {
        const v2u r0 = wor[2 * i], r1 = wor[2 * i + 1];
        wb[0][i >> 2][i & 3] = __builtin_amdgcn_perm(r1[0], r0[0], 0x05040100u); wb[1][i >> 2][i & 3] = __builtin_amdgcn_perm(r1[0], r0[0], 0x07060302u);
        wb[2][i >> 2][i & 3] = __builtin_amdgcn_perm(r1[1], r0[1], 0x05040100u); wb[3][i >> 2][i & 3] = __builtin_amdgcn_perm(r1[1], r0[1], 0x07060302u);
      }
#pragma unroll
      for (int i = 0; i < 4; ++i) asm volatile("" : "+v"(wb[i][0]), "+v"(wb[i][1]), "+v"(wb[i][2]), "+v"(wb[i][3]));
    }
    SB();
  };
  static_for<2 * GROUPS>(point);

  {
    const f32x2 o2 = (bs[0] + bs[1]) * mw;
    const unsigned oh = pk_bf16(o2[0], o2[1]), ol = pk_bf16(o2[0] - lo(oh), o2[1] - hi(oh));
    const int ar = (slot0 & 7) * 68 + 4 * c + g;
    ldsA[ar] = oh; ldsA[8 * 68 + ar] = ol;
    if (is_fin && w < 3) {
      f32x4 e = await_dense(e0, rs, dpoff, tagA, tagB);
      if (gp >= NFFN) e = f32x4{0.f, 0.f, 0.f, 0.f};
      const float r = fold4(e[0], e[1], e[2], e[3]);
      if (c < 4) ldsF[w * 16 + 4 * c + g] = r;
    }
  }
  __syncthreads();
  {
    v4u af[4];
#pragma unroll
    for (int ks = 0; ks < 4; ++ks) af[ks] = *(const v4u*)(ldsA + c * 68 + 16 * ks + 4 * g);
    SB();
    f32x4 d[4];
#pragma unroll
    for (int t = 0; t < 4; ++t) d[t] = f32x4{0.f, 0.f, 0.f, 0.f};
#pragma unroll
    for (int ks = 0; ks < 4; ++ks)
#pragma unroll
      for (int t = 0; t < 4; ++t) d[t] = __builtin_amdgcn_mfma_f32_16x16x32_bf16(__builtin_bit_cast(bf16x8, af[ks]), __builtin_bit_cast(bf16x8, wb[t][ks]), d[t], 0, 0, 0);
    float y[4];
#pragma unroll
    for (int t = 0; t < 4; ++t) y[t] = (d[t][0] + d[t][1]) + (d[t][2] + d[t][3]);
    float u0 = fold32(y[0], y[2]), u1 = fold32(y[1], y[3]);
    u0 = fold16(u0, u0); u1 = fold16(u1, u1);
    const auto r0 = __builtin_amdgcn_permlane32_swap(__float_as_uint(u0), __float_as_uint(u0), false, false), r1 = __builtin_amdgcn_permlane32_swap(__float_as_uint(u1), __float_as_uint(u1), false, false);
    st_dense(rs, ysto, f32x4{__uint_as_float(r0[0]), __uint_as_float(r1[0]), __uint_as_float(r0[1]), __uint_as_float(r1[1])}, tagA, tagB);
  }

  if (is_fin) {
    __builtin_amdgcn_s_barrier();
    const float dv = ldsF[c] + ldsF[16 + c] + ldsF[32 + c];
    float gte = dv * __builtin_amdgcn_rcpf(1.f + __expf(-dv));
    asm volatile("" : "+v"(gte));
    __builtin_amdgcn_s_sleep(11);
    v4u y0;
    bool ok = false;
#pragma unroll 1
    for (unsigned spin = 0; spin < SPIN_CAP; ++spin) {
      asm volatile("" ::: "memory");
      if (!ok) { y0 = ld_unit(rs, yoff); ok = ok_dense(y0, tagA, tagB); }
      if (__all(ok)) break;
      __builtin_amdgcn_s_sleep(1);
    }
    const f32x4 yv = __builtin_bit_cast(f32x4, y0 & 0xfffffc00u);
    const float r = fold4(yv[0], yv[1], yv[2], yv[3]);
    if (c < 4) ldsF[128 + w * 16 + 4 * c + g] = r;
    __syncthreads();
    if (tid < 16) {
      float sv = 0.f;
#pragma unroll
      for (int ww = 0; ww < NWAVE; ++ww) sv += ldsF[128 + ww * 16 + tid];
      const float o = __uint_as_float((unsigned)xo << 16) + sv * gte;
      const __bf16 ob = (__bf16)o;
      __builtin_amdgcn_raw_buffer_store_b16(__builtin_bit_cast(unsigned short, ob), RSRC(KAP(8), 2 * D * 2), ocol * 2, 0, 0);
      if (cid == 0 && tid == 0)
        __hip_atomic_store((GAS unsigned long long*)(pws + WS_EPOCH), ((unsigned long long)tagB << 32) | tagA, __ATOMIC_RELAXED, __HIP_MEMORY_SCOPE_AGENT);
    }
  }
  asm volatile(".Lk_all_end:");
}

extern "C" void kernel_launch(void* const* d_in, const int* in_sizes, int n_in, void* d_out, int out_size, void* d_ws, size_t ws_size,
                              hipStream_t stream) {
  Args a;
  a.x = (const unsigned short*)d_in[0];
  a.k = (const unsigned short*)d_in[1];
  a.v = (const unsigned short*)d_in[2];
  a.wq = (const unsigned short*)d_in[3];
  a.wo = (const unsigned short*)d_in[4];
  a.wg = (const unsigned short*)d_in[6];
  a.wu = (const unsigned short*)d_in[7];
  a.wd = (const unsigned short*)d_in[8];
  a.out = (unsigned short*)d_out;
  a.ws = (unsigned char*)d_ws;
  hipLaunchKernelGGL(k_all, dim3(256), dim3(NTHR), 0, stream, a);
}
```

```cpp
#include <hip/hip_runtime.h>
#include <hip/hip_bf16.h>
#include <stdint.h>

typedef unsigned v4u __attribute__((ext_vector_type(4)));
typedef float f32x4 __attribute__((ext_vector_type(4)));
typedef float f32x2 __attribute__((ext_vector_type(2)));
typedef __bf16 bf16x2 __attribute__((ext_vector_type(2)));
typedef __bf16 bf16x8 __attribute__((ext_vector_type(8)));
typedef unsigned v16u __attribute__((ext_vector_type(16)));
typedef unsigned v2u __attribute__((ext_vector_type(2)));
#define GAS __attribute__((address_space(1)))

constexpr int D = 512, H = 32, SK = 8192, DH = 128, F = 1376;
constexpr int NSPLIT = 4, KEYS_PER_WG = SK / NSPLIT;
constexpr int NWAVE = 8, NTHR = NWAVE * 64;
constexpr int NFFN = F / 32;
constexpr int U = 4;
constexpr int GROUPS = KEYS_PER_WG / NWAVE / (4 * U);
#define TP_ 1720
#define T0_ 2350
#define EX1_ 78
constexpr unsigned step_of(int p) { return TP_ + (p < 9 ? EX1_ : 0); }
constexpr int LATE_PRIO = 400;
constexpr int won(int p) { return p <= 1 ? 0 : p >= 28 ? 32 : (p - 1) * 32 / 27; }
static_assert(TP_ % 8 == 0, "a slot is TP / 8");

constexpr int WS_EPOCH = 0;
constexpr int WS_ML = 4096;
constexpr int WS_Y = WS_ML + 256 * 16;
constexpr int WS_DP = WS_Y + 256 * 128 * 16;
constexpr int WS_S1 = WS_DP + NFFN * 256 * 16;
constexpr int WS_END = WS_S1 + 32 * 8 * 2 * 64 * 16;
constexpr int OOB = 0x7ffffff0;
constexpr unsigned SPIN_CAP = 400000;

#define SB() __builtin_amdgcn_sched_barrier(0)

__device__ __forceinline__ float dot2(unsigned k, unsigned q, float acc) {
  return __builtin_amdgcn_fdot2_f32_bf16(__builtin_bit_cast(bf16x2, k), __builtin_bit_cast(bf16x2, q), acc, false);
}
template <int CTRL> __device__ __forceinline__ float dpp(float x) {
  return __builtin_bit_cast(float, __builtin_amdgcn_mov_dpp(__builtin_bit_cast(int, x), CTRL, 0xf, 0xf, true));
}
__device__ __forceinline__ float row16_sum(float x) {
  x += dpp<0x128>(x);
  x += dpp<0x124>(x);
  x += dpp<0x122>(x);
  x += dpp<0x121>(x);
  return x;
}
__device__ __forceinline__ float fold32(float a, float b) {
  auto t = __builtin_amdgcn_permlane32_swap(__float_as_uint(a), __float_as_uint(b), false, false);
  return __uint_as_float(t[0]) + __uint_as_float(t[1]);
}
__device__ __forceinline__ float fold16(float a, float b) {
  auto t = __builtin_amdgcn_permlane16_swap(__float_as_uint(a), __float_as_uint(b), false, false);
  return __uint_as_float(t[0]) + __uint_as_float(t[1]);
}
__device__ __forceinline__ float xrow_sum(float x) { x = fold16(x, x); return fold32(x, x); }
__device__ __forceinline__ float xrow_max(float x) {
  auto s = __builtin_amdgcn_permlane16_swap(__float_as_uint(x), __float_as_uint(x), false, false);
  x = fmaxf(__uint_as_float(s[0]), __uint_as_float(s[1]));
  auto t = __builtin_amdgcn_permlane32_swap(__float_as_uint(x), __float_as_uint(x), false, false);
  return fmaxf(__uint_as_float(t[0]), __uint_as_float(t[1]));
}
__device__ __forceinline__ f32x2 fold8(const float (&v)[8]) {
  const float u0 = fold32(v[0], v[4]), u1 = fold32(v[1], v[5]), u2 = fold32(v[2], v[6]), u3 = fold32(v[3], v[7]);
  return f32x2{fold16(u0, u2), fold16(u1, u3)};
}
__device__ __forceinline__ float fold4(float v0, float v1, float v2, float v3) {
  float r = fold16(fold32(v0, v2), fold32(v1, v3));
  r += dpp<0x124>(r); r += dpp<0x128>(r);
  return r;
}
__device__ __forceinline__ float lo(unsigned u) { return __uint_as_float(u << 16); }
__device__ __forceinline__ float hi(unsigned u) { return __uint_as_float(u & 0xffff0000u); }
__device__ __forceinline__ unsigned pk_bf16(float a, float b) {
  bf16x2 r = {(__bf16)a, (__bf16)b};
  return __builtin_bit_cast(unsigned, r);
}
__device__ __forceinline__ unsigned sel4(v4u v, int g) { const unsigned a = (g & 1) ? v[1] : v[0], b = (g & 1) ? v[3] : v[2]; return (g & 2) ? b : a; }
__device__ __forceinline__ int s1off(int hg, int r, int k) { return WS_S1 + (hg * 8 + ((r + hg) & 7)) * 2048 + ((k ^ (hg >> 3)) & 1) * 1024; }
__device__ __forceinline__ float gelu_tanh(float x) {
  const float u = 0.7978845608028654f * (x + 0.044715f * x * x * x);
  const float e = __expf(2.f * u);
  const float t = 1.f - 2.f * __builtin_amdgcn_rcpf(e + 1.f);
  return 0.5f * x * (1.f + t);
}
__device__ __forceinline__ void pace_wait(unsigned& due16, unsigned step16) {
  asm volatile("1:\n\ts_memrealtime s[90:91]\n\ts_waitcnt lgkmcnt(0)\n\ts_lshl_b32 s90, s90, 4\n\ts_sub_u32 s91, s90, %0\n\ts_cmp_ge_i32 s91, 0\n\ts_cbranch_scc1 2f\n\ts_sleep 2\n\ts_branch 1b\n"
               "2:\n\ts_add_u32 %0, %0, %1\n\ts_setprio 0\n\ts_cmp_lt_i32 s91, %2\n\ts_cbranch_scc1 3f\n\ts_setprio 2\n3:"
               : "+s"(due16) : "s"(step16), "i"(LATE_PRIO) : "s90", "s91", "scc");
}
__device__ __forceinline__ unsigned now16() { return (unsigned)__builtin_amdgcn_s_memrealtime() << 4; }
__device__ __forceinline__ unsigned next_tag(unsigned e) { return ((e + 1u) & 1023u) | ((e + 1024u) & (1023u << 10)) | ((e + (1u << 20)) & 0xfff00000u); }
constexpr int AUX_SC1 = 16;
__device__ __forceinline__ void st_unit(__amdgpu_buffer_rsrc_t rs, int off, float v0, float v1, unsigned ta, unsigned tb) {
  __builtin_amdgcn_raw_buffer_store_b128(v4u{__float_as_uint(v0), ta, __float_as_uint(v1), tb}, rs, off, 0, AUX_SC1);
}
__device__ __forceinline__ v4u ld_unit(__amdgpu_buffer_rsrc_t rs, int off) { return __builtin_amdgcn_raw_buffer_load_b128(rs, off, 0, AUX_SC1); }
__device__ __forceinline__ unsigned enc22(float v, unsigned t) { return ((__float_as_uint(v) + 0x200u) & 0xfffffc00u) | t; }
__device__ __forceinline__ void st_dense(__amdgpu_buffer_rsrc_t rs, int off, f32x4 v, unsigned ta, unsigned tb) {
  __builtin_amdgcn_raw_buffer_store_b128(v4u{enc22(v[0], ta & 1023u), enc22(v[1], (ta >> 10) & 1023u), enc22(v[2], (ta >> 20) & 1023u), enc22(v[3], tb & 1023u)}, rs, off, 0, AUX_SC1);
}
__device__ __forceinline__ bool ok_dense(v4u u, unsigned ta, unsigned tb) { return ((((u[0] ^ ta) | (u[1] ^ (ta >> 10))) | ((u[2] ^ (ta >> 20)) | (u[3] ^ tb))) & 1023u) == 0u; }
__device__ __forceinline__ f32x4 await_dense(v4u u, __amdgpu_buffer_rsrc_t rs, int off, unsigned ta, unsigned tb) {
  if (!__all(ok_dense(u, ta, tb))) {
#pragma unroll 1
    for (unsigned spin = 0; spin < SPIN_CAP; ++spin) {
      __builtin_amdgcn_s_sleep(1);
      asm volatile("" ::: "memory");
      u = ld_unit(rs, off);
      if (__all(ok_dense(u, ta, tb))) break;
    }
  }
  return __builtin_bit_cast(f32x4, u & 0xfffffc00u);
}
__device__ __forceinline__ void await_unit(v4u& u, __amdgpu_buffer_rsrc_t rs, int off, unsigned ta, unsigned tb) {
  if (!__all(((u[1] ^ ta) | (u[3] ^ tb)) == 0u)) {
#pragma unroll 1
    for (unsigned spin = 0; spin < SPIN_CAP; ++spin) {
      __builtin_amdgcn_s_sleep(1);
      asm volatile("" ::: "memory");
      u = ld_unit(rs, off);
      if (__all(((u[1] ^ ta) | (u[3] ^ tb)) == 0u)) break;
    }
  }
}

__device__ __forceinline__ bool last_wave(unsigned* cnt, int lane) {
  asm volatile("s_waitcnt lgkmcnt(0)" ::: "memory");
  unsigned old = 0;
  if (lane == 0) old = __hip_atomic_fetch_add(cnt, 1u, __ATOMIC_RELAXED, __HIP_MEMORY_SCOPE_WORKGROUP);
  old = __builtin_amdgcn_readfirstlane(old);
  asm volatile("" ::: "memory");
  return old == NWAVE - 1;
}

__device__ __forceinline__ void wait_all(unsigned* cnt, unsigned n = NWAVE) {
  while (__builtin_amdgcn_readfirstlane(__hip_atomic_load(cnt, __ATOMIC_RELAXED, __HIP_MEMORY_SCOPE_WORKGROUP)) < n) __builtin_amdgcn_s_sleep(1);
  asm volatile("" ::: "memory");
}
template <int I> struct IC { static constexpr int value = I; };
template <int N, int I = 0, typename Fn> __device__ __forceinline__ void static_for(Fn& fn) { if constexpr (I < N) { fn(IC<I>{}); static_for<N, I + 1>(fn); } }

struct Args {
  const unsigned short* x; const unsigned short* wq;
  const unsigned short* k; const unsigned short* v; const unsigned short* wo; const unsigned short* wg;
  const unsigned short* wu; const unsigned short* wd; unsigned short* out; unsigned char* ws;
};

__global__ void __launch_bounds__(NTHR, 2) k_all(Args a) {
  __shared__ __attribute__((aligned(16))) float lds[8 * 1024 + 16 * 68 + 16 + 8 * 128 + 8 * 128 + 32 + 16];
  float* ldsY = lds;
  unsigned* ldsA = (unsigned*)(lds + 8 * 1024);
  float* ldsM = lds + 8 * 1024 + 16 * 68;
  float* ldsF = ldsM + 16;
  float* ldsQ = ldsF + 8 * 128;
  float* ldsS = ldsQ + 8 * 128;
  unsigned* ldsC = (unsigned*)(ldsS + 32);
  const int bid = blockIdx.x;
  const int xcd = bid & 7, j = bid >> 3;
  const int tid = threadIdx.x, lane = tid & 63, w = __builtin_amdgcn_readfirstlane(tid >> 6), lo16 = lane * 16;
  v16u ka; v4u kb; unsigned long long tptr, pc0; unsigned csz;
  asm volatile("s_load_dwordx16 %0, %5, 0x0\n\ts_load_dwordx4 %1, %5, 0x40\n\ts_load_dwordx2 %2, %5, %6 offset:0x10\n\t"
               "s_getpc_b64 %3\n.Lk_all_pc0:\n\ts_mov_b32 %4, (.Lk_all_end-.Lk_all_pc0)&0xfffffff0"
               : "=&s"(ka), "=&s"(kb), "=&s"(tptr), "=&s"(pc0), "=&s"(csz) : "s"(__builtin_amdgcn_kernarg_segment_ptr()), "s"(8 * w));
#define RSRC(p_, n_) __builtin_amdgcn_make_buffer_rsrc((void*)(p_), 0, (n_), 0x00020000)
  const v4u pfc = __builtin_amdgcn_raw_buffer_load_b128(RSRC(pc0 & ~127ull, w == 4 ? csz : 0u), lo16, j * 1024, 0);
  ldsC[tid & 15] = 0u;
  SB();
  const int h = xcd * 4 + (j >> 3), b = (j >> 2) & 1, s = j & 3;
  const int g = lane >> 4, c = lane & 15;
  const int r8 = j & 7;
  int qwo = (2 * g * H * DH + 8 * c) * 2, xso = (lane >> 5) * D * 2 + (lane & 31) * 4, qso = ((64 * r8 + 8 * w) * H * DH + h * DH) * 2;
  unsigned tof = ((unsigned)(b * H + h) * ((0x03u >> w) & 1u) << 21) + ((unsigned)h * ((0x04u >> w) & 1u) << 17);
  asm volatile("" : "+v"(qwo), "+v"(xso), "+v"(tof));
  asm volatile("s_waitcnt lgkmcnt(0)\n\ts_barrier" : "+s"(ka), "+s"(kb), "+s"(tptr), "+s"(qso) :: "memory");
#define KAP(i) ((i) < 8 ? (unsigned long long)ka[2 * (i) % 16] | ((unsigned long long)ka[(2 * (i) + 1) % 16] << 32) : (unsigned long long)kb[2 * (i) % 4] | ((unsigned long long)kb[(2 * (i) + 1) % 4] << 32))
  const unsigned long long px = KAP(0), pws = KAP(9);
  const unsigned long long ep = __hip_atomic_load((const GAS unsigned long long*)(tptr + tof), __ATOMIC_RELAXED, __HIP_MEMORY_SCOPE_AGENT);
  SB();
  const unsigned xst = __builtin_amdgcn_raw_buffer_load_b32(RSRC(px, 2 * D * 2), xso, 128 * w, 0);
  SB();
  const __amdgpu_buffer_rsrc_t rq = RSRC(KAP(1), D * H * DH * 2);
  const v4u sw0 = __builtin_amdgcn_raw_buffer_load_b128(rq, qwo, qso, 2), sw1 = __builtin_amdgcn_raw_buffer_load_b128(rq, qwo, qso + H * DH * 2, 2);
  v4u xq0, xq1;
  asm volatile("s_load_dwordx4 %0, %2, 0x0\n\ts_load_dwordx4 %1, %2, 0x400" : "=&s"(xq0), "=&s"(xq1) : "s"(px + (64 * r8 + 8 * w) * 2));
  SB();
  const __amdgpu_buffer_rsrc_t rw = RSRC(KAP(4) + (size_t)h * DH * D * 2, DH * D * 2);
  const v4u pf0 = __builtin_amdgcn_raw_buffer_load_b128(rw, lo16, (16 * r8 + 2 * w) * 1024, 0), pf1 = __builtin_amdgcn_raw_buffer_load_b128(rw, lo16 + 1024, (16 * r8 + 2 * w) * 1024, 0);
  SB();
  const int fb = xcd * 3 + (j >> 3);
  const bool has_f1 = (j < 24) && (fb < 22);
  const bool is_fin = j >= 24;
  const int cid = xcd * 8 + (j - 24);
  const bool has_f2 = is_fin && cid < NFFN;
  const int row0 = 64 * r8 + 8 * w + 2 * g;
  const int fcol = 128 * (fb < 11 ? fb : fb - 11) + 8 * c;
  int fx1 = (row0 * F + fcol) * 2, fx2 = (32 * cid + 4 * w) * D * 2 + lo16;
  asm volatile("" : "+v"(fx1), "+v"(fx2));
  const int fxo = has_f1 ? (fcol < F ? fx1 : OOB - 4 * F * 2) : fx2, fxo2 = has_f1 ? OOB - 4096 : fx2, fst = has_f1 ? F * 2 : D * 2;
  unsigned f_lo = fb < 11 ? ka[10] : ka[12], f_hi = fb < 11 ? ka[11] : ka[13];
  asm volatile("" : "+s"(f_lo), "+s"(f_hi));
  const __amdgpu_buffer_rsrc_t rf = RSRC((unsigned long long)(has_f1 ? f_lo : ka[14]) | ((unsigned long long)(has_f1 ? f_hi : ka[15]) << 32), (has_f1 || has_f2) ? D * F * 2 : 0);
  v4u ffw[4];
  ffw[0] = __builtin_amdgcn_raw_buffer_load_b128(rf, fxo, 0, 2);
  ffw[1] = __builtin_amdgcn_raw_buffer_load_b128(rf, fxo, fst, 2);
  ffw[2] = __builtin_amdgcn_raw_buffer_load_b128(rf, fxo2 + 2048, 0, 2);
  ffw[3] = __builtin_amdgcn_raw_buffer_load_b128(rf, fxo2 + 3072, 0, 2);
  SB();
  if (w == 0) {
    asm volatile("" :: "v"(ep));
    __hip_atomic_store(ldsC + 10, now16() | 1u, __ATOMIC_RELAXED, __HIP_MEMORY_SCOPE_WORKGROUP);
  }
  SB();
  const __amdgpu_buffer_rsrc_t rs = RSRC(pws, WS_END);

  const __amdgpu_buffer_rsrc_t rk = RSRC(KAP(2) + (size_t)(b * H + h) * SK * DH * 2, SK * DH * 2);
  const __amdgpu_buffer_rsrc_t rv = RSRC(KAP(3) + (size_t)(b * H + h) * SK * DH * 2, SK * DH * 2);
#define GOFF(gi_) (((gi_) & 1) * ((1 << 20) | (1 << 12)) | ((gi_) & 2) * ((1 << 18) | (1 << 13)) | (((gi_) & 4) << 16) | (((gi_) & 8) << 14))
  v4u rg[2 * GROUPS][U];

  constexpr float C2 = 0.08838834764831845f * 1.4426950408889634f;
  float sp[4] = {0.f, 0.f, 0.f, 0.f};
  float mw = 0.f;
  f32x2 bs[8];
  unsigned tagA, tagB, qp[4], slot0;
  v2u wor[32]; v4u wb[4][4], fu, qu, mu, e0; unsigned short xo;
  unsigned xs0, xs1;
  int fuoff, ysto, yoff;
  const int wvo = g * 8192 + w * 128 + c * 8;
  const int qoff = s1off(h, w, 0) + (b * 32 + (lane & 31)) * 16;
  const int pid = (b * H + h) * NSPLIT + s, muoff = WS_ML + ((pid & ~3) + (lane & 3)) * 16;
  const int bc = (cid >> 5) & 1, cu0 = (cid & 31) * 4;
  const int gp = tid >> 2, qd = tid & 3, ocol = bc * D + 4 * cu0 + c;
  const int dpoff = (is_fin && w < 3) ? WS_DP + ((gp < NFFN ? gp : NFFN - 1) * 256 + bc * 128 + cu0 + qd) * 16 : OOB;

  auto qk = [&](auto gic) __attribute__((always_inline)) {
    constexpr int gi = decltype(gic)::value;
    float d[U];
#pragma unroll
    for (int u = 0; u < U; ++u) d[u] = dot2(rg[gi][u][3], qp[3], dot2(rg[gi][u][2], qp[2], dot2(rg[gi][u][1], qp[1], dot2(rg[gi][u][0], qp[0], 0.f))));
    const bool o1 = c & 1, o2 = c & 2;
    const float x0 = (o1 ? d[1] : d[0]) + dpp<0xB1>(o1 ? d[0] : d[1]), x1 = (o1 ? d[3] : d[2]) + dpp<0xB1>(o1 ? d[2] : d[3]);
    float x = (o2 ? x1 : x0) + dpp<0x4E>(o2 ? x0 : x1);
    x += dpp<0x128>(x); x += dpp<0x124>(x);
    sp[gi / 4] = __builtin_bit_cast(float, __builtin_amdgcn_update_dpp(__builtin_bit_cast(int, sp[gi / 4]), __builtin_bit_cast(int, x), 0xE4, 0xf, 1 << (gi % 4), false));
    asm volatile("" : "+v"(sp[gi / 4]));
    SB();
  };
  auto pv = [&](auto gic) __attribute__((always_inline)) {
    constexpr int gi = decltype(gic)::value;
    const float p[4] = {dpp<0x150 + 4 * (gi % 4)>(sp[gi / 4]), dpp<0x151 + 4 * (gi % 4)>(sp[gi / 4]), dpp<0x152 + 4 * (gi % 4)>(sp[gi / 4]), dpp<0x153 + 4 * (gi % 4)>(sp[gi / 4])};
    float x[8];
#pragma unroll
    for (int k = 0; k < 4; ++k) { x[2 * k] = p[0] * lo(rg[GROUPS + gi][0][k]); x[2 * k + 1] = p[0] * hi(rg[GROUPS + gi][0][k]); }
#pragma unroll
    for (int u = 1; u < U; ++u) {
#pragma unroll
      for (int k = 0; k < 4; ++k) { x[2 * k] = fmaf(p[u], lo(rg[GROUPS + gi][u][k]), x[2 * k]); x[2 * k + 1] = fmaf(p[u], hi(rg[GROUPS + gi][u][k]), x[2 * k + 1]); }
    }
    const f32x2 y = fold8(x);
    if constexpr (gi < 8) bs[gi] = y; else bs[gi - 8] += y;
    if constexpr (gi >= 12) bs[gi - 12] += bs[gi - 8];
    if constexpr (gi >= 14) bs[gi - 14] += bs[gi - 12];
    asm volatile("" : "+v"(bs[gi >= 14 ? gi - 14 : gi >= 12 ? gi - 12 : gi % 8]));
    SB();
  };

  {
    unsigned tk;
    while ((tk = __builtin_amdgcn_readfirstlane(__hip_atomic_load(ldsC + 10, __ATOMIC_RELAXED, __HIP_MEMORY_SCOPE_WORKGROUP))) == 0u) __builtin_amdgcn_s_sleep(1);
    slot0 = __builtin_amdgcn_readfirstlane((__builtin_amdgcn_readfirstlane(tk) + (unsigned)T0_) / (unsigned)(TP_ / 8) + (unsigned)w);
  }
  const int kvo = ((slot0 & 1) << 13) | ((slot0 & 6) << 14) | ((s & 1) << 12) | ((s >> 1) << 14);
  unsigned due = __builtin_amdgcn_readfirstlane(slot0 * (unsigned)(TP_ / 8));

  auto point = [&](auto pc) __attribute__((always_inline)) {
    constexpr int p = decltype(pc)::value;
    int so = kvo ^ GOFF((slot0 >> 3) + p);
    asm volatile("" : "+s"(so));
    pace_wait(due, step_of(p));
    SB();
#pragma unroll
    for (int u = 0; u < U; ++u) rg[p][u] = __builtin_amdgcn_raw_buffer_load_b128(p < GROUPS ? rk : rv, lo16 + u * 1024, so, 2);
    SB();
    if constexpr (p == 4) qu = ld_unit(rs, qoff);
    if constexpr (p == 7) fu = ld_unit(rs, fuoff);
    if constexpr (p == 29) mu = ld_unit(rs, muoff);
#pragma unroll
    for (int i = won(p); i < won(p + 1); ++i) wor[i] = __builtin_amdgcn_raw_buffer_load_b64(rw, wvo, (32 * (i >> 3) + (i & 7)) * 1024, 0);
    if constexpr (p == 31) {
      e0 = ld_unit(rs, dpoff);
      xo = ((const GAS unsigned short*)px)[ocol];
    }
    SB();
    if constexpr (p == 0) {
      asm volatile("s_waitcnt lgkmcnt(0)" : "+s"(xq0), "+s"(xq1));
      xs0 = sel4(xq0, g); xs1 = sel4(xq1, g);
      float sm = row16_sum(lo(xst) + hi(xst)); sm = fold16(sm, sm) * (1.f / 64);
      const float d0 = lo(xst) - sm, d1 = hi(xst) - sm;
      float m2 = row16_sum(fmaf(d0, d0, d1 * d1)); m2 = fold16(m2, m2);
      if ((lane & 31) == 0) *(f32x2*)(ldsS + (lane >> 5) * 16 + 2 * w) = f32x2{sm, m2};
      float t[24];
#pragma unroll
      for (int k = 0; k < 4; ++k) {
        const float al = lo(sw0[k]), ah = hi(sw0[k]), bl = lo(sw1[k]), bh = hi(sw1[k]);
        t[2 * k] = fmaf(hi(xs0), bl, lo(xs0) * al); t[2 * k + 1] = fmaf(hi(xs0), bh, lo(xs0) * ah);
        t[8 + 2 * k] = fmaf(hi(xs1), bl, lo(xs1) * al); t[8 + 2 * k + 1] = fmaf(hi(xs1), bh, lo(xs1) * ah);
        t[16 + 2 * k] = al + bl; t[16 + 2 * k + 1] = ah + bh;
      }
#pragma unroll
      for (int k = 0; k < 8; ++k) t[k] = fold16(fold32(t[k], t[16 + k]), fold32(t[8 + k], t[16 + k]));
      *(f32x4*)(ldsY + w * 512 + g * 128 + 8 * c) = f32x4{t[0], t[1], t[2], t[3]};
      *(f32x4*)(ldsY + w * 512 + g * 128 + 8 * c + 4) = f32x4{t[4], t[5], t[6], t[7]};
      if (tid == 448) *(unsigned long long*)(ldsC + 8) = ep;
      asm volatile("" :: "v"(ep));
      if (last_wave(ldsC, lane)) {
        f32x4 st[8];
#pragma unroll
        for (int i = 0; i < 8; ++i) st[i] = *(const f32x4*)(ldsS + 4 * i);
        tagA = next_tag(__builtin_amdgcn_readfirstlane(ldsC[8])); tagB = __builtin_amdgcn_readfirstlane(ldsC[9]) + 0x9E3779B9u;
        const float mean0 = ((st[0][0] + st[0][2]) + (st[1][0] + st[1][2]) + (st[2][0] + st[2][2]) + (st[3][0] + st[3][2])) * 0.125f;
        const float mean1 = ((st[4][0] + st[4][2]) + (st[5][0] + st[5][2]) + (st[6][0] + st[6][2]) + (st[7][0] + st[7][2])) * 0.125f;
        float q0 = 0.f, q1 = 0.f, d0 = 0.f, d1 = 0.f;
#pragma unroll
        for (int i = 0; i < 4; ++i) {
          q0 += st[i][1] + st[i][3]; q1 += st[4 + i][1] + st[4 + i][3];
          const float a0 = st[i][0] - mean0, b0 = st[i][2] - mean0, a1 = st[4 + i][0] - mean1, b1 = st[4 + i][2] - mean1;
          d0 += fmaf(a0, a0, b0 * b0); d1 += fmaf(a1, a1, b1 * b1);
        }
        const float rstd0 = __builtin_amdgcn_rsqf(fmaf(64.f, d0, q0) * (1.f / D) + 1e-5f), rstd1 = __builtin_amdgcn_rsqf(fmaf(64.f, d1, q1) * (1.f / D) + 1e-5f);
        if (lane == 0) *(f32x4*)(ldsC + 12) = f32x4{mean0, rstd0, mean1, rstd1};
        last_wave(ldsC, lane);
        f32x4 z = {0.f, 0.f, 0.f, 0.f}, cs = {0.f, 0.f, 0.f, 0.f};
#pragma unroll
        for (int ww = 0; ww < NWAVE; ++ww) { z += *(const f32x4*)(ldsY + ww * 512 + 4 * lane); cs += *(const f32x4*)(ldsY + ww * 512 + 256 + 4 * (lane & 31)); }
        st_dense(rs, s1off(h, r8, 0) + lo16, (z - cs * (lane < 32 ? mean0 : mean1)) * (lane < 32 ? rstd0 : rstd1), tagA, tagB);
      }
    }
    if constexpr (p == 2) {
      wait_all(ldsC, NWAVE + 1);
      tagA = next_tag(__builtin_amdgcn_readfirstlane(ldsC[8])); tagB = __builtin_amdgcn_readfirstlane(ldsC[9]) + 0x9E3779B9u;
      asm volatile("" :: "v"(pf0), "v"(pf1), "v"(pfc));
      if (has_f1) {
        const f32x4 ln = *(const f32x4*)(ldsC + 12);
        const float n00 = (lo(xs0) - ln[0]) * ln[1], n01 = (hi(xs0) - ln[0]) * ln[1], n10 = (lo(xs1) - ln[2]) * ln[3], n11 = (hi(xs1) - ln[2]) * ln[3];
        float t[8];
#pragma unroll
        for (int k = 0; k < 4; ++k) {
          const float al = lo(ffw[0][k]), ah = hi(ffw[0][k]), bl = lo(ffw[1][k]), bh = hi(ffw[1][k]);
          t[2 * k] = fold32(fmaf(n01, bl, n00 * al), fmaf(n11, bl, n10 * al)); t[2 * k + 1] = fold32(fmaf(n01, bh, n00 * ah), fmaf(n11, bh, n10 * ah));
        }
        *(f32x4*)(ldsY + 4096 + w * 256 + (g >> 1) * 128 + 8 * c + 4 * (g & 1)) = f32x4{fold16(t[0], t[4]), fold16(t[1], t[5]), fold16(t[2], t[6]), fold16(t[3], t[7])};
        last_wave(ldsC + 2, lane);
      }
    }
    if constexpr (p == 5) {
      int gq = g; asm volatile("" : "+v"(gq));
      const int fbs = (gq >> 1) * 11 + (cid >> 2), hs = (fbs / 3) * 4 + fbs % 3;
      fuoff = has_f2 ? s1off(hs, w, 1) + ((g & 1) * 32 + 8 * (cid & 3) + (c & 7)) * 16 : OOB;
      asm volatile("" : "+v"(fuoff));
    }
    if constexpr (p == 4) {
      if (has_f1 && w == 0) {
        wait_all(ldsC + 2);
        f32x4 z = {0.f, 0.f, 0.f, 0.f};
#pragma unroll
        for (int ww = 0; ww < NWAVE; ++ww) z += *(const f32x4*)(ldsY + 4096 + ww * 256 + 4 * lane);
        st_dense(rs, s1off(h, r8, 1) + lo16, z, tagA, tagB);
      }
    }
    if constexpr (p == 6) {
      *(f32x4*)(ldsQ + w * 128 + 4 * (lane & 31)) = await_dense(qu, rs, qoff, tagA, tagB);
      last_wave(ldsC + 1, lane);
    }
    if constexpr (p == 8) {
      wait_all(ldsC + 1);
      f32x4 q0 = {0, 0, 0, 0}, q1 = {0, 0, 0, 0};
#pragma unroll
      for (int ww = 0; ww < NWAVE; ++ww) { q0 += *(const f32x4*)(ldsQ + ww * 128 + 8 * c); q1 += *(const f32x4*)(ldsQ + ww * 128 + 8 * c + 4); }
      qp[0] = pk_bf16(q0[0], q0[1]); qp[1] = pk_bf16(q0[2], q0[3]); qp[2] = pk_bf16(q1[0], q1[1]); qp[3] = pk_bf16(q1[2], q1[3]);
      qk(IC<0>{});
    }
    if constexpr (p == 9) { qk(IC<1>{}); qk(IC<2>{}); qk(IC<3>{}); }
    if constexpr (p == 10) { qk(IC<4>{}); qk(IC<5>{}); }
    if constexpr (p >= 10 && p < GROUPS + 4) qk(IC<p - 4>{});
    if constexpr (p == 10) {
      if (has_f2) {
        *(f32x4*)(ldsF + w * 128 + g * 32 + 4 * (c & 7)) = await_dense(fu, rs, fuoff, tagA, tagB);
        last_wave(ldsC + 5, lane);
      }
    }
    if constexpr (p == 12) {
      if (has_f2) {
        wait_all(ldsC + 5);
        float gs = 0.f, us = 0.f;
        const int hx = ((lane >> 2) & 1) * 32 + 4 * w + (lane & 3);
#pragma unroll
        for (int ww = 0; ww < NWAVE; ++ww) { gs += ldsF[ww * 128 + hx]; us += ldsF[ww * 128 + 64 + hx]; }
        const float hv = gelu_tanh(gs) * us;
        float dd[16];
#pragma unroll
        for (int k = 0; k < 16; ++k) dd[k] = 0.f;
#pragma unroll
        for (int r = 0; r < 4; ++r) {
          const float h0 = __builtin_bit_cast(float, __builtin_amdgcn_readlane(__builtin_bit_cast(int, hv), r));
          const float h1 = __builtin_bit_cast(float, __builtin_amdgcn_readlane(__builtin_bit_cast(int, hv), 4 + r));
#pragma unroll
          for (int k = 0; k < 4; ++k) {
            const float wl = lo(ffw[r][k]), wh = hi(ffw[r][k]);
            dd[2 * k] = fmaf(h0, wl, dd[2 * k]); dd[2 * k + 1] = fmaf(h0, wh, dd[2 * k + 1]);
            dd[8 + 2 * k] = fmaf(h1, wl, dd[8 + 2 * k]); dd[8 + 2 * k + 1] = fmaf(h1, wh, dd[8 + 2 * k + 1]);
          }
        }
        *(f32x4*)(ldsY + w * 1024 + 8 * lane) = f32x4{dd[0], dd[1], dd[2], dd[3]};
        *(f32x4*)(ldsY + w * 1024 + 8 * lane + 4) = f32x4{dd[4], dd[5], dd[6], dd[7]};
        *(f32x4*)(ldsY + w * 1024 + 512 + 8 * lane) = f32x4{dd[8], dd[9], dd[10], dd[11]};
        *(f32x4*)(ldsY + w * 1024 + 512 + 8 * lane + 4) = f32x4{dd[12], dd[13], dd[14], dd[15]};
        last_wave(ldsC + 3, lane);
      }
    }
    if constexpr (p == 28) {
      if (has_f2 && w < 4) {
        wait_all(ldsC + 3);
        f32x4 dv = {0.f, 0.f, 0.f, 0.f};
#pragma unroll
        for (int ww = 0; ww < NWAVE; ++ww) dv += *(const f32x4*)(ldsY + ww * 1024 + 4 * tid);
        st_dense(rs, WS_DP + (cid * 256 + tid) * 16, dv, tagA, tagB);
      }
    }
    if constexpr (p == GROUPS + 3) {
      float x = fmaxf(fmaxf(sp[0], sp[1]), fmaxf(sp[2], sp[3]));
      x = fmaxf(x, dpp<0x128>(x)); x = fmaxf(x, dpp<0x124>(x)); x = fmaxf(x, dpp<0x122>(x)); x = fmaxf(x, dpp<0x121>(x));
      mw = xrow_max(x) * C2;
#pragma unroll
      for (int i = 0; i < 4; ++i) sp[i] = __builtin_amdgcn_exp2f(fmaf(sp[i], C2, -mw));
      const float lw = xrow_sum(row16_sum((sp[0] + sp[2]) + (sp[1] + sp[3])));
      if (lane == 0) { ldsM[slot0 & 7] = mw; ldsM[8 + (slot0 & 7)] = lw; }
      last_wave(ldsC + 4, lane);
    }
    if constexpr (p == 21) {
      if (w == 0) {
        wait_all(ldsC + 4);
        const float mi = ldsM[lane & 7], li = ldsM[8 + (lane & 7)];
        float M = fmaxf(mi, dpp<0x124>(mi)); M = fmaxf(M, dpp<0x122>(M)); M = fmaxf(M, dpp<0x121>(M));
        float L = __builtin_amdgcn_exp2f(mi - M) * li; L += dpp<0x124>(L); L += dpp<0x122>(L); L += dpp<0x121>(L);
        if (lane == 0) st_unit(rs, WS_ML + pid * 16, M, L, tagA, tagB);
      }
    }
    if constexpr (p == 31) {
      await_unit(mu, rs, muoff, tagA, tagB);
      const float mi = __uint_as_float(mu[0]);
      float M = fmaxf(mi, dpp<0xB1>(mi)); M = fmaxf(M, dpp<0x4E>(M));
      float L = __builtin_amdgcn_exp2f(mi - M) * __uint_as_float(mu[2]); L += dpp<0xB1>(L); L += dpp<0x4E>(L);
      mw = __builtin_amdgcn_exp2f(mw - M) * __builtin_amdgcn_rcpf(L);
      ysto = g ? OOB : WS_Y + (pid * 128 + 16 * w + c) * 16;
      yoff = WS_Y + ((bc * 128 + gp) * 128 + cu0 + qd) * 16;
      asm volatile("" : "+v"(mw), "+v"(ysto), "+v"(yoff));
    }
    if constexpr (p >= 20 && p < 28) pv(IC<p - 20>{});
    if constexpr (p == 28 || p == 29) { pv(IC<2 * p - 48>{}); pv(IC<2 * p - 47>{}); }
    if constexpr (p == 30) pv(IC<12>{});
    if constexpr (p == 31) { pv(IC<13>{}); pv(IC<14>{}); pv(IC<15>{}); }
    if constexpr (p == 30) {
#pragma unroll
      for (int i = 0; i < 16; ++i) {
        const v2u r0 = wor[2 * i], r1 = wor[2 * i + 1];
        wb[0][i >> 2][i & 3] = __builtin_amdgcn_perm(r1[0], r0[0], 0x05040100u); wb[1][i >> 2][i & 3] = __builtin_amdgcn_perm(r1[0], r0[0], 0x07060302u);
        wb[2][i >> 2][i & 3] = __builtin_amdgcn_perm(r1[1], r0[1], 0x05040100u); wb[3][i >> 2][i & 3] = __builtin_amdgcn_perm(r1[1], r0[1], 0x07060302u);
      }
#pragma unroll
      for (int i = 0; i < 4; ++i) asm volatile("" : "+v"(wb[i][0]), "+v"(wb[i][1]), "+v"(wb[i][2]), "+v"(wb[i][3]));
    }
    SB();
  };
  static_for<2 * GROUPS>(point);

  {
    const f32x2 o2 = (bs[0] + bs[1]) * mw;
    const unsigned oh = pk_bf16(o2[0], o2[1]), ol = pk_bf16(o2[0] - lo(oh), o2[1] - hi(oh));
    const int ar = (slot0 & 7) * 68 + 4 * c + g;
    ldsA[ar] = oh; ldsA[8 * 68 + ar] = ol;
    if (is_fin && w < 3) {
      f32x4 e = await_dense(e0, rs, dpoff, tagA, tagB);
      if (gp >= NFFN) e = f32x4{0.f, 0.f, 0.f, 0.f};
      const float r = fold4(e[0], e[1], e[2], e[3]);
      if (c < 4) ldsF[w * 16 + 4 * c + g] = r;
    }
  }
  __syncthreads();
  {
    v4u af[4];
#pragma unroll
    for (int ks = 0; ks < 4; ++ks) af[ks] = *(const v4u*)(ldsA + c * 68 + 16 * ks + 4 * g);
    SB();
    f32x4 d[4];
#pragma unroll
    for (int t = 0; t < 4; ++t) d[t] = f32x4{0.f, 0.f, 0.f, 0.f};
#pragma unroll
    for (int ks = 0; ks < 4; ++ks)
#pragma unroll
      for (int t = 0; t < 4; ++t) d[t] = __builtin_amdgcn_mfma_f32_16x16x32_bf16(__builtin_bit_cast(bf16x8, af[ks]), __builtin_bit_cast(bf16x8, wb[t][ks]), d[t], 0, 0, 0);
    float y[4];
#pragma unroll
    for (int t = 0; t < 4; ++t) y[t] = (d[t][0] + d[t][1]) + (d[t][2] + d[t][3]);
    float u0 = fold32(y[0], y[2]), u1 = fold32(y[1], y[3]);
    u0 = fold16(u0, u0); u1 = fold16(u1, u1);
    const auto r0 = __builtin_amdgcn_permlane32_swap(__float_as_uint(u0), __float_as_uint(u0), false, false), r1 = __builtin_amdgcn_permlane32_swap(__float_as_uint(u1), __float_as_uint(u1), false, false);
    st_dense(rs, ysto, f32x4{__uint_as_float(r0[0]), __uint_as_float(r1[0]), __uint_as_float(r0[1]), __uint_as_float(r1[1])}, tagA, tagB);
  }

  if (is_fin) {
    __builtin_amdgcn_s_barrier();
    const float dv = ldsF[c] + ldsF[16 + c] + ldsF[32 + c];
    float gte = dv * __builtin_amdgcn_rcpf(1.f + __expf(-dv));
    asm volatile("" : "+v"(gte));
    __builtin_amdgcn_s_sleep(11);
    v4u y0;
    bool ok = false;
#pragma unroll 1
    for (unsigned spin = 0; spin < SPIN_CAP; ++spin) {
      asm volatile("" ::: "memory");
      if (!ok) { y0 = ld_unit(rs, yoff); ok = ok_dense(y0, tagA, tagB); }
      if (__all(ok)) break;
      __builtin_amdgcn_s_sleep(1);
    }
    const f32x4 yv = __builtin_bit_cast(f32x4, y0 & 0xfffffc00u);
    const float r = fold4(yv[0], yv[1], yv[2], yv[3]);
    if (c < 4) ldsF[128 + w * 16 + 4 * c + g] = r;
    __syncthreads();
    if (tid < 16) {
      float sv = 0.f;
#pragma unroll
      for (int ww = 0; ww < NWAVE; ++ww) sv += ldsF[128 + ww * 16 + tid];
      const float o = __uint_as_float((unsigned)xo << 16) + sv * gte;
      const __bf16 ob = (__bf16)o;
      __builtin_amdgcn_raw_buffer_store_b16(__builtin_bit_cast(unsigned short, ob), RSRC(KAP(8), 2 * D * 2), ocol * 2, 0, 0);
      if (cid == 0 && tid == 0)
        __hip_atomic_store((GAS unsigned long long*)(pws + WS_EPOCH), ((unsigned long long)tagB << 32) | tagA, __ATOMIC_RELAXED, __HIP_MEMORY_SCOPE_AGENT);
    }
  }
  asm volatile(".Lk_all_end:");
}

extern "C" void kernel_launch(void* const* d_in, const int* in_sizes, int n_in, void* d_out, int out_size, void* d_ws, size_t ws_size,
                              hipStream_t stream) {
  Args a;
  a.x = (const unsigned short*)d_in[0];
  a.k = (const unsigned short*)d_in[1];
  a.v = (const unsigned short*)d_in[2];
  a.wq = (const unsigned short*)d_in[3];
  a.wo = (const unsigned short*)d_in[4];
  a.wg = (const unsigned short*)d_in[6];
  a.wu = (const unsigned short*)d_in[7];
  a.wd = (const unsigned short*)d_in[8];
  a.out = (unsigned short*)d_out;
  a.ws = (unsigned char*)d_ws;
  hipLaunchKernelGGL(k_all, dim3(256), dim3(NTHR), 0, stream, a);
}
```

```cpp
#include <hip/hip_runtime.h>
#include <hip/hip_bf16.h>
#include <stdint.h>

typedef unsigned v4u __attribute__((ext_vector_type(4)));
typedef float f32x4 __attribute__((ext_vector_type(4)));
typedef float f32x2 __attribute__((ext_vector_type(2)));
typedef __bf16 bf16x2 __attribute__((ext_vector_type(2)));
typedef __bf16 bf16x8 __attribute__((ext_vector_type(8)));
typedef unsigned v16u __attribute__((ext_vector_type(16)));
typedef unsigned v2u __attribute__((ext_vector_type(2)));
#define GAS __attribute__((address_space(1)))

constexpr int D = 512, H = 32, SK = 8192, DH = 128, F = 1376;
constexpr int NSPLIT = 4, KEYS_PER_WG = SK / NSPLIT;
constexpr int NWAVE = 8, NTHR = NWAVE * 64;
constexpr int NFFN = F / 32;
constexpr int U = 4;
constexpr int GROUPS = KEYS_PER_WG / NWAVE / (4 * U);
#define TP_ 1728
#define T0_ 2350
#define EX1_ 70
constexpr unsigned step_of(int p) { return TP_ + (p < 9 ? EX1_ : 0); }
constexpr int LATE_PRIO = 400;
constexpr int won(int p) { return p <= 1 ? 0 : p >= 28 ? 32 : (p - 1) * 32 / 27; }
static_assert(TP_ % 8 == 0, "a slot is TP / 8");

constexpr int WS_EPOCH = 0;
constexpr int WS_ML = 4096;
constexpr int WS_Y = WS_ML + 256 * 16;
constexpr int WS_DP = WS_Y + 256 * 128 * 16;
constexpr int WS_S1 = WS_DP + NFFN * 256 * 16;
constexpr int WS_DBG = WS_S1 + 32 * 8 * 2 * 64 * 16;
constexpr int WS_END = WS_DBG + 256 * 16;
#define DBG_ 1
constexpr int OOB = 0x7ffffff0;
constexpr unsigned SPIN_CAP = 400000;

#define SB() __builtin_amdgcn_sched_barrier(0)

__device__ __forceinline__ float dot2(unsigned k, unsigned q, float acc) {
  return __builtin_amdgcn_fdot2_f32_bf16(__builtin_bit_cast(bf16x2, k), __builtin_bit_cast(bf16x2, q), acc, false);
}
template <int CTRL> __device__ __forceinline__ float dpp(float x) {
  return __builtin_bit_cast(float, __builtin_amdgcn_mov_dpp(__builtin_bit_cast(int, x), CTRL, 0xf, 0xf, true));
}
__device__ __forceinline__ float row16_sum(float x) {
  x += dpp<0x128>(x);
  x += dpp<0x124>(x);
  x += dpp<0x122>(x);
  x += dpp<0x121>(x);
  return x;
}
__device__ __forceinline__ float fold32(float a, float b) {
  auto t = __builtin_amdgcn_permlane32_swap(__float_as_uint(a), __float_as_uint(b), false, false);
  return __uint_as_float(t[0]) + __uint_as_float(t[1]);
}
__device__ __forceinline__ float fold16(float a, float b) {
  auto t = __builtin_amdgcn_permlane16_swap(__float_as_uint(a), __float_as_uint(b), false, false);
  return __uint_as_float(t[0]) + __uint_as_float(t[1]);
}
__device__ __forceinline__ float xrow_sum(float x) { x = fold16(x, x); return fold32(x, x); }
__device__ __forceinline__ float xrow_max(float x) {
  auto s = __builtin_amdgcn_permlane16_swap(__float_as_uint(x), __float_as_uint(x), false, false);
  x = fmaxf(__uint_as_float(s[0]), __uint_as_float(s[1]));
  auto t = __builtin_amdgcn_permlane32_swap(__float_as_uint(x), __float_as_uint(x), false, false);
  return fmaxf(__uint_as_float(t[0]), __uint_as_float(t[1]));
}
__device__ __forceinline__ f32x2 fold8(const float (&v)[8]) {
  const float u0 = fold32(v[0], v[4]), u1 = fold32(v[1], v[5]), u2 = fold32(v[2], v[6]), u3 = fold32(v[3], v[7]);
  return f32x2{fold16(u0, u2), fold16(u1, u3)};
}
__device__ __forceinline__ float fold4(float v0, float v1, float v2, float v3) {
  float r = fold16(fold32(v0, v2), fold32(v1, v3));
  r += dpp<0x124>(r); r += dpp<0x128>(r);
  return r;
}
__device__ __forceinline__ float lo(unsigned u) { return __uint_as_float(u << 16); }
__device__ __forceinline__ float hi(unsigned u) { return __uint_as_float(u & 0xffff0000u); }
__device__ __forceinline__ unsigned pk_bf16(float a, float b) {
  bf16x2 r = {(__bf16)a, (__bf16)b};
  return __builtin_bit_cast(unsigned, r);
}
__device__ __forceinline__ unsigned sel4(v4u v, int g) { const unsigned a = (g & 1) ? v[1] : v[0], b = (g & 1) ? v[3] : v[2]; return (g & 2) ? b : a; }
__device__ __forceinline__ int s1off(int hg, int r, int k) { return WS_S1 + (hg * 8 + ((r + hg) & 7)) * 2048 + ((k ^ (hg >> 3)) & 1) * 1024; }
__device__ __forceinline__ float gelu_tanh(float x) {
  const float u = 0.7978845608028654f * (x + 0.044715f * x * x * x);
  const float e = __expf(2.f * u);
  const float t = 1.f - 2.f * __builtin_amdgcn_rcpf(e + 1.f);
  return 0.5f * x * (1.f + t);
}
__device__ __forceinline__ void pace_wait(unsigned& due16, unsigned step16) {
  asm volatile("1:\n\ts_memrealtime s[90:91]\n\ts_waitcnt lgkmcnt(0)\n\ts_lshl_b32 s90, s90, 4\n\ts_sub_u32 s91, s90, %0\n\ts_cmp_ge_i32 s91, 0\n\ts_cbranch_scc1 2f\n\ts_sleep 2\n\ts_branch 1b\n"
               "2:\n\ts_add_u32 %0, %0, %1\n\ts_setprio 0\n\ts_cmp_lt_i32 s91, %2\n\ts_cbranch_scc1 3f\n\ts_setprio 2\n3:"
               : "+s"(due16) : "s"(step16), "i"(LATE_PRIO) : "s90", "s91", "scc");
}
__device__ __forceinline__ unsigned now16() { return (unsigned)__builtin_amdgcn_s_memrealtime() << 4; }
__device__ __forceinline__ unsigned next_tag(unsigned e) { return ((e + 1u) & 1023u) | ((e + 1024u) & (1023u << 10)) | ((e + (1u << 20)) & 0xfff00000u); }
constexpr int AUX_SC1 = 16;
__device__ __forceinline__ void st_unit(__amdgpu_buffer_rsrc_t rs, int off, float v0, float v1, unsigned ta, unsigned tb) {
  __builtin_amdgcn_raw_buffer_store_b128(v4u{__float_as_uint(v0), ta, __float_as_uint(v1), tb}, rs, off, 0, AUX_SC1);
}
__device__ __forceinline__ v4u ld_unit(__amdgpu_buffer_rsrc_t rs, int off) { return __builtin_amdgcn_raw_buffer_load_b128(rs, off, 0, AUX_SC1); }
__device__ __forceinline__ unsigned enc22(float v, unsigned t) { return ((__float_as_uint(v) + 0x200u) & 0xfffffc00u) | t; }
__device__ __forceinline__ void st_dense(__amdgpu_buffer_rsrc_t rs, int off, f32x4 v, unsigned ta, unsigned tb) {
  __builtin_amdgcn_raw_buffer_store_b128(v4u{enc22(v[0], ta & 1023u), enc22(v[1], (ta >> 10) & 1023u), enc22(v[2], (ta >> 20) & 1023u), enc22(v[3], tb & 1023u)}, rs, off, 0, AUX_SC1);
}
__device__ __forceinline__ bool ok_dense(v4u u, unsigned ta, unsigned tb) { return ((((u[0] ^ ta) | (u[1] ^ (ta >> 10))) | ((u[2] ^ (ta >> 20)) | (u[3] ^ tb))) & 1023u) == 0u; }
__device__ __forceinline__ f32x4 await_dense(v4u u, __amdgpu_buffer_rsrc_t rs, int off, unsigned ta, unsigned tb) {
  if (!__all(ok_dense(u, ta, tb))) {
#pragma unroll 1
    for (unsigned spin = 0; spin < SPIN_CAP; ++spin) {
      __builtin_amdgcn_s_sleep(1);
      asm volatile("" ::: "memory");
      u = ld_unit(rs, off);
      if (__all(ok_dense(u, ta, tb))) break;
    }
  }
  return __builtin_bit_cast(f32x4, u & 0xfffffc00u);
}
__device__ __forceinline__ void await_unit(v4u& u, __amdgpu_buffer_rsrc_t rs, int off, unsigned ta, unsigned tb) {
  if (!__all(((u[1] ^ ta) | (u[3] ^ tb)) == 0u)) {
#pragma unroll 1
    for (unsigned spin = 0; spin < SPIN_CAP; ++spin) {
      __builtin_amdgcn_s_sleep(1);
      asm volatile("" ::: "memory");
      u = ld_unit(rs, off);
      if (__all(((u[1] ^ ta) | (u[3] ^ tb)) == 0u)) break;
    }
  }
}

__device__ __forceinline__ bool last_wave(unsigned* cnt, int lane) {
  asm volatile("s_waitcnt lgkmcnt(0)" ::: "memory");
  unsigned old = 0;
  if (lane == 0) old = __hip_atomic_fetch_add(cnt, 1u, __ATOMIC_RELAXED, __HIP_MEMORY_SCOPE_WORKGROUP);
  old = __builtin_amdgcn_readfirstlane(old);
  asm volatile("" ::: "memory");
  return old == NWAVE - 1;
}

__device__ __forceinline__ void wait_all(unsigned* cnt, unsigned n = NWAVE) {
  while (__builtin_amdgcn_readfirstlane(__hip_atomic_load(cnt, __ATOMIC_RELAXED, __HIP_MEMORY_SCOPE_WORKGROUP)) < n) __builtin_amdgcn_s_sleep(1);
  asm volatile("" ::: "memory");
}
template <int I> struct IC { static constexpr int value = I; };
template <int N, int I = 0, typename Fn> __device__ __forceinline__ void static_for(Fn& fn) { if constexpr (I < N) { fn(IC<I>{}); static_for<N, I + 1>(fn); } }

struct Args {
  const unsigned short* x; const unsigned short* wq;
  const unsigned short* k; const unsigned short* v; const unsigned short* wo; const unsigned short* wg;
  const unsigned short* wu; const unsigned short* wd; unsigned short* out; unsigned char* ws;
};

__global__ void __launch_bounds__(NTHR, 2) k_all(Args a) {
  __shared__ __attribute__((aligned(16))) float lds[8 * 1024 + 16 * 68 + 16 + 8 * 128 + 8 * 128 + 32 + 16];
  float* ldsY = lds;
  unsigned* ldsA = (unsigned*)(lds + 8 * 1024);
  float* ldsM = lds + 8 * 1024 + 16 * 68;
  float* ldsF = ldsM + 16;
  float* ldsQ = ldsF + 8 * 128;
  float* ldsS = ldsQ + 8 * 128;
  unsigned* ldsC = (unsigned*)(ldsS + 32);
  const unsigned tstart = now16();
  const int bid = blockIdx.x;
  const int xcd = bid & 7, j = bid >> 3;
  const int tid = threadIdx.x, lane = tid & 63, w = __builtin_amdgcn_readfirstlane(tid >> 6), lo16 = lane * 16;
  v16u ka; v4u kb; unsigned long long tptr, pc0; unsigned csz;
  asm volatile("s_load_dwordx16 %0, %5, 0x0\n\ts_load_dwordx4 %1, %5, 0x40\n\ts_load_dwordx2 %2, %5, %6 offset:0x10\n\t"
               "s_getpc_b64 %3\n.Lk_all_pc0:\n\ts_mov_b32 %4, (.Lk_all_end-.Lk_all_pc0)&0xfffffff0"
               : "=&s"(ka), "=&s"(kb), "=&s"(tptr), "=&s"(pc0), "=&s"(csz) : "s"(__builtin_amdgcn_kernarg_segment_ptr()), "s"(8 * w));
#define RSRC(p_, n_) __builtin_amdgcn_make_buffer_rsrc((void*)(p_), 0, (n_), 0x00020000)
  const v4u pfc = __builtin_amdgcn_raw_buffer_load_b128(RSRC(pc0 & ~127ull, w == 4 ? csz : 0u), lo16, j * 1024, 0);
  ldsC[tid & 15] = 0u;
  SB();
  const int h = xcd * 4 + (j >> 3), b = (j >> 2) & 1, s = j & 3;
  const int g = lane >> 4, c = lane & 15;
  const int r8 = j & 7;
  int qwo = (2 * g * H * DH + 8 * c) * 2, xso = (lane >> 5) * D * 2 + (lane & 31) * 4, qso = ((64 * r8 + 8 * w) * H * DH + h * DH) * 2;
  unsigned tof = ((unsigned)(b * H + h) * ((0x03u >> w) & 1u) << 21) + ((unsigned)h * ((0x04u >> w) & 1u) << 17);
  asm volatile("" : "+v"(qwo), "+v"(xso), "+v"(tof));
  asm volatile("s_waitcnt lgkmcnt(0)\n\ts_barrier" : "+s"(ka), "+s"(kb), "+s"(tptr), "+s"(qso) :: "memory");
#define KAP(i) ((i) < 8 ? (unsigned long long)ka[2 * (i) % 16] | ((unsigned long long)ka[(2 * (i) + 1) % 16] << 32) : (unsigned long long)kb[2 * (i) % 4] | ((unsigned long long)kb[(2 * (i) + 1) % 4] << 32))
  const unsigned long long px = KAP(0), pws = KAP(9);
  const unsigned long long ep = __hip_atomic_load((const GAS unsigned long long*)(tptr + tof), __ATOMIC_RELAXED, __HIP_MEMORY_SCOPE_AGENT);
  SB();
  const unsigned xst = __builtin_amdgcn_raw_buffer_load_b32(RSRC(px, 2 * D * 2), xso, 128 * w, 0);
  SB();
  const __amdgpu_buffer_rsrc_t rq = RSRC(KAP(1), D * H * DH * 2);
  const v4u sw0 = __builtin_amdgcn_raw_buffer_load_b128(rq, qwo, qso, 2), sw1 = __builtin_amdgcn_raw_buffer_load_b128(rq, qwo, qso + H * DH * 2, 2);
  v4u xq0, xq1;
  asm volatile("s_load_dwordx4 %0, %2, 0x0\n\ts_load_dwordx4 %1, %2, 0x400" : "=&s"(xq0), "=&s"(xq1) : "s"(px + (64 * r8 + 8 * w) * 2));
  SB();
  const __amdgpu_buffer_rsrc_t rw = RSRC(KAP(4) + (size_t)h * DH * D * 2, DH * D * 2);
  const v4u pf0 = __builtin_amdgcn_raw_buffer_load_b128(rw, lo16, (16 * r8 + 2 * w) * 1024, 0), pf1 = __builtin_amdgcn_raw_buffer_load_b128(rw, lo16 + 1024, (16 * r8 + 2 * w) * 1024, 0);
  SB();
  const int fb = xcd * 3 + (j >> 3);
  const bool has_f1 = (j < 24) && (fb < 22);
  const bool is_fin = j >= 24;
  const int cid = xcd * 8 + (j - 24);
  const bool has_f2 = is_fin && cid < NFFN;
  const int row0 = 64 * r8 + 8 * w + 2 * g;
  const int fcol = 128 * (fb < 11 ? fb : fb - 11) + 8 * c;
  int fx1 = (row0 * F + fcol) * 2, fx2 = (32 * cid + 4 * w) * D * 2 + lo16;
  asm volatile("" : "+v"(fx1), "+v"(fx2));
  const int fxo = has_f1 ? (fcol < F ? fx1 : OOB - 4 * F * 2) : fx2, fxo2 = has_f1 ? OOB - 4096 : fx2, fst = has_f1 ? F * 2 : D * 2;
  unsigned f_lo = fb < 11 ? ka[10] : ka[12], f_hi = fb < 11 ? ka[11] : ka[13];
  asm volatile("" : "+s"(f_lo), "+s"(f_hi));
  const __amdgpu_buffer_rsrc_t rf = RSRC((unsigned long long)(has_f1 ? f_lo : ka[14]) | ((unsigned long long)(has_f1 ? f_hi : ka[15]) << 32), (has_f1 || has_f2) ? D * F * 2 : 0);
  v4u ffw[4];
  ffw[0] = __builtin_amdgcn_raw_buffer_load_b128(rf, fxo, 0, 2);
  ffw[1] = __builtin_amdgcn_raw_buffer_load_b128(rf, fxo, fst, 2);
  ffw[2] = __builtin_amdgcn_raw_buffer_load_b128(rf, fxo2 + 2048, 0, 2);
  ffw[3] = __builtin_amdgcn_raw_buffer_load_b128(rf, fxo2 + 3072, 0, 2);
  SB();
  if (w == 0) {
    asm volatile("" :: "v"(ep));
    __hip_atomic_store(ldsC + 10, now16() | 1u, __ATOMIC_RELAXED, __HIP_MEMORY_SCOPE_WORKGROUP);
  }
  SB();
  const __amdgpu_buffer_rsrc_t rs = RSRC(pws, WS_END);

  const __amdgpu_buffer_rsrc_t rk = RSRC(KAP(2) + (size_t)(b * H + h) * SK * DH * 2, SK * DH * 2);
  const __amdgpu_buffer_rsrc_t rv = RSRC(KAP(3) + (size_t)(b * H + h) * SK * DH * 2, SK * DH * 2);
#define GOFF(gi_) (((gi_) & 1) * ((1 << 20) | (1 << 12)) | ((gi_) & 2) * ((1 << 18) | (1 << 13)) | (((gi_) & 4) << 16) | (((gi_) & 8) << 14))
  v4u rg[2 * GROUPS][U];

  constexpr float C2 = 0.08838834764831845f * 1.4426950408889634f;
  float sp[4] = {0.f, 0.f, 0.f, 0.f};
  float mw = 0.f;
  f32x2 bs[8];
  unsigned tagA, tagB, qp[4], slot0;
  v2u wor[32]; v4u wb[4][4], fu, qu, mu, e0; unsigned short xo;
  unsigned xs0, xs1;
  int fuoff, ysto, yoff;
  const int wvo = g * 8192 + w * 128 + c * 8;
  const int qoff = s1off(h, w, 0) + (b * 32 + (lane & 31)) * 16;
  const int pid = (b * H + h) * NSPLIT + s, muoff = WS_ML + ((pid & ~3) + (lane & 3)) * 16;
  const int bc = (cid >> 5) & 1, cu0 = (cid & 31) * 4;
  const int gp = tid >> 2, qd = tid & 3, ocol = bc * D + 4 * cu0 + c;
  const int dpoff = (is_fin && w < 3) ? WS_DP + ((gp < NFFN ? gp : NFFN - 1) * 256 + bc * 128 + cu0 + qd) * 16 : OOB;

  auto qk = [&](auto gic) __attribute__((always_inline)) {
    constexpr int gi = decltype(gic)::value;
    float d[U];
#pragma unroll
    for (int u = 0; u < U; ++u) d[u] = dot2(rg[gi][u][3], qp[3], dot2(rg[gi][u][2], qp[2], dot2(rg[gi][u][1], qp[1], dot2(rg[gi][u][0], qp[0], 0.f))));
    const bool o1 = c & 1, o2 = c & 2;
    const float x0 = (o1 ? d[1] : d[0]) + dpp<0xB1>(o1 ? d[0] : d[1]), x1 = (o1 ? d[3] : d[2]) + dpp<0xB1>(o1 ? d[2] : d[3]);
    float x = (o2 ? x1 : x0) + dpp<0x4E>(o2 ? x0 : x1);
    x += dpp<0x128>(x); x += dpp<0x124>(x);
    sp[gi / 4] = __builtin_bit_cast(float, __builtin_amdgcn_update_dpp(__builtin_bit_cast(int, sp[gi / 4]), __builtin_bit_cast(int, x), 0xE4, 0xf, 1 << (gi % 4), false));
    asm volatile("" : "+v"(sp[gi / 4]));
    SB();
  };
  auto pv = [&](auto gic) __attribute__((always_inline)) {
    constexpr int gi = decltype(gic)::value;
    const float p[4] = {dpp<0x150 + 4 * (gi % 4)>(sp[gi / 4]), dpp<0x151 + 4 * (gi % 4)>(sp[gi / 4]), dpp<0x152 + 4 * (gi % 4)>(sp[gi / 4]), dpp<0x153 + 4 * (gi % 4)>(sp[gi / 4])};
    float x[8];
#pragma unroll
    for (int k = 0; k < 4; ++k) { x[2 * k] = p[0] * lo(rg[GROUPS + gi][0][k]); x[2 * k + 1] = p[0] * hi(rg[GROUPS + gi][0][k]); }
#pragma unroll
    for (int u = 1; u < U; ++u) {
#pragma unroll
      for (int k = 0; k < 4; ++k) { x[2 * k] = fmaf(p[u], lo(rg[GROUPS + gi][u][k]), x[2 * k]); x[2 * k + 1] = fmaf(p[u], hi(rg[GROUPS + gi][u][k]), x[2 * k + 1]); }
    }
    const f32x2 y = fold8(x);
    if constexpr (gi < 8) bs[gi] = y; else bs[gi - 8] += y;
    if constexpr (gi >= 12) bs[gi - 12] += bs[gi - 8];
    if constexpr (gi >= 14) bs[gi - 14] += bs[gi - 12];
    asm volatile("" : "+v"(bs[gi >= 14 ? gi - 14 : gi >= 12 ? gi - 12 : gi % 8]));
    SB();
  };

  {
    unsigned tk;
    while ((tk = __builtin_amdgcn_readfirstlane(__hip_atomic_load(ldsC + 10, __ATOMIC_RELAXED, __HIP_MEMORY_SCOPE_WORKGROUP))) == 0u) __builtin_amdgcn_s_sleep(1);
    slot0 = __builtin_amdgcn_readfirstlane((__builtin_amdgcn_readfirstlane(tk) + (unsigned)T0_) / (unsigned)(TP_ / 8) + (unsigned)w);
  }
  const int kvo = ((slot0 & 1) << 13) | ((slot0 & 6) << 14) | ((s & 1) << 12) | ((s >> 1) << 14);
  unsigned due = __builtin_amdgcn_readfirstlane(slot0 * (unsigned)(TP_ / 8));

  auto point = [&](auto pc) __attribute__((always_inline)) {
    constexpr int p = decltype(pc)::value;
    int so = kvo ^ GOFF((slot0 >> 3) + p);
    asm volatile("" : "+s"(so));
    pace_wait(due, step_of(p));
    SB();
#pragma unroll
    for (int u = 0; u < U; ++u) rg[p][u] = __builtin_amdgcn_raw_buffer_load_b128(p < GROUPS ? rk : rv, lo16 + u * 1024, so, 2);
    SB();
    if constexpr (p == 4) qu = ld_unit(rs, qoff);
    if constexpr (p == 7) fu = ld_unit(rs, fuoff);
    if constexpr (p == 29) mu = ld_unit(rs, muoff);
#pragma unroll
    for (int i = won(p); i < won(p + 1); ++i) wor[i] = __builtin_amdgcn_raw_buffer_load_b64(rw, wvo, (32 * (i >> 3) + (i & 7)) * 1024, 0);
    if constexpr (p == 31) {
      e0 = ld_unit(rs, dpoff);
      xo = ((const GAS unsigned short*)px)[ocol];
    }
    SB();
    if constexpr (p == 0) {
      asm volatile("s_waitcnt lgkmcnt(0)" : "+s"(xq0), "+s"(xq1));
      xs0 = sel4(xq0, g); xs1 = sel4(xq1, g);
      float sm = row16_sum(lo(xst) + hi(xst)); sm = fold16(sm, sm) * (1.f / 64);
      const float d0 = lo(xst) - sm, d1 = hi(xst) - sm;
      float m2 = row16_sum(fmaf(d0, d0, d1 * d1)); m2 = fold16(m2, m2);
      if ((lane & 31) == 0) *(f32x2*)(ldsS + (lane >> 5) * 16 + 2 * w) = f32x2{sm, m2};
      float t[24];
#pragma unroll
      for (int k = 0; k < 4; ++k) {
        const float al = lo(sw0[k]), ah = hi(sw0[k]), bl = lo(sw1[k]), bh = hi(sw1[k]);
        t[2 * k] = fmaf(hi(xs0), bl, lo(xs0) * al); t[2 * k + 1] = fmaf(hi(xs0), bh, lo(xs0) * ah);
        t[8 + 2 * k] = fmaf(hi(xs1), bl, lo(xs1) * al); t[8 + 2 * k + 1] = fmaf(hi(xs1), bh, lo(xs1) * ah);
        t[16 + 2 * k] = al + bl; t[16 + 2 * k + 1] = ah + bh;
      }
#pragma unroll
      for (int k = 0; k < 8; ++k) t[k] = fold16(fold32(t[k], t[16 + k]), fold32(t[8 + k], t[16 + k]));
      *(f32x4*)(ldsY + w * 512 + g * 128 + 8 * c) = f32x4{t[0], t[1], t[2], t[3]};
      *(f32x4*)(ldsY + w * 512 + g * 128 + 8 * c + 4) = f32x4{t[4], t[5], t[6], t[7]};
      if (tid == 448) *(unsigned long long*)(ldsC + 8) = ep;
      asm volatile("" :: "v"(ep));
      if (last_wave(ldsC, lane)) {
        f32x4 st[8];
#pragma unroll
        for (int i = 0; i < 8; ++i) st[i] = *(const f32x4*)(ldsS + 4 * i);
        tagA = next_tag(__builtin_amdgcn_readfirstlane(ldsC[8])); tagB = __builtin_amdgcn_readfirstlane(ldsC[9]) + 0x9E3779B9u;
        const float mean0 = ((st[0][0] + st[0][2]) + (st[1][0] + st[1][2]) + (st[2][0] + st[2][2]) + (st[3][0] + st[3][2])) * 0.125f;
        const float mean1 = ((st[4][0] + st[4][2]) + (st[5][0] + st[5][2]) + (st[6][0] + st[6][2]) + (st[7][0] + st[7][2])) * 0.125f;
        float q0 = 0.f, q1 = 0.f, d0 = 0.f, d1 = 0.f;
#pragma unroll
        for (int i = 0; i < 4; ++i) {
          q0 += st[i][1] + st[i][3]; q1 += st[4 + i][1] + st[4 + i][3];
          const float a0 = st[i][0] - mean0, b0 = st[i][2] - mean0, a1 = st[4 + i][0] - mean1, b1 = st[4 + i][2] - mean1;
          d0 += fmaf(a0, a0, b0 * b0); d1 += fmaf(a1, a1, b1 * b1);
        }
        const float rstd0 = __builtin_amdgcn_rsqf(fmaf(64.f, d0, q0) * (1.f / D) + 1e-5f), rstd1 = __builtin_amdgcn_rsqf(fmaf(64.f, d1, q1) * (1.f / D) + 1e-5f);
        if (lane == 0) *(f32x4*)(ldsC + 12) = f32x4{mean0, rstd0, mean1, rstd1};
        last_wave(ldsC, lane);
        f32x4 z = {0.f, 0.f, 0.f, 0.f}, cs = {0.f, 0.f, 0.f, 0.f};
#pragma unroll
        for (int ww = 0; ww < NWAVE; ++ww) { z += *(const f32x4*)(ldsY + ww * 512 + 4 * lane); cs += *(const f32x4*)(ldsY + ww * 512 + 256 + 4 * (lane & 31)); }
        st_dense(rs, s1off(h, r8, 0) + lo16, (z - cs * (lane < 32 ? mean0 : mean1)) * (lane < 32 ? rstd0 : rstd1), tagA, tagB);
      }
    }
    if constexpr (p == 2) {
      wait_all(ldsC, NWAVE + 1);
      tagA = next_tag(__builtin_amdgcn_readfirstlane(ldsC[8])); tagB = __builtin_amdgcn_readfirstlane(ldsC[9]) + 0x9E3779B9u;
      asm volatile("" :: "v"(pf0), "v"(pf1), "v"(pfc));
      if (has_f1) {
        const f32x4 ln = *(const f32x4*)(ldsC + 12);
        const float n00 = (lo(xs0) - ln[0]) * ln[1], n01 = (hi(xs0) - ln[0]) * ln[1], n10 = (lo(xs1) - ln[2]) * ln[3], n11 = (hi(xs1) - ln[2]) * ln[3];
        float t[8];
#pragma unroll
        for (int k = 0; k < 4; ++k) {
          const float al = lo(ffw[0][k]), ah = hi(ffw[0][k]), bl = lo(ffw[1][k]), bh = hi(ffw[1][k]);
          t[2 * k] = fold32(fmaf(n01, bl, n00 * al), fmaf(n11, bl, n10 * al)); t[2 * k + 1] = fold32(fmaf(n01, bh, n00 * ah), fmaf(n11, bh, n10 * ah));
        }
        *(f32x4*)(ldsY + 4096 + w * 256 + (g >> 1) * 128 + 8 * c + 4 * (g & 1)) = f32x4{fold16(t[0], t[4]), fold16(t[1], t[5]), fold16(t[2], t[6]), fold16(t[3], t[7])};
        last_wave(ldsC + 2, lane);
      }
    }
    if constexpr (p == 5) {
      int gq = g; asm volatile("" : "+v"(gq));
      const int fbs = (gq >> 1) * 11 + (cid >> 2), hs = (fbs / 3) * 4 + fbs % 3;
      fuoff = has_f2 ? s1off(hs, w, 1) + ((g & 1) * 32 + 8 * (cid & 3) + (c & 7)) * 16 : OOB;
      asm volatile("" : "+v"(fuoff));
    }
    if constexpr (p == 4) {
      if (has_f1 && w == 0) {
        wait_all(ldsC + 2);
        f32x4 z = {0.f, 0.f, 0.f, 0.f};
#pragma unroll
        for (int ww = 0; ww < NWAVE; ++ww) z += *(const f32x4*)(ldsY + 4096 + ww * 256 + 4 * lane);
        st_dense(rs, s1off(h, r8, 1) + lo16, z, tagA, tagB);
      }
    }
    if constexpr (p == 6) {
      *(f32x4*)(ldsQ + w * 128 + 4 * (lane & 31)) = await_dense(qu, rs, qoff, tagA, tagB);
      last_wave(ldsC + 1, lane);
    }
    if constexpr (p == 8) {
      wait_all(ldsC + 1);
      f32x4 q0 = {0, 0, 0, 0}, q1 = {0, 0, 0, 0};
#pragma unroll
      for (int ww = 0; ww < NWAVE; ++ww) { q0 += *(const f32x4*)(ldsQ + ww * 128 + 8 * c); q1 += *(const f32x4*)(ldsQ + ww * 128 + 8 * c + 4); }
      qp[0] = pk_bf16(q0[0], q0[1]); qp[1] = pk_bf16(q0[2], q0[3]); qp[2] = pk_bf16(q1[0], q1[1]); qp[3] = pk_bf16(q1[2], q1[3]);
      qk(IC<0>{});
    }
    if constexpr (p == 9) { qk(IC<1>{}); qk(IC<2>{}); qk(IC<3>{}); }
    if constexpr (p == 10) { qk(IC<4>{}); qk(IC<5>{}); }
    if constexpr (p >= 10 && p < GROUPS + 4) qk(IC<p - 4>{});
    if constexpr (p == 10) {
      if (has_f2) {
        *(f32x4*)(ldsF + w * 128 + g * 32 + 4 * (c & 7)) = await_dense(fu, rs, fuoff, tagA, tagB);
        last_wave(ldsC + 5, lane);
      }
    }
    if constexpr (p == 12) {
      if (has_f2) {
        wait_all(ldsC + 5);
        float gs = 0.f, us = 0.f;
        const int hx = ((lane >> 2) & 1) * 32 + 4 * w + (lane & 3);
#pragma unroll
        for (int ww = 0; ww < NWAVE; ++ww) { gs += ldsF[ww * 128 + hx]; us += ldsF[ww * 128 + 64 + hx]; }
        const float hv = gelu_tanh(gs) * us;
        float dd[16];
#pragma unroll
        for (int k = 0; k < 16; ++k) dd[k] = 0.f;
#pragma unroll
        for (int r = 0; r < 4; ++r) {
          const float h0 = __builtin_bit_cast(float, __builtin_amdgcn_readlane(__builtin_bit_cast(int, hv), r));
          const float h1 = __builtin_bit_cast(float, __builtin_amdgcn_readlane(__builtin_bit_cast(int, hv), 4 + r));
#pragma unroll
          for (int k = 0; k < 4; ++k) {
            const float wl = lo(ffw[r][k]), wh = hi(ffw[r][k]);
            dd[2 * k] = fmaf(h0, wl, dd[2 * k]); dd[2 * k + 1] = fmaf(h0, wh, dd[2 * k + 1]);
            dd[8 + 2 * k] = fmaf(h1, wl, dd[8 + 2 * k]); dd[8 + 2 * k + 1] = fmaf(h1, wh, dd[8 + 2 * k + 1]);
          }
        }
        *(f32x4*)(ldsY + w * 1024 + 8 * lane) = f32x4{dd[0], dd[1], dd[2], dd[3]};
        *(f32x4*)(ldsY + w * 1024 + 8 * lane + 4) = f32x4{dd[4], dd[5], dd[6], dd[7]};
        *(f32x4*)(ldsY + w * 1024 + 512 + 8 * lane) = f32x4{dd[8], dd[9], dd[10], dd[11]};
        *(f32x4*)(ldsY + w * 1024 + 512 + 8 * lane + 4) = f32x4{dd[12], dd[13], dd[14], dd[15]};
        last_wave(ldsC + 3, lane);
      }
    }
    if constexpr (p == 28) {
      if (has_f2 && w < 4) {
        wait_all(ldsC + 3);
        f32x4 dv = {0.f, 0.f, 0.f, 0.f};
#pragma unroll
        for (int ww = 0; ww < NWAVE; ++ww) dv += *(const f32x4*)(ldsY + ww * 1024 + 4 * tid);
        st_dense(rs, WS_DP + (cid * 256 + tid) * 16, dv, tagA, tagB);
      }
    }
    if constexpr (p == GROUPS + 3) {
      float x = fmaxf(fmaxf(sp[0], sp[1]), fmaxf(sp[2], sp[3]));
      x = fmaxf(x, dpp<0x128>(x)); x = fmaxf(x, dpp<0x124>(x)); x = fmaxf(x, dpp<0x122>(x)); x = fmaxf(x, dpp<0x121>(x));
      mw = xrow_max(x) * C2;
#pragma unroll
      for (int i = 0; i < 4; ++i) sp[i] = __builtin_amdgcn_exp2f(fmaf(sp[i], C2, -mw));
      const float lw = xrow_sum(row16_sum((sp[0] + sp[2]) + (sp[1] + sp[3])));
      if (lane == 0) { ldsM[slot0 & 7] = mw; ldsM[8 + (slot0 & 7)] = lw; }
      last_wave(ldsC + 4, lane);
    }
    if constexpr (p == 21) {
      if (w == 0) {
        wait_all(ldsC + 4);
        const float mi = ldsM[lane & 7], li = ldsM[8 + (lane & 7)];
        float M = fmaxf(mi, dpp<0x124>(mi)); M = fmaxf(M, dpp<0x122>(M)); M = fmaxf(M, dpp<0x121>(M));
        float L = __builtin_amdgcn_exp2f(mi - M) * li; L += dpp<0x124>(L); L += dpp<0x122>(L); L += dpp<0x121>(L);
        if (lane == 0) st_unit(rs, WS_ML + pid * 16, M, L, tagA, tagB);
        if (lane == 0) st_unit(rs, WS_DBG + bid * 16, __uint_as_float(DBG_ == 3 ? ldsC[10] : tstart), 0.f, tagA, tagB);
      }
    }
    if constexpr (p == 31) {
      await_unit(mu, rs, muoff, tagA, tagB);
      const float mi = __uint_as_float(mu[0]);
      float M = fmaxf(mi, dpp<0xB1>(mi)); M = fmaxf(M, dpp<0x4E>(M));
      float L = __builtin_amdgcn_exp2f(mi - M) * __uint_as_float(mu[2]); L += dpp<0xB1>(L); L += dpp<0x4E>(L);
      mw = __builtin_amdgcn_exp2f(mw - M) * __builtin_amdgcn_rcpf(L);
      ysto = g ? OOB : WS_Y + (pid * 128 + 16 * w + c) * 16;
      yoff = WS_Y + ((bc * 128 + gp) * 128 + cu0 + qd) * 16;
      asm volatile("" : "+v"(mw), "+v"(ysto), "+v"(yoff));
    }
    if constexpr (p >= 20 && p < 28) pv(IC<p - 20>{});
    if constexpr (p == 28 || p == 29) { pv(IC<2 * p - 48>{}); pv(IC<2 * p - 47>{}); }
    if constexpr (p == 30) pv(IC<12>{});
    if constexpr (p == 31) { pv(IC<13>{}); pv(IC<14>{}); pv(IC<15>{}); }
    if constexpr (p == 30) {
#pragma unroll
      for (int i = 0; i < 16; ++i) {
        const v2u r0 = wor[2 * i], r1 = wor[2 * i + 1];
        wb[0][i >> 2][i & 3] = __builtin_amdgcn_perm(r1[0], r0[0], 0x05040100u); wb[1][i >> 2][i & 3] = __builtin_amdgcn_perm(r1[0], r0[0], 0x07060302u);
        wb[2][i >> 2][i & 3] = __builtin_amdgcn_perm(r1[1], r0[1], 0x05040100u); wb[3][i >> 2][i & 3] = __builtin_amdgcn_perm(r1[1], r0[1], 0x07060302u);
      }
#pragma unroll
      for (int i = 0; i < 4; ++i) asm volatile("" : "+v"(wb[i][0]), "+v"(wb[i][1]), "+v"(wb[i][2]), "+v"(wb[i][3]));
    }
    SB();
  };
  static_for<2 * GROUPS>(point);

  {
    const f32x2 o2 = (bs[0] + bs[1]) * mw;
    const unsigned oh = pk_bf16(o2[0], o2[1]), ol = pk_bf16(o2[0] - lo(oh), o2[1] - hi(oh));
    const int ar = (slot0 & 7) * 68 + 4 * c + g;
    ldsA[ar] = oh; ldsA[8 * 68 + ar] = ol;
    if (is_fin && w < 3) {
      f32x4 e = await_dense(e0, rs, dpoff, tagA, tagB);
      if (gp >= NFFN) e = f32x4{0.f, 0.f, 0.f, 0.f};
      const float r = fold4(e[0], e[1], e[2], e[3]);
      if (c < 4) ldsF[w * 16 + 4 * c + g] = r;
    }
  }
  __syncthreads();
  {
    v4u af[4];
#pragma unroll
    for (int ks = 0; ks < 4; ++ks) af[ks] = *(const v4u*)(ldsA + c * 68 + 16 * ks + 4 * g);
    SB();
    f32x4 d[4];
#pragma unroll
    for (int t = 0; t < 4; ++t) d[t] = f32x4{0.f, 0.f, 0.f, 0.f};
#pragma unroll
    for (int ks = 0; ks < 4; ++ks)
#pragma unroll
      for (int t = 0; t < 4; ++t) d[t] = __builtin_amdgcn_mfma_f32_16x16x32_bf16(__builtin_bit_cast(bf16x8, af[ks]), __builtin_bit_cast(bf16x8, wb[t][ks]), d[t], 0, 0, 0);
    float y[4];
#pragma unroll
    for (int t = 0; t < 4; ++t) y[t] = (d[t][0] + d[t][1]) + (d[t][2] + d[t][3]);
    float u0 = fold32(y[0], y[2]), u1 = fold32(y[1], y[3]);
    u0 = fold16(u0, u0); u1 = fold16(u1, u1);
    const auto r0 = __builtin_amdgcn_permlane32_swap(__float_as_uint(u0), __float_as_uint(u0), false, false), r1 = __builtin_amdgcn_permlane32_swap(__float_as_uint(u1), __float_as_uint(u1), false, false);
    st_dense(rs, ysto, f32x4{__uint_as_float(r0[0]), __uint_as_float(r1[0]), __uint_as_float(r0[1]), __uint_as_float(r1[1])}, tagA, tagB);
  }

  if (is_fin) {
    __builtin_amdgcn_s_barrier();
    const float dv = ldsF[c] + ldsF[16 + c] + ldsF[32 + c];
    float gte = dv * __builtin_amdgcn_rcpf(1.f + __expf(-dv));
    asm volatile("" : "+v"(gte));
    __builtin_amdgcn_s_sleep(11);
    v4u y0;
    bool ok = false;
#pragma unroll 1
    for (unsigned spin = 0; spin < SPIN_CAP; ++spin) {
      asm volatile("" ::: "memory");
      if (!ok) { y0 = ld_unit(rs, yoff); ok = ok_dense(y0, tagA, tagB); }
      if (__all(ok)) break;
      __builtin_amdgcn_s_sleep(1);
    }
    const f32x4 yv = __builtin_bit_cast(f32x4, y0 & 0xfffffc00u);
    const float r = fold4(yv[0], yv[1], yv[2], yv[3]);
    if (c < 4) ldsF[128 + w * 16 + 4 * c + g] = r;
    if (cid == 0 && w == 0) {
      int mn = 1 << 30, mx = -(1 << 30);
      for (int i = 0; i < 4; ++i) { v4u u = ld_unit(rs, WS_DBG + (i * 64 + lane) * 16); await_unit(u, rs, WS_DBG + (i * 64 + lane) * 16, tagA, tagB);
        const int d = (int)(u[0] - tstart); mn = min(mn, d); mx = max(mx, d); }
      for (int o = 1; o < 64; o <<= 1) { mn = min(mn, __shfl_xor(mn, o)); mx = max(mx, __shfl_xor(mx, o)); }
      unsigned d2 = __builtin_amdgcn_readfirstlane(tstart + (DBG_ == 1 ? mn : mx) + 100 * 1600);
      pace_wait(d2, 0);
    }
    __syncthreads();
    if (tid < 16) {
      float sv = 0.f;
#pragma unroll
      for (int ww = 0; ww < NWAVE; ++ww) sv += ldsF[128 + ww * 16 + tid];
      const float o = __uint_as_float((unsigned)xo << 16) + sv * gte;
      const __bf16 ob = (__bf16)o;
      __builtin_amdgcn_raw_buffer_store_b16(__builtin_bit_cast(unsigned short, ob), RSRC(KAP(8), 2 * D * 2), ocol * 2, 0, 0);
      if (cid == 0 && tid == 0)
        __hip_atomic_store((GAS unsigned long long*)(pws + WS_EPOCH), ((unsigned long long)tagB << 32) | tagA, __ATOMIC_RELAXED, __HIP_MEMORY_SCOPE_AGENT);
    }
  }
  asm volatile(".Lk_all_end:");
}

extern "C" void kernel_launch(void* const* d_in, const int* in_sizes, int n_in, void* d_out, int out_size, void* d_ws, size_t ws_size,
                              hipStream_t stream) {
  Args a;
  a.x = (const unsigned short*)d_in[0];
  a.k = (const unsigned short*)d_in[1];
  a.v = (const unsigned short*)d_in[2];
  a.wq = (const unsigned short*)d_in[3];
  a.wo = (const unsigned short*)d_in[4];
  a.wg = (const unsigned short*)d_in[6];
  a.wu = (const unsigned short*)d_in[7];
  a.wd = (const unsigned short*)d_in[8];
  a.out = (unsigned short*)d_out;
  a.ws = (unsigned char*)d_ws;
  hipLaunchKernelGGL(k_all, dim3(256), dim3(NTHR), 0, stream, a);
}
```

```cpp
#include <hip/hip_runtime.h>
#include <hip/hip_bf16.h>
#include <stdint.h>

typedef unsigned v4u __attribute__((ext_vector_type(4)));
typedef float f32x4 __attribute__((ext_vector_type(4)));
typedef float f32x2 __attribute__((ext_vector_type(2)));
typedef __bf16 bf16x2 __attribute__((ext_vector_type(2)));
typedef __bf16 bf16x8 __attribute__((ext_vector_type(8)));
typedef unsigned v16u __attribute__((ext_vector_type(16)));
typedef unsigned v2u __attribute__((ext_vector_type(2)));
#define GAS __attribute__((address_space(1)))

constexpr int D = 512, H = 32, SK = 8192, DH = 128, F = 1376;
constexpr int NSPLIT = 4, KEYS_PER_WG = SK / NSPLIT;
constexpr int NWAVE = 8, NTHR = NWAVE * 64;
constexpr int NFFN = F / 32;
constexpr int U = 4;
constexpr int GROUPS = KEYS_PER_WG / NWAVE / (4 * U);
#define TP_ 1728
#define T0_ 2350
#define EX1_ 70
constexpr unsigned step_of(int p) { return TP_ + (p < 9 ? EX1_ : 0); }
constexpr int LATE_PRIO = 400;
constexpr int won(int p) { return p <= 1 ? 0 : p >= 28 ? 32 : (p - 1) * 32 / 27; }
static_assert(TP_ % 8 == 0, "a slot is TP / 8");

constexpr int WS_EPOCH = 0;
constexpr int WS_ML = 4096;
constexpr int WS_Y = WS_ML + 256 * 16;
constexpr int WS_DP = WS_Y + 256 * 128 * 16;
constexpr int WS_S1 = WS_DP + NFFN * 256 * 16;
constexpr int WS_END = WS_S1 + 32 * 8 * 2 * 64 * 16;
constexpr int OOB = 0x7ffffff0;
constexpr unsigned SPIN_CAP = 400000;

#define SB() __builtin_amdgcn_sched_barrier(0)

__device__ __forceinline__ float dot2(unsigned k, unsigned q, float acc) {
  return __builtin_amdgcn_fdot2_f32_bf16(__builtin_bit_cast(bf16x2, k), __builtin_bit_cast(bf16x2, q), acc, false);
}
template <int CTRL> __device__ __forceinline__ float dpp(float x) {
  return __builtin_bit_cast(float, __builtin_amdgcn_mov_dpp(__builtin_bit_cast(int, x), CTRL, 0xf, 0xf, true));
}
__device__ __forceinline__ float row16_sum(float x) {
  x += dpp<0x128>(x);
  x += dpp<0x124>(x);
  x += dpp<0x122>(x);
  x += dpp<0x121>(x);
  return x;
}
__device__ __forceinline__ float fold32(float a, float b) {
  auto t = __builtin_amdgcn_permlane32_swap(__float_as_uint(a), __float_as_uint(b), false, false);
  return __uint_as_float(t[0]) + __uint_as_float(t[1]);
}
__device__ __forceinline__ float fold16(float a, float b) {
  auto t = __builtin_amdgcn_permlane16_swap(__float_as_uint(a), __float_as_uint(b), false, false);
  return __uint_as_float(t[0]) + __uint_as_float(t[1]);
}
__device__ __forceinline__ float xrow_sum(float x) { x = fold16(x, x); return fold32(x, x); }
__device__ __forceinline__ float xrow_max(float x) {
  auto s = __builtin_amdgcn_permlane16_swap(__float_as_uint(x), __float_as_uint(x), false, false);
  x = fmaxf(__uint_as_float(s[0]), __uint_as_float(s[1]));
  auto t = __builtin_amdgcn_permlane32_swap(__float_as_uint(x), __float_as_uint(x), false, false);
  return fmaxf(__uint_as_float(t[0]), __uint_as_float(t[1]));
}
__device__ __forceinline__ f32x2 fold8(const float (&v)[8]) {
  const float u0 = fold32(v[0], v[4]), u1 = fold32(v[1], v[5]), u2 = fold32(v[2], v[6]), u3 = fold32(v[3], v[7]);
  return f32x2{fold16(u0, u2), fold16(u1, u3)};
}
__device__ __forceinline__ float fold4(float v0, float v1, float v2, float v3) {
  float r = fold16(fold32(v0, v2), fold32(v1, v3));
  r += dpp<0x124>(r); r += dpp<0x128>(r);
  return r;
}
__device__ __forceinline__ float lo(unsigned u) { return __uint_as_float(u << 16); }
__device__ __forceinline__ float hi(unsigned u) { return __uint_as_float(u & 0xffff0000u); }
__device__ __forceinline__ unsigned pk_bf16(float a, float b) {
  bf16x2 r = {(__bf16)a, (__bf16)b};
  return __builtin_bit_cast(unsigned, r);
}
__device__ __forceinline__ unsigned sel4(v4u v, int g) { const unsigned a = (g & 1) ? v[1] : v[0], b = (g & 1) ? v[3] : v[2]; return (g & 2) ? b : a; }
__device__ __forceinline__ int s1off(int hg, int r, int k) { return WS_S1 + (hg * 8 + ((r + hg) & 7)) * 2048 + ((k ^ (hg >> 3)) & 1) * 1024; }
__device__ __forceinline__ float gelu_tanh(float x) {
  const float u = 0.7978845608028654f * (x + 0.044715f * x * x * x);
  const float e = __expf(2.f * u);
  const float t = 1.f - 2.f * __builtin_amdgcn_rcpf(e + 1.f);
  return 0.5f * x * (1.f + t);
}
__device__ __forceinline__ void pace_wait(unsigned& due16, unsigned step16) {
  asm volatile("1:\n\ts_memrealtime s[90:91]\n\ts_waitcnt lgkmcnt(0)\n\ts_lshl_b32 s90, s90, 4\n\ts_sub_u32 s91, s90, %0\n\ts_cmp_ge_i32 s91, 0\n\ts_cbranch_scc1 2f\n\ts_sleep 2\n\ts_branch 1b\n"
               "2:\n\ts_add_u32 %0, %0, %1\n\ts_setprio 0\n\ts_cmp_lt_i32 s91, %2\n\ts_cbranch_scc1 3f\n\ts_setprio 2\n3:"
               : "+s"(due16) : "s"(step16), "i"(LATE_PRIO) : "s90", "s91", "scc");
}
__device__ __forceinline__ unsigned now16() { return (unsigned)__builtin_amdgcn_s_memrealtime() << 4; }
__device__ __forceinline__ unsigned next_tag(unsigned e) { return ((e + 1u) & 1023u) | ((e + 1024u) & (1023u << 10)) | ((e + (1u << 20)) & 0xfff00000u); }
constexpr int AUX_SC1 = 16;
__device__ __forceinline__ void st_unit(__amdgpu_buffer_rsrc_t rs, int off, float v0, float v1, unsigned ta, unsigned tb) {
  __builtin_amdgcn_raw_buffer_store_b128(v4u{__float_as_uint(v0), ta, __float_as_uint(v1), tb}, rs, off, 0, AUX_SC1);
}
__device__ __forceinline__ v4u ld_unit(__amdgpu_buffer_rsrc_t rs, int off) { return __builtin_amdgcn_raw_buffer_load_b128(rs, off, 0, AUX_SC1); }
__device__ __forceinline__ unsigned enc22(float v, unsigned t) { return ((__float_as_uint(v) + 0x200u) & 0xfffffc00u) | t; }
__device__ __forceinline__ void st_dense(__amdgpu_buffer_rsrc_t rs, int off, f32x4 v, unsigned ta, unsigned tb) {
  __builtin_amdgcn_raw_buffer_store_b128(v4u{enc22(v[0], ta & 1023u), enc22(v[1], (ta >> 10) & 1023u), enc22(v[2], (ta >> 20) & 1023u), enc22(v[3], tb & 1023u)}, rs, off, 0, AUX_SC1);
}
__device__ __forceinline__ bool ok_dense(v4u u, unsigned ta, unsigned tb) { return ((((u[0] ^ ta) | (u[1] ^ (ta >> 10))) | ((u[2] ^ (ta >> 20)) | (u[3] ^ tb))) & 1023u) == 0u; }
__device__ __forceinline__ f32x4 await_dense(v4u u, __amdgpu_buffer_rsrc_t rs, int off, unsigned ta, unsigned tb) {
  if (!__all(ok_dense(u, ta, tb))) {
#pragma unroll 1
    for (unsigned spin = 0; spin < SPIN_CAP; ++spin) {
      __builtin_amdgcn_s_sleep(1);
      asm volatile("" ::: "memory");
      u = ld_unit(rs, off);
      if (__all(ok_dense(u, ta, tb))) break;
    }
  }
  return __builtin_bit_cast(f32x4, u & 0xfffffc00u);
}
__device__ __forceinline__ void await_unit(v4u& u, __amdgpu_buffer_rsrc_t rs, int off, unsigned ta, unsigned tb) {
  if (!__all(((u[1] ^ ta) | (u[3] ^ tb)) == 0u)) {
#pragma unroll 1
    for (unsigned spin = 0; spin < SPIN_CAP; ++spin) {
      __builtin_amdgcn_s_sleep(1);
      asm volatile("" ::: "memory");
      u = ld_unit(rs, off);
      if (__all(((u[1] ^ ta) | (u[3] ^ tb)) == 0u)) break;
    }
  }
}

__device__ __forceinline__ bool last_wave(unsigned* cnt, int lane) {
  asm volatile("s_waitcnt lgkmcnt(0)" ::: "memory");
  unsigned old = 0;
  if (lane == 0) old = __hip_atomic_fetch_add(cnt, 1u, __ATOMIC_RELAXED, __HIP_MEMORY_SCOPE_WORKGROUP);
  old = __builtin_amdgcn_readfirstlane(old);
  asm volatile("" ::: "memory");
  return old == NWAVE - 1;
}

__device__ __forceinline__ void wait_all(unsigned* cnt, unsigned n = NWAVE) {
  while (__builtin_amdgcn_readfirstlane(__hip_atomic_load(cnt, __ATOMIC_RELAXED, __HIP_MEMORY_SCOPE_WORKGROUP)) < n) __builtin_amdgcn_s_sleep(1);
  asm volatile("" ::: "memory");
}
template <int I> struct IC { static constexpr int value = I; };
template <int N, int I = 0, typename Fn> __device__ __forceinline__ void static_for(Fn& fn) { if constexpr (I < N) { fn(IC<I>{}); static_for<N, I + 1>(fn); } }

struct Args {
  const unsigned short* x; const unsigned short* wq;
  const unsigned short* k; const unsigned short* v; const unsigned short* wo; const unsigned short* wg;
  const unsigned short* wu; const unsigned short* wd; unsigned short* out; unsigned char* ws;
};

__global__ void __launch_bounds__(NTHR, 2) k_all(Args a) {
  __shared__ __attribute__((aligned(16))) float lds[8 * 1024 + 16 * 68 + 16 + 8 * 128 + 8 * 128 + 32 + 16];
  float* ldsY = lds;
  unsigned* ldsA = (unsigned*)(lds + 8 * 1024);
  float* ldsM = lds + 8 * 1024 + 16 * 68;
  float* ldsF = ldsM + 16;
  float* ldsQ = ldsF + 8 * 128;
  float* ldsS = ldsQ + 8 * 128;
  unsigned* ldsC = (unsigned*)(ldsS + 32);
  const int bid = blockIdx.x;
  const int xcd = bid & 7, j = bid >> 3;
  const int tid = threadIdx.x, lane = tid & 63, w = __builtin_amdgcn_readfirstlane(tid >> 6), lo16 = lane * 16;
  v16u ka; v4u kb; unsigned long long tptr, pc0; unsigned csz;
  asm volatile("s_load_dwordx16 %0, %5, 0x0\n\ts_load_dwordx4 %1, %5, 0x40\n\ts_load_dwordx2 %2, %5, %6 offset:0x10\n\t"
               "s_getpc_b64 %3\n.Lk_all_pc0:\n\ts_mov_b32 %4, (.Lk_all_end-.Lk_all_pc0)&0xfffffff0"
               : "=&s"(ka), "=&s"(kb), "=&s"(tptr), "=&s"(pc0), "=&s"(csz) : "s"(__builtin_amdgcn_kernarg_segment_ptr()), "s"(8 * w));
#define RSRC(p_, n_) __builtin_amdgcn_make_buffer_rsrc((void*)(p_), 0, (n_), 0x00020000)
  const v4u pfc = __builtin_amdgcn_raw_buffer_load_b128(RSRC(pc0 & ~127ull, w == 4 ? csz : 0u), lo16, j * 1024, 0);
  ldsC[tid & 15] = 0u;
  SB();
  const int h = xcd * 4 + (j >> 3), b = (j >> 2) & 1, s = j & 3;
  const int g = lane >> 4, c = lane & 15;
  const int r8 = j & 7;
  int qwo = (2 * g * H * DH + 8 * c) * 2, xso = (lane >> 5) * D * 2 + (lane & 31) * 4, qso = ((64 * r8 + 8 * w) * H * DH + h * DH) * 2;
  unsigned tof = ((unsigned)(b * H + h) * ((0x03u >> w) & 1u) << 21) + ((unsigned)h * ((0x04u >> w) & 1u) << 17);
  asm volatile("" : "+v"(qwo), "+v"(xso), "+v"(tof));
  asm volatile("s_waitcnt lgkmcnt(0)\n\ts_barrier" : "+s"(ka), "+s"(kb), "+s"(tptr), "+s"(qso) :: "memory");
#define KAP(i) ((i) < 8 ? (unsigned long long)ka[2 * (i) % 16] | ((unsigned long long)ka[(2 * (i) + 1) % 16] << 32) : (unsigned long long)kb[2 * (i) % 4] | ((unsigned long long)kb[(2 * (i) + 1) % 4] << 32))
  const unsigned long long px = KAP(0), pws = KAP(9);
  const unsigned long long ep = __hip_atomic_load((const GAS unsigned long long*)(tptr + tof), __ATOMIC_RELAXED, __HIP_MEMORY_SCOPE_AGENT);
  SB();
  const unsigned xst = __builtin_amdgcn_raw_buffer_load_b32(RSRC(px, 2 * D * 2), xso, 128 * w, 0);
  SB();
  const __amdgpu_buffer_rsrc_t rq = RSRC(KAP(1), D * H * DH * 2);
  const v4u sw0 = __builtin_amdgcn_raw_buffer_load_b128(rq, qwo, qso, 2), sw1 = __builtin_amdgcn_raw_buffer_load_b128(rq, qwo, qso + H * DH * 2, 2);
  v4u xq0, xq1;
  asm volatile("s_load_dwordx4 %0, %2, 0x0\n\ts_load_dwordx4 %1, %2, 0x400" : "=&s"(xq0), "=&s"(xq1) : "s"(px + (64 * r8 + 8 * w) * 2));
  SB();
  const __amdgpu_buffer_rsrc_t rw = RSRC(KAP(4) + (size_t)h * DH * D * 2, DH * D * 2);
  const v4u pf0 = __builtin_amdgcn_raw_buffer_load_b128(rw, lo16, (16 * r8 + 2 * w) * 1024, 0), pf1 = __builtin_amdgcn_raw_buffer_load_b128(rw, lo16 + 1024, (16 * r8 + 2 * w) * 1024, 0);
  SB();
  const int fb = xcd * 3 + (j >> 3);
  const bool has_f1 = (j < 24) && (fb < 22);
  const bool is_fin = j >= 24;
  const int cid = xcd * 8 + (j - 24);
  const bool has_f2 = is_fin && cid < NFFN;
  const int row0 = 64 * r8 + 8 * w + 2 * g;
  const int fcol = 128 * (fb < 11 ? fb : fb - 11) + 8 * c;
  int fx1 = (row0 * F + fcol) * 2, fx2 = (32 * cid + 4 * w) * D * 2 + lo16;
  asm volatile("" : "+v"(fx1), "+v"(fx2));
  const int fxo = has_f1 ? (fcol < F ? fx1 : OOB - 4 * F * 2) : fx2, fxo2 = has_f1 ? OOB - 4096 : fx2, fst = has_f1 ? F * 2 : D * 2;
  unsigned f_lo = fb < 11 ? ka[10] : ka[12], f_hi = fb < 11 ? ka[11] : ka[13];
  asm volatile("" : "+s"(f_lo), "+s"(f_hi));
  const __amdgpu_buffer_rsrc_t rf = RSRC((unsigned long long)(has_f1 ? f_lo : ka[14]) | ((unsigned long long)(has_f1 ? f_hi : ka[15]) << 32), (has_f1 || has_f2) ? D * F * 2 : 0);
  v4u ffw[4];
  ffw[0] = __builtin_amdgcn_raw_buffer_load_b128(rf, fxo, 0, 2);
  ffw[1] = __builtin_amdgcn_raw_buffer_load_b128(rf, fxo, fst, 2);
  ffw[2] = __builtin_amdgcn_raw_buffer_load_b128(rf, fxo2 + 2048, 0, 2);
  ffw[3] = __builtin_amdgcn_raw_buffer_load_b128(rf, fxo2 + 3072, 0, 2);
  SB();
  if (w == 0) {
    asm volatile("" :: "v"(ep));
    __hip_atomic_store(ldsC + 10, now16() | 1u, __ATOMIC_RELAXED, __HIP_MEMORY_SCOPE_WORKGROUP);
  }
  SB();
  const __amdgpu_buffer_rsrc_t rs = RSRC(pws, WS_END);

  const __amdgpu_buffer_rsrc_t rk = RSRC(KAP(2) + (size_t)(b * H + h) * SK * DH * 2, SK * DH * 2);
  const __amdgpu_buffer_rsrc_t rv = RSRC(KAP(3) + (size_t)(b * H + h) * SK * DH * 2, SK * DH * 2);
#define GOFF(gi_) (((gi_) & 1) * ((1 << 20) | (1 << 12)) | ((gi_) & 2) * ((1 << 18) | (1 << 13)) | (((gi_) & 4) << 16) | (((gi_) & 8) << 14))
  v4u rg[2 * GROUPS][U];

  constexpr float C2 = 0.08838834764831845f * 1.4426950408889634f;
  float sp[4] = {0.f, 0.f, 0.f, 0.f};
  float mw = 0.f;
  f32x2 bs[8];
  unsigned tagA, tagB, qp[4], slot0;
  v2u wor[32]; v4u wb[4][4], fu, qu, mu, e0; unsigned short xo;
  unsigned xs0, xs1;
  int fuoff, ysto, yoff;
  const int wvo = g * 8192 + w * 128 + c * 8;
  const int qoff = s1off(h, w, 0) + (b * 32 + (lane & 31)) * 16;
  const int pid = (b * H + h) * NSPLIT + s, muoff = WS_ML + ((pid & ~3) + (lane & 3)) * 16;
  const int bc = (cid >> 5) & 1, cu0 = (cid & 31) * 4;
  const int gp = tid >> 2, qd = tid & 3, ocol = bc * D + 4 * cu0 + c;
  const int dpoff = (is_fin && w < 3) ? WS_DP + ((gp < NFFN ? gp : NFFN - 1) * 256 + bc * 128 + cu0 + qd) * 16 : OOB;

  auto qk = [&](auto gic) __attribute__((always_inline)) {
    constexpr int gi = decltype(gic)::value;
    float d[U];
#pragma unroll
    for (int u = 0; u < U; ++u) d[u] = dot2(rg[gi][u][3], qp[3], dot2(rg[gi][u][2], qp[2], dot2(rg[gi][u][1], qp[1], dot2(rg[gi][u][0], qp[0], 0.f))));
    const bool o1 = c & 1, o2 = c & 2;
    const float x0 = (o1 ? d[1] : d[0]) + dpp<0xB1>(o1 ? d[0] : d[1]), x1 = (o1 ? d[3] : d[2]) + dpp<0xB1>(o1 ? d[2] : d[3]);
    float x = (o2 ? x1 : x0) + dpp<0x4E>(o2 ? x0 : x1);
    x += dpp<0x128>(x); x += dpp<0x124>(x);
    sp[gi / 4] = __builtin_bit_cast(float, __builtin_amdgcn_update_dpp(__builtin_bit_cast(int, sp[gi / 4]), __builtin_bit_cast(int, x), 0xE4, 0xf, 1 << (gi % 4), false));
    asm volatile("" : "+v"(sp[gi / 4]));
    SB();
  };
  auto pv = [&](auto gic) __attribute__((always_inline)) {
    constexpr int gi = decltype(gic)::value;
    const float p[4] = {dpp<0x150 + 4 * (gi % 4)>(sp[gi / 4]), dpp<0x151 + 4 * (gi % 4)>(sp[gi / 4]), dpp<0x152 + 4 * (gi % 4)>(sp[gi / 4]), dpp<0x153 + 4 * (gi % 4)>(sp[gi / 4])};
    float x[8];
#pragma unroll
    for (int k = 0; k < 4; ++k) { x[2 * k] = p[0] * lo(rg[GROUPS + gi][0][k]); x[2 * k + 1] = p[0] * hi(rg[GROUPS + gi][0][k]); }
#pragma unroll
    for (int u = 1; u < U; ++u) {
#pragma unroll
      for (int k = 0; k < 4; ++k) { x[2 * k] = fmaf(p[u], lo(rg[GROUPS + gi][u][k]), x[2 * k]); x[2 * k + 1] = fmaf(p[u], hi(rg[GROUPS + gi][u][k]), x[2 * k + 1]); }
    }
    const f32x2 y = fold8(x);
    if constexpr (gi < 8) bs[gi] = y; else bs[gi - 8] += y;
    if constexpr (gi >= 12) bs[gi - 12] += bs[gi - 8];
    if constexpr (gi >= 14) bs[gi - 14] += bs[gi - 12];
    asm volatile("" : "+v"(bs[gi >= 14 ? gi - 14 : gi >= 12 ? gi - 12 : gi % 8]));
    SB();
  };

  {
    unsigned tk;
    while ((tk = __builtin_amdgcn_readfirstlane(__hip_atomic_load(ldsC + 10, __ATOMIC_RELAXED, __HIP_MEMORY_SCOPE_WORKGROUP))) == 0u) __builtin_amdgcn_s_sleep(1);
    slot0 = __builtin_amdgcn_readfirstlane((__builtin_amdgcn_readfirstlane(tk) + (unsigned)T0_) / (unsigned)(TP_ / 8) + (unsigned)w);
  }
  const int kvo = ((slot0 & 1) << 13) | ((slot0 & 6) << 14) | ((s & 1) << 12) | ((s >> 1) << 14);
  unsigned due = __builtin_amdgcn_readfirstlane(slot0 * (unsigned)(TP_ / 8));

  auto point = [&](auto pc) __attribute__((always_inline)) {
    constexpr int p = decltype(pc)::value;
    int so = kvo ^ GOFF((slot0 >> 3) + p);
    asm volatile("" : "+s"(so));
    pace_wait(due, step_of(p));
    SB();
#pragma unroll
    for (int u = 0; u < U; ++u) rg[p][u] = __builtin_amdgcn_raw_buffer_load_b128(p < GROUPS ? rk : rv, lo16 + u * 1024, so, 2);
    SB();
    if constexpr (p == 4) qu = ld_unit(rs, qoff);
    if constexpr (p == 7) fu = ld_unit(rs, fuoff);
    if constexpr (p == 29) mu = ld_unit(rs, muoff);
#pragma unroll
    for (int i = won(p); i < won(p + 1); ++i) wor[i] = __builtin_amdgcn_raw_buffer_load_b64(rw, wvo, (32 * (i >> 3) + (i & 7)) * 1024, 0);
    if constexpr (p == 31) {
      e0 = ld_unit(rs, dpoff);
      xo = ((const GAS unsigned short*)px)[ocol];
    }
    SB();
    if constexpr (p == 0) {
      asm volatile("s_waitcnt lgkmcnt(0)" : "+s"(xq0), "+s"(xq1));
      xs0 = sel4(xq0, g); xs1 = sel4(xq1, g);
      float sm = row16_sum(lo(xst) + hi(xst)); sm = fold16(sm, sm) * (1.f / 64);
      const float d0 = lo(xst) - sm, d1 = hi(xst) - sm;
      float m2 = row16_sum(fmaf(d0, d0, d1 * d1)); m2 = fold16(m2, m2);
      if ((lane & 31) == 0) *(f32x2*)(ldsS + (lane >> 5) * 16 + 2 * w) = f32x2{sm, m2};
      float t[24];
#pragma unroll
      for (int k = 0; k < 4; ++k) {
        const float al = lo(sw0[k]), ah = hi(sw0[k]), bl = lo(sw1[k]), bh = hi(sw1[k]);
        t[2 * k] = fmaf(hi(xs0), bl, lo(xs0) * al); t[2 * k + 1] = fmaf(hi(xs0), bh, lo(xs0) * ah);
        t[8 + 2 * k] = fmaf(hi(xs1), bl, lo(xs1) * al); t[8 + 2 * k + 1] = fmaf(hi(xs1), bh, lo(xs1) * ah);
        t[16 + 2 * k] = al + bl; t[16 + 2 * k + 1] = ah + bh;
      }
#pragma unroll
      for (int k = 0; k < 8; ++k) t[k] = fold16(fold32(t[k], t[16 + k]), fold32(t[8 + k], t[16 + k]));
      *(f32x4*)(ldsY + w * 512 + g * 128 + 8 * c) = f32x4{t[0], t[1], t[2], t[3]};
      *(f32x4*)(ldsY + w * 512 + g * 128 + 8 * c + 4) = f32x4{t[4], t[5], t[6], t[7]};
      if (tid == 448) *(unsigned long long*)(ldsC + 8) = ep;
      asm volatile("" :: "v"(ep));
      if (last_wave(ldsC, lane)) {
        f32x4 st[8];
#pragma unroll
        for (int i = 0; i < 8; ++i) st[i] = *(const f32x4*)(ldsS + 4 * i);
        tagA = next_tag(__builtin_amdgcn_readfirstlane(ldsC[8])); tagB = __builtin_amdgcn_readfirstlane(ldsC[9]) + 0x9E3779B9u;
        const float mean0 = ((st[0][0] + st[0][2]) + (st[1][0] + st[1][2]) + (st[2][0] + st[2][2]) + (st[3][0] + st[3][2])) * 0.125f;
        const float mean1 = ((st[4][0] + st[4][2]) + (st[5][0] + st[5][2]) + (st[6][0] + st[6][2]) + (st[7][0] + st[7][2])) * 0.125f;
        float q0 = 0.f, q1 = 0.f, d0 = 0.f, d1 = 0.f;
#pragma unroll
        for (int i = 0; i < 4; ++i) {
          q0 += st[i][1] + st[i][3]; q1 += st[4 + i][1] + st[4 + i][3];
          const float a0 = st[i][0] - mean0, b0 = st[i][2] - mean0, a1 = st[4 + i][0] - mean1, b1 = st[4 + i][2] - mean1;
          d0 += fmaf(a0, a0, b0 * b0); d1 += fmaf(a1, a1, b1 * b1);
        }
        const float rstd0 = __builtin_amdgcn_rsqf(fmaf(64.f, d0, q0) * (1.f / D) + 1e-5f), rstd1 = __builtin_amdgcn_rsqf(fmaf(64.f, d1, q1) * (1.f / D) + 1e-5f);
        if (lane == 0) *(f32x4*)(ldsC + 12) = f32x4{mean0, rstd0, mean1, rstd1};
        last_wave(ldsC, lane);
        f32x4 z = {0.f, 0.f, 0.f, 0.f}, cs = {0.f, 0.f, 0.f, 0.f};
#pragma unroll
        for (int ww = 0; ww < NWAVE; ++ww) { z += *(const f32x4*)(ldsY + ww * 512 + 4 * lane); cs += *(const f32x4*)(ldsY + ww * 512 + 256 + 4 * (lane & 31)); }
        st_dense(rs, s1off(h, r8, 0) + lo16, (z - cs * (lane < 32 ? mean0 : mean1)) * (lane < 32 ? rstd0 : rstd1), tagA, tagB);
      }
    }
    if constexpr (p == 2) {
      wait_all(ldsC, NWAVE + 1);
      tagA = next_tag(__builtin_amdgcn_readfirstlane(ldsC[8])); tagB = __builtin_amdgcn_readfirstlane(ldsC[9]) + 0x9E3779B9u;
      asm volatile("" :: "v"(pf0), "v"(pf1), "v"(pfc));
      if (has_f1) {
        const f32x4 ln = *(const f32x4*)(ldsC + 12);
        const float n00 = (lo(xs0) - ln[0]) * ln[1], n01 = (hi(xs0) - ln[0]) * ln[1], n10 = (lo(xs1) - ln[2]) * ln[3], n11 = (hi(xs1) - ln[2]) * ln[3];
        float t[8];
#pragma unroll
        for (int k = 0; k < 4; ++k) {
          const float al = lo(ffw[0][k]), ah = hi(ffw[0][k]), bl = lo(ffw[1][k]), bh = hi(ffw[1][k]);
          t[2 * k] = fold32(fmaf(n01, bl, n00 * al), fmaf(n11, bl, n10 * al)); t[2 * k + 1] = fold32(fmaf(n01, bh, n00 * ah), fmaf(n11, bh, n10 * ah));
        }
        *(f32x4*)(ldsY + 4096 + w * 256 + (g >> 1) * 128 + 8 * c + 4 * (g & 1)) = f32x4{fold16(t[0], t[4]), fold16(t[1], t[5]), fold16(t[2], t[6]), fold16(t[3], t[7])};
        last_wave(ldsC + 2, lane);
      }
    }
    if constexpr (p == 5) {
      int gq = g; asm volatile("" : "+v"(gq));
      const int fbs = (gq >> 1) * 11 + (cid >> 2), hs = (fbs / 3) * 4 + fbs % 3;
      fuoff = has_f2 ? s1off(hs, w, 1) + ((g & 1) * 32 + 8 * (cid & 3) + (c & 7)) * 16 : OOB;
      asm volatile("" : "+v"(fuoff));
    }
    if constexpr (p == 4) {
      if (has_f1 && w == 0) {
        wait_all(ldsC + 2);
        f32x4 z = {0.f, 0.f, 0.f, 0.f};
#pragma unroll
        for (int ww = 0; ww < NWAVE; ++ww) z += *(const f32x4*)(ldsY + 4096 + ww * 256 + 4 * lane);
        st_dense(rs, s1off(h, r8, 1) + lo16, z, tagA, tagB);
      }
    }
    if constexpr (p == 6) {
      *(f32x4*)(ldsQ + w * 128 + 4 * (lane & 31)) = await_dense(qu, rs, qoff, tagA, tagB);
      last_wave(ldsC + 1, lane);
    }
    if constexpr (p == 8) {
      wait_all(ldsC + 1);
      f32x4 q0 = {0, 0, 0, 0}, q1 = {0, 0, 0, 0};
#pragma unroll
      for (int ww = 0; ww < NWAVE; ++ww) { q0 += *(const f32x4*)(ldsQ + ww * 128 + 8 * c); q1 += *(const f32x4*)(ldsQ + ww * 128 + 8 * c + 4); }
      qp[0] = pk_bf16(q0[0], q0[1]); qp[1] = pk_bf16(q0[2], q0[3]); qp[2] = pk_bf16(q1[0], q1[1]); qp[3] = pk_bf16(q1[2], q1[3]);
      qk(IC<0>{});
    }
    if constexpr (p == 9) { qk(IC<1>{}); qk(IC<2>{}); qk(IC<3>{}); }
    if constexpr (p == 10) { qk(IC<4>{}); qk(IC<5>{}); }
    if constexpr (p >= 10 && p < GROUPS + 4) qk(IC<p - 4>{});
    if constexpr (p == 10) {
      if (has_f2) {
        *(f32x4*)(ldsF + w * 128 + g * 32 + 4 * (c & 7)) = await_dense(fu, rs, fuoff, tagA, tagB);
        last_wave(ldsC + 5, lane);
      }
    }
    if constexpr (p == 12) {
      if (has_f2) {
        wait_all(ldsC + 5);
        float gs = 0.f, us = 0.f;
        const int hx = ((lane >> 2) & 1) * 32 + 4 * w + (lane & 3);
#pragma unroll
        for (int ww = 0; ww < NWAVE; ++ww) { gs += ldsF[ww * 128 + hx]; us += ldsF[ww * 128 + 64 + hx]; }
        const float hv = gelu_tanh(gs) * us;
        float dd[16];
#pragma unroll
        for (int k = 0; k < 16; ++k) dd[k] = 0.f;
#pragma unroll
        for (int r = 0; r < 4; ++r) {
          const float h0 = __builtin_bit_cast(float, __builtin_amdgcn_readlane(__builtin_bit_cast(int, hv), r));
          const float h1 = __builtin_bit_cast(float, __builtin_amdgcn_readlane(__builtin_bit_cast(int, hv), 4 + r));
#pragma unroll
          for (int k = 0; k < 4; ++k) {
            const float wl = lo(ffw[r][k]), wh = hi(ffw[r][k]);
            dd[2 * k] = fmaf(h0, wl, dd[2 * k]); dd[2 * k + 1] = fmaf(h0, wh, dd[2 * k + 1]);
            dd[8 + 2 * k] = fmaf(h1, wl, dd[8 + 2 * k]); dd[8 + 2 * k + 1] = fmaf(h1, wh, dd[8 + 2 * k + 1]);
          }
        }
        *(f32x4*)(ldsY + w * 1024 + 8 * lane) = f32x4{dd[0], dd[1], dd[2], dd[3]};
        *(f32x4*)(ldsY + w * 1024 + 8 * lane + 4) = f32x4{dd[4], dd[5], dd[6], dd[7]};
        *(f32x4*)(ldsY + w * 1024 + 512 + 8 * lane) = f32x4{dd[8], dd[9], dd[10], dd[11]};
        *(f32x4*)(ldsY + w * 1024 + 512 + 8 * lane + 4) = f32x4{dd[12], dd[13], dd[14], dd[15]};
        last_wave(ldsC + 3, lane);
      }
    }
    if constexpr (p == 28) {
      if (has_f2 && w < 4) {
        wait_all(ldsC + 3);
        f32x4 dv = {0.f, 0.f, 0.f, 0.f};
#pragma unroll
        for (int ww = 0; ww < NWAVE; ++ww) dv += *(const f32x4*)(ldsY + ww * 1024 + 4 * tid);
        st_dense(rs, WS_DP + (cid * 256 + tid) * 16, dv, tagA, tagB);
      }
    }
    if constexpr (p == GROUPS + 3) {
      float x = fmaxf(fmaxf(sp[0], sp[1]), fmaxf(sp[2], sp[3]));
      x = fmaxf(x, dpp<0x128>(x)); x = fmaxf(x, dpp<0x124>(x)); x = fmaxf(x, dpp<0x122>(x)); x = fmaxf(x, dpp<0x121>(x));
      mw = xrow_max(x) * C2;
#pragma unroll
      for (int i = 0; i < 4; ++i) sp[i] = __builtin_amdgcn_exp2f(fmaf(sp[i], C2, -mw));
      const float lw = xrow_sum(row16_sum((sp[0] + sp[2]) + (sp[1] + sp[3])));
      if (lane == 0) { ldsM[slot0 & 7] = mw; ldsM[8 + (slot0 & 7)] = lw; }
      last_wave(ldsC + 4, lane);
    }
    if constexpr (p == 21) {
      if (w == 0) {
        wait_all(ldsC + 4);
        const float mi = ldsM[lane & 7], li = ldsM[8 + (lane & 7)];
        float M = fmaxf(mi, dpp<0x124>(mi)); M = fmaxf(M, dpp<0x122>(M)); M = fmaxf(M, dpp<0x121>(M));
        float L = __builtin_amdgcn_exp2f(mi - M) * li; L += dpp<0x124>(L); L += dpp<0x122>(L); L += dpp<0x121>(L);
        if (lane == 0) st_unit(rs, WS_ML + pid * 16, M, L, tagA, tagB);
      }
    }
    if constexpr (p == 31) {
      await_unit(mu, rs, muoff, tagA, tagB);
      const float mi = __uint_as_float(mu[0]);
      float M = fmaxf(mi, dpp<0xB1>(mi)); M = fmaxf(M, dpp<0x4E>(M));
      float L = __builtin_amdgcn_exp2f(mi - M) * __uint_as_float(mu[2]); L += dpp<0xB1>(L); L += dpp<0x4E>(L);
      mw = __builtin_amdgcn_exp2f(mw - M) * __builtin_amdgcn_rcpf(L);
      ysto = g ? OOB : WS_Y + (pid * 128 + 16 * w + c) * 16;
      yoff = WS_Y + ((bc * 128 + gp) * 128 + cu0 + qd) * 16;
      asm volatile("" : "+v"(mw), "+v"(ysto), "+v"(yoff));
    }
    if constexpr (p >= 20 && p < 28) pv(IC<p - 20>{});
    if constexpr (p == 28 || p == 29) { pv(IC<2 * p - 48>{}); pv(IC<2 * p - 47>{}); }
    if constexpr (p == 30) pv(IC<12>{});
    if constexpr (p == 31) { pv(IC<13>{}); pv(IC<14>{}); pv(IC<15>{}); }
    if constexpr (p == 30) {
#pragma unroll
      for (int i = 0; i < 16; ++i) {
        const v2u r0 = wor[2 * i], r1 = wor[2 * i + 1];
        wb[0][i >> 2][i & 3] = __builtin_amdgcn_perm(r1[0], r0[0], 0x05040100u); wb[1][i >> 2][i & 3] = __builtin_amdgcn_perm(r1[0], r0[0], 0x07060302u);
        wb[2][i >> 2][i & 3] = __builtin_amdgcn_perm(r1[1], r0[1], 0x05040100u); wb[3][i >> 2][i & 3] = __builtin_amdgcn_perm(r1[1], r0[1], 0x07060302u);
      }
#pragma unroll
      for (int i = 0; i < 4; ++i) asm volatile("" : "+v"(wb[i][0]), "+v"(wb[i][1]), "+v"(wb[i][2]), "+v"(wb[i][3]));
    }
    SB();
  };
  static_for<2 * GROUPS>(point);

  {
    const f32x2 o2 = (bs[0] + bs[1]) * mw;
    const unsigned oh = pk_bf16(o2[0], o2[1]), ol = pk_bf16(o2[0] - lo(oh), o2[1] - hi(oh));
    const int ar = (slot0 & 7) * 68 + 4 * c + g;
    ldsA[ar] = oh; ldsA[8 * 68 + ar] = ol;
    if (is_fin && w < 3) {
      f32x4 e = await_dense(e0, rs, dpoff, tagA, tagB);
      if (gp >= NFFN) e = f32x4{0.f, 0.f, 0.f, 0.f};
      const float r = fold4(e[0], e[1], e[2], e[3]);
      if (c < 4) ldsF[w * 16 + 4 * c + g] = r;
    }
  }
  __syncthreads();
  {
    v4u af[4];
#pragma unroll
    for (int ks = 0; ks < 4; ++ks) af[ks] = *(const v4u*)(ldsA + c * 68 + 16 * ks + 4 * g);
    SB();
    f32x4 d[4];
#pragma unroll
    for (int t = 0; t < 4; ++t) d[t] = f32x4{0.f, 0.f, 0.f, 0.f};
#pragma unroll
    for (int ks = 0; ks < 4; ++ks)
#pragma unroll
      for (int t = 0; t < 4; ++t) d[t] = __builtin_amdgcn_mfma_f32_16x16x32_bf16(__builtin_bit_cast(bf16x8, af[ks]), __builtin_bit_cast(bf16x8, wb[t][ks]), d[t], 0, 0, 0);
    float y[4];
#pragma unroll
    for (int t = 0; t < 4; ++t) y[t] = (d[t][0] + d[t][1]) + (d[t][2] + d[t][3]);
    float u0 = fold32(y[0], y[2]), u1 = fold32(y[1], y[3]);
    u0 = fold16(u0, u0); u1 = fold16(u1, u1);
    const auto r0 = __builtin_amdgcn_permlane32_swap(__float_as_uint(u0), __float_as_uint(u0), false, false), r1 = __builtin_amdgcn_permlane32_swap(__float_as_uint(u1), __float_as_uint(u1), false, false);
    st_dense(rs, ysto, f32x4{__uint_as_float(r0[0]), __uint_as_float(r1[0]), __uint_as_float(r0[1]), __uint_as_float(r1[1])}, tagA, tagB);
  }

  if (is_fin) {
    __builtin_amdgcn_s_barrier();
    const float dv = ldsF[c] + ldsF[16 + c] + ldsF[32 + c];
    float gte = dv * __builtin_amdgcn_rcpf(1.f + __expf(-dv));
    asm volatile("" : "+v"(gte));
    __builtin_amdgcn_s_sleep(11);
    v4u y0;
    bool ok = false;
#pragma unroll 1
    for (unsigned spin = 0; spin < SPIN_CAP; ++spin) {
      asm volatile("" ::: "memory");
      if (!ok) { y0 = ld_unit(rs, yoff); ok = ok_dense(y0, tagA, tagB); }
      if (__all(ok)) break;
      __builtin_amdgcn_s_sleep(1);
    }
    const f32x4 yv = __builtin_bit_cast(f32x4, y0 & 0xfffffc00u);
    const float r = fold4(yv[0], yv[1], yv[2], yv[3]);
    if (c < 4) ldsF[128 + w * 16 + 4 * c + g] = r;
    __syncthreads();
    if (tid < 16) {
      float sv = 0.f;
#pragma unroll
      for (int ww = 0; ww < NWAVE; ++ww) sv += ldsF[128 + ww * 16 + tid];
      const float o = __uint_as_float((unsigned)xo << 16) + sv * gte;
      const __bf16 ob = (__bf16)o;
      __builtin_amdgcn_raw_buffer_store_b16(__builtin_bit_cast(unsigned short, ob), RSRC(KAP(8), 2 * D * 2), ocol * 2, 0, 0);
      if (cid == 0 && tid == 0)
        __hip_atomic_store((GAS unsigned long long*)(pws + WS_EPOCH), ((unsigned long long)tagB << 32) | tagA, __ATOMIC_RELAXED, __HIP_MEMORY_SCOPE_AGENT);
    }
  }
  asm volatile(".Lk_all_end:");
}

extern "C" void kernel_launch(void* const* d_in, const int* in_sizes, int n_in, void* d_out, int out_size, void* d_ws, size_t ws_size,
                              hipStream_t stream) {
  Args a;
  a.x = (const unsigned short*)d_in[0];
  a.k = (const unsigned short*)d_in[1];
  a.v = (const unsigned short*)d_in[2];
  a.wq = (const unsigned short*)d_in[3];
  a.wo = (const unsigned short*)d_in[4];
  a.wg = (const unsigned short*)d_in[6];
  a.wu = (const unsigned short*)d_in[7];
  a.wd = (const unsigned short*)d_in[8];
  a.out = (unsigned short*)d_out;
  a.ws = (unsigned char*)d_ws;
  hipLaunchKernelGGL(k_all, dim3(256), dim3(NTHR), 0, stream, a);
}
```
